```python
import torch

def _softmax(x, axis=-1):
    return torch.softmax(x, dim=axis)
def _gelu(x):
    return torch.nn.functional.gelu(x, approximate="tanh")
def _silu(x):
    return torch.nn.functional.silu(x)
def _sigmoid(x):
    return torch.sigmoid(x)
def _layernorm(x, axis=-1, eps=1e-5):
    m = x.mean(dim=axis, keepdim=True)
    v = x.var(dim=axis, keepdim=True, unbiased=False)
    return (x - m) / torch.sqrt(v + eps)
def _scatter_add(tgt, idx, src, axis=0):
    return tgt.clone().index_add_(axis, idx, src)
def _segment_sum(src, seg, n):
    out = torch.zeros((n,) + src.shape[1:], dtype=src.dtype)
    return out.index_add_(0, seg, src)
def _pad(x, pw):
    flat = tuple(v for pair in reversed(pw) for v in pair)
    return torch.nn.functional.pad(x, flat)

kernel_name = 'parallel_sum__grouped_kv_mqa__mixer_channel_mlp__prompt'

def setup_inputs():

    _w = (lambda a: (lambda t: t.to(torch.bfloat16) if t.is_floating_point() else t)(torch.as_tensor(a)))
    def _A_setup():
        B, S, D, H = 1, 2048, 1024, 32
        Dh = D // H
        return {
            "x": _w((lambda *s: torch.randn(*s, dtype=torch.bfloat16) * 0.05)(B, S, D)),
            "wq": _w((lambda *s: torch.randn(*s, dtype=torch.bfloat16) * 0.05)(D, H * Dh)),
            "wk": _w((lambda *s: torch.randn(*s, dtype=torch.bfloat16) * 0.05)(D, Dh)),
            "wv": _w((lambda *s: torch.randn(*s, dtype=torch.bfloat16) * 0.05)(D, Dh)),
            "wo": _w((lambda *s: torch.randn(*s, dtype=torch.bfloat16) * 0.05)(H * Dh, D)),
        }
    _A_in = _A_setup()

    def _B_setup():
        B, S, D, F = 1, 2048, 1024, 3584
        return {
            "x": _w((lambda *s: torch.randn(*s, dtype=torch.bfloat16) * 0.05)(B, S, D)),
            "w1": _w((lambda *s: torch.randn(*s, dtype=torch.bfloat16) * 3.2)(D, F) * (D ** -0.5)),
            "b1": _w((lambda *s: torch.randn(*s, dtype=torch.bfloat16) * 1.6)(F)),
            "w2": _w((lambda *s: torch.randn(*s, dtype=torch.bfloat16) * 0.4)(F, D) * (F ** -0.5)),
            "b2": _w((lambda *s: torch.randn(*s, dtype=torch.bfloat16) * 0.8)(D)),
        }
    _B_in = _B_setup()

    return {
        "A_x": _A_in["x"],
        "A_wq": _A_in["wq"],
        "A_wk": _A_in["wk"],
        "A_wv": _A_in["wv"],
        "A_wo": _A_in["wo"],
        "B_x": _B_in["x"],
        "B_w1": _B_in["w1"],
        "B_b1": _B_in["b1"],
        "B_w2": _B_in["w2"],
        "B_b2": _B_in["b2"],
    }

def reference(A_x, A_wq, A_wk, A_wv, A_wo, B_x, B_w1, B_b1, B_w2, B_b2):
    def _A_ref(x, wq, wk, wv, wo):
        B, S, D = x.shape
        H = 32
        Dh = D // H
        q = (lambda x, *d: x.permute(*d))((x @ wq).reshape(B, S, H, Dh), 0, 2, 1, 3)
        k = x @ wk
        v = x @ wv
        scores = torch.einsum("bhqd,bkd->bhqk", q, k) * (Dh ** -0.5)
        p = _softmax(scores, axis=-1)
        o = torch.einsum("bhqk,bkd->bhqd", p, v)
        return (lambda x, *d: x.permute(*d))(o, 0, 2, 1, 3).reshape(B, S, H * Dh) @ wo

    def _B_ref(x, w1, b1, w2, b2):
        y = _layernorm(x, axis=-1)
        h = _gelu(torch.einsum("bsd,df->bsf", y, w1) + b1)
        return torch.einsum("bsf,fd->bsd", h, w2) + b2

    x = A_x
    xn = _layernorm(x, axis=-1)
    return x + _A_ref(xn, A_wq, A_wk, A_wv, A_wo) + _B_ref(xn, B_w1, B_b1, B_w2, B_b2)
```

```cpp
#include <hip/hip_runtime.h>
#include <hip/hip_bf16.h>
typedef unsigned short u16;
__device__ __forceinline__ float b2f(u16 v){ return __uint_as_float(((unsigned)v)<<16); }
__device__ __forceinline__ u16 f2b(float f){ unsigned u=__float_as_uint(f); u += 0x7fffu + ((u>>16)&1u); return (u16)(u>>16); }
constexpr int S=2048, D=1024, F=3584, H=32, DH=32;

__global__ void ln_kernel(const u16* x, u16* xn, u16* y){
  int row=blockIdx.x; int t=threadIdx.x;
  float v[16]; float s=0;
  for(int i=0;i<16;i++){ v[i]=b2f(x[row*D+t+64*i]); s+=v[i]; }
  for(int o=32;o>0;o>>=1) s+=__shfl_xor(s,o);
  float m=s/D; float q=0;
  for(int i=0;i<16;i++){ v[i]-=m; q+=v[i]*v[i]; }
  for(int o=32;o>0;o>>=1) q+=__shfl_xor(q,o);
  float r=1.0f/sqrtf(q/D+1e-5f);
  s=0;
  for(int i=0;i<16;i++){ u16 b=f2b(v[i]*r); xn[row*D+t+64*i]=b; v[i]=b2f(b); s+=v[i]; }
  for(int o=32;o>0;o>>=1) s+=__shfl_xor(s,o);
  m=s/D; q=0;
  for(int i=0;i<16;i++){ v[i]-=m; q+=v[i]*v[i]; }
  for(int o=32;o>0;o>>=1) q+=__shfl_xor(q,o);
  r=1.0f/sqrtf(q/D+1e-5f);
  for(int i=0;i<16;i++){ y[row*D+t+64*i]=f2b(v[i]*r); }
}
__global__ void gemm_naive(const u16* A,const u16* B,u16* C,const u16* bias,int M,int N,int K,int mode){
  __shared__ float As[16][17], Bs[16][17];
  int tx=threadIdx.x&15, ty=threadIdx.x>>4;
  int row=blockIdx.y*16+ty, col=blockIdx.x*16+tx;
  float acc=0;
  for(int k0=0;k0<K;k0+=16){
    As[ty][tx]=b2f(A[(size_t)row*K+k0+tx]);
    Bs[ty][tx]=b2f(B[(size_t)(k0+ty)*N+col]);
    __syncthreads();
    for(int k=0;k<16;k++) acc+=As[ty][k]*Bs[k][tx];
    __syncthreads();
  }
  if(mode==1){ float x=acc+b2f(bias[col]); float u=0.7978845608f*(x+0.044715f*x*x*x); acc=0.5f*x*(1.f+tanhf(u)); }
  C[(size_t)row*N+col]=f2b(acc);
}
__global__ void attn_naive(const u16* q,const u16* k,const u16* v,u16* o){
  int idx=blockIdx.x*blockDim.x+threadIdx.x;
  float qr[DH], acc[DH];
  for(int d=0;d<DH;d++){ qr[d]=b2f(q[(size_t)idx*DH+d])*0.17677669529f; acc[d]=0; }
  float m=-1e30f,l=0;
  for(int j=0;j<S;j++){
    float s=0;
    for(int d=0;d<DH;d++) s+=qr[d]*b2f(k[j*DH+d]);
    float mn=fmaxf(m,s); float a=__expf(m-mn); float p=__expf(s-mn);
    l=l*a+p;
    for(int d=0;d<DH;d++) acc[d]=acc[d]*a+p*b2f(v[j*DH+d]);
    m=mn;
  }
  for(int d=0;d<DH;d++) o[(size_t)idx*DH+d]=f2b(acc[d]/l);
}
__global__ void final_naive(const u16* x,const u16* o,const u16* wo,const u16* h,const u16* w2,const u16* b2,u16* out){
  __shared__ float As[16][17], Bs[16][17];
  int tx=threadIdx.x&15, ty=threadIdx.x>>4;
  int row=blockIdx.y*16+ty, col=blockIdx.x*16+tx;
  float acc=0;
  for(int k0=0;k0<D;k0+=16){
    As[ty][tx]=b2f(o[(size_t)row*D+k0+tx]);
    Bs[ty][tx]=b2f(wo[(size_t)(k0+ty)*D+col]);
    __syncthreads();
    for(int k=0;k<16;k++) acc+=As[ty][k]*Bs[k][tx];
    __syncthreads();
  }
  for(int k0=0;k0<F;k0+=16){
    As[ty][tx]=b2f(h[(size_t)row*F+k0+tx]);
    Bs[ty][tx]=b2f(w2[(size_t)(k0+ty)*D+col]);
    __syncthreads();
    for(int k=0;k<16;k++) acc+=As[ty][k]*Bs[k][tx];
    __syncthreads();
  }
  out[(size_t)row*D+col]=f2b(acc+b2f(b2[col])+b2f(x[(size_t)row*D+col]));
}
extern "C" void kernel_launch(void* const* d_in, const int* in_sizes, int n_in,
                              void* d_out, int out_size, void* d_ws, size_t ws_size,
                              hipStream_t stream) {
  const u16* x=(const u16*)d_in[0]; const u16* wq=(const u16*)d_in[1]; const u16* wk=(const u16*)d_in[2];
  const u16* wv=(const u16*)d_in[3]; const u16* wo=(const u16*)d_in[4];
  const u16* w1=(const u16*)d_in[6]; const u16* b1=(const u16*)d_in[7]; const u16* w2=(const u16*)d_in[8]; const u16* b2=(const u16*)d_in[9];
  u16* ws=(u16*)d_ws;
  u16* xn=ws; u16* y=xn+S*D; u16* q=y+S*D; u16* k=q+S*D; u16* v=k+S*DH; u16* o=v+S*DH; u16* h=o+S*D;
  ln_kernel<<<S,64,0,stream>>>(x,xn,y);
  gemm_naive<<<dim3(D/16,S/16),256,0,stream>>>(xn,wq,q,nullptr,S,D,D,0);
  gemm_naive<<<dim3(DH/16,S/16),256,0,stream>>>(xn,wk,k,nullptr,S,DH,D,0);
  gemm_naive<<<dim3(DH/16,S/16),256,0,stream>>>(xn,wv,v,nullptr,S,DH,D,0);
  gemm_naive<<<dim3(F/16,S/16),256,0,stream>>>(y,w1,h,b1,S,F,D,1);
  attn_naive<<<65536/256,256,0,stream>>>(q,k,v,o);
  final_naive<<<dim3(D/16,S/16),256,0,stream>>>(x,o,wo,h,w2,b2,(u16*)d_out);
}
```

```cpp
#include <hip/hip_runtime.h>
#include <stdint.h>

typedef unsigned short u16;
typedef __bf16 bf16x8 __attribute__((ext_vector_type(8)));
typedef __bf16 bf16x2 __attribute__((ext_vector_type(2)));
typedef short s16x4 __attribute__((ext_vector_type(4)));
typedef short s16x8 __attribute__((ext_vector_type(8)));
typedef float f32x16 __attribute__((ext_vector_type(16)));
typedef float f32x4 __attribute__((ext_vector_type(4)));
typedef float f32x2 __attribute__((ext_vector_type(2)));
typedef unsigned u32x4 __attribute__((ext_vector_type(4)));
typedef unsigned u32x2 __attribute__((ext_vector_type(2)));
#define LAS __attribute__((address_space(3)))
#define GAS __attribute__((address_space(1)))
#define DI __device__ __forceinline__
#define SB() __builtin_amdgcn_sched_barrier(0)
#define PIN(x) asm volatile("" : "+v"(x))
#define MFMA32(a, b, c) __builtin_amdgcn_mfma_f32_32x32x16_bf16((a), (b), (c), 0, 0, 0)
#define MFMA16(a, b, c) __builtin_amdgcn_mfma_f32_16x16x32_bf16((a), (b), (c), 0, 0, 0)

constexpr int S_ = 2048, D_ = 1024, F_ = 3584;
constexpr size_t WS_FLAGS = 0;
constexpr size_t WS_R2   = 65536;
constexpr size_t WS_KV   = WS_R2 + 8192;
constexpr size_t WS_XN   = WS_KV + 262144;
constexpr size_t WS_O    = WS_XN + 4194304;
constexpr size_t WS_H    = WS_O + 4194304;
constexpr size_t WS_PART = WS_H + (size_t)112 * 128 * 1024;
constexpr size_t WS_STAMP = WS_PART + (size_t)256 * 32768;

constexpr float QSCALE = 0.17677669529663687f * 1.4426950408889634f;

DI float bf2f(u16 v) { return __uint_as_float(((unsigned)v) << 16); }
DI unsigned cvtpk(float lo, float hi) { f32x2 v = {lo, hi}; bf16x2 b = __builtin_convertvector(v, bf16x2); return __builtin_bit_cast(unsigned, b); }
DI float lo2f(unsigned u) { return __uint_as_float(u << 16); }
DI float hi2f(unsigned u) { return __uint_as_float(u & 0xffff0000u); }
DI __amdgpu_buffer_rsrc_t mkrsrc(const void* p, unsigned bytes) { return __builtin_amdgcn_make_buffer_rsrc((void*)p, 0, bytes, 0x00020000); }
DI void dma16(unsigned lds_addr, unsigned voff, __amdgpu_buffer_rsrc_t rsrc, unsigned soff) {
  asm volatile("s_mov_b32 m0, %0\n\ts_nop 0\n\tbuffer_load_dwordx4 %1, %2, %3 offen lds" :: "s"(lds_addr), "v"(voff), "s"(rsrc), "s"(soff));
}
#define WAIT_BAR(N) asm volatile("s_waitcnt vmcnt(" #N ") lgkmcnt(0)\n\ts_barrier" ::: "memory")
DI bf16x8 ldsfrag(const LAS char* p) { return *(const LAS bf16x8*)p; }
typedef short v4i16_t __attribute__((ext_vector_type(4)));
DI bf16x8 ldstr(const LAS char* p0, const LAS char* p1) {
  v4i16_t a = __builtin_amdgcn_ds_read_tr16_b64_v4i16((LAS v4i16_t*)p0);
  v4i16_t b = __builtin_amdgcn_ds_read_tr16_b64_v4i16((LAS v4i16_t*)p1);
  s16x8 v = __builtin_shufflevector(a, b, 0, 1, 2, 3, 4, 5, 6, 7);
  return __builtin_bit_cast(bf16x8, v);
}
DI void st_wt16(__amdgpu_buffer_rsrc_t r, unsigned voff, u32x4 v) { __builtin_amdgcn_raw_buffer_store_b128(v, r, voff, 0, 16); }
#define STAMP(i)

__global__ __launch_bounds__(512) void k0_ln_kv(const u16* __restrict__ x, const u16* __restrict__ wk, const u16* __restrict__ wv, char* __restrict__ ws) {
  __shared__ __attribute__((aligned(16))) char lds[65536 + 16384 + 2048 + 1024];
  float* part = (float*)(lds + 65536);
  float* fin = (float*)(lds + 65536 + 16384);
  float* red = (float*)(lds + 65536 + 16384 + 2048);
  const int tid = threadIdx.x, l = tid & 63, w = __builtin_amdgcn_readfirstlane(tid >> 6);
  const int rb = blockIdx.x & 127, hf = blockIdx.x >> 7;
  const int r = l & 15, c = l >> 4;
  if (blockIdx.x == 0) { for (int i = tid; i < 16384 / 16; i += 512) ((u32x4*)(ws + WS_FLAGS))[i] = (u32x4){0, 0, 0, 0}; }
  const unsigned lds0 = (unsigned)(uintptr_t)lds;
  {
    __amdgpu_buffer_rsrc_t rw = mkrsrc(hf ? wv : wk, 65536);
#pragma unroll
    for (int i = 0; i < 8; ++i) dma16(lds0 + w * 8192 + i * 1024, l * 16, rw, w * 8192 + i * 1024);
  }
  u32x4 xv[4];
  const u16* xr = x + (size_t)(rb * 16 + r) * D_ + w * 128 + c * 8;
#pragma unroll
  for (int i = 0; i < 4; ++i) xv[i] = *(const u32x4*)(xr + i * 32);
  float v[32];
#pragma unroll
  for (int i = 0; i < 4; ++i)
#pragma unroll
    for (int j = 0; j < 4; ++j) { v[i * 8 + 2 * j] = lo2f(xv[i][j]); v[i * 8 + 2 * j + 1] = hi2f(xv[i][j]); }
  float s = 0.f;
#pragma unroll
  for (int i = 0; i < 32; ++i) s += v[i];
  s += __shfl_xor(s, 16); s += __shfl_xor(s, 32);
  if (c == 0) red[w * 16 + r] = s;
  __syncthreads();
  float mean = 0.f;
#pragma unroll
  for (int i = 0; i < 8; ++i) mean += red[i * 16 + r];
  mean *= (1.0f / D_);
  float q = 0.f;
#pragma unroll
  for (int i = 0; i < 32; ++i) { v[i] -= mean; q += v[i] * v[i]; }
  q += __shfl_xor(q, 16); q += __shfl_xor(q, 32);
  if (c == 0) red[128 + w * 16 + r] = q;
  __syncthreads();
  float var = 0.f;
#pragma unroll
  for (int i = 0; i < 8; ++i) var += red[128 + i * 16 + r];
  var *= (1.0f / D_);
  const float rstd = 1.0f / sqrtf(var + 1e-5f);
  if (hf == 0 && w == 0 && c == 0) ((float*)(ws + WS_R2))[rb * 16 + r] = 1.0f / sqrtf(var * rstd * rstd + 1e-5f);
  u32x4 xn[4];
#pragma unroll
  for (int i = 0; i < 4; ++i)
#pragma unroll
    for (int j = 0; j < 4; ++j) xn[i][j] = cvtpk(v[i * 8 + 2 * j] * rstd, v[i * 8 + 2 * j + 1] * rstd);
#pragma unroll
  for (int i = 0; i < 2; ++i) { const int ii = 2 * i + hf; *(u32x4*)(ws + WS_XN + ((size_t)(4 * w + ii) * 128 + rb) * 1024 + l * 16) = hf ? xn[2 * i + 1] : xn[2 * i]; (void)ii; }
  asm volatile("s_waitcnt vmcnt(0)" ::: "memory");
  f32x4 acc[2] = {{0.f, 0.f, 0.f, 0.f}, {0.f, 0.f, 0.f, 0.f}};
  const LAS char* wb = (const LAS char*)lds + w * 8192 + (8 * c + ((l & 15) >> 2)) * 64 + (l & 3) * 8;
#pragma unroll
  for (int i = 0; i < 4; ++i)
#pragma unroll
    for (int b = 0; b < 2; ++b) {
      bf16x8 wf = ldstr(wb + i * 2048 + b * 32, wb + i * 2048 + b * 32 + 256);
      acc[b] = MFMA16(__builtin_bit_cast(bf16x8, xn[i]), wf, acc[b]);
    }
#pragma unroll
  for (int b = 0; b < 2; ++b)
#pragma unroll
    for (int j = 0; j < 4; ++j) part[(w * 16 + 4 * c + j) * 32 + 16 * b + r] = acc[b][j];
  __syncthreads();
  { float t = 0.f;
#pragma unroll
    for (int i = 0; i < 8; ++i) t += part[i * 512 + tid];
    fin[tid] = t; }
  __syncthreads();
  if (tid < 64) {
    float o[8];
    char* dst = ws + WS_KV + (size_t)(rb >> 1) * 4096;
    if (hf == 0) { const int m = tid & 15, h = (tid >> 4) & 1, st = tid >> 5;
#pragma unroll
      for (int j = 0; j < 8; ++j) o[j] = fin[m * 32 + 16 * st + 8 * (j >> 2) + 4 * h + (j & 3)];
      dst += st * 1024 + h * 512 + (16 * (rb & 1) + m) * 16;
    } else { const int d = tid & 31, h = tid >> 5;
#pragma unroll
      for (int j = 0; j < 8; ++j) o[j] = fin[(8 * (j >> 2) + 4 * h + (j & 3)) * 32 + d];
      dst += 2048 + (rb & 1) * 1024 + h * 512 + d * 16;
    }
    *(u32x4*)dst = (u32x4){cvtpk(o[0], o[1]), cvtpk(o[2], o[3]), cvtpk(o[4], o[5]), cvtpk(o[6], o[7])};
  }
}

#define LSUM_MFMA 0
DI float fadd_s(float a, float b) { float r; asm("v_add_f32_e32 %0, %1, %2" : "=v"(r) : "v"(a), "v"(b)); return r; }

DI f32x16 attn_slow(const char* __restrict__ kv, bf16x8 q0, bf16x8 q1) {
  const int l = threadIdx.x & 63;
  f32x16 O = f32x16{};
  float m = -3.0e38f, ls = 0.f;
#pragma unroll 1
  for (int t = 0; t < 64; ++t) {
    const char* p = kv + t * 4096 + l * 16;
    const bf16x8 k0 = *(const bf16x8*)p, k1 = *(const bf16x8*)(p + 1024), v0 = *(const bf16x8*)(p + 2048), v1 = *(const bf16x8*)(p + 3072);
    f32x16 s = MFMA32(k0, q0, f32x16{}); s = MFMA32(k1, q1, s);
    float mx = s[0];
#pragma unroll
    for (int i = 1; i < 16; ++i) mx = fmaxf(mx, s[i]);
    mx = fmaxf(mx, __shfl_xor(mx, 32));
    const float mn = fmaxf(m, mx), al = __builtin_amdgcn_exp2f(m - mn);
    m = mn; float rs = 0.f; u32x4 p0, p1;
#pragma unroll
    for (int i = 0; i < 16; ++i) { s[i] = __builtin_amdgcn_exp2f(s[i] - mn); rs += s[i]; O[i] *= al; }
    rs += __shfl_xor(rs, 32); ls = ls * al + rs;
#pragma unroll
    for (int j = 0; j < 4; ++j) { p0[j] = cvtpk(s[2 * j], s[2 * j + 1]); p1[j] = cvtpk(s[8 + 2 * j], s[8 + 2 * j + 1]); }
    O = MFMA32(v0, __builtin_bit_cast(bf16x8, p0), O); O = MFMA32(v1, __builtin_bit_cast(bf16x8, p1), O);
  }
#pragma unroll
  for (int i = 0; i < 16; ++i) O[i] /= ls;
  return O;
}

DI void role_attn(int a, const u16* __restrict__ wq, char* __restrict__ ws, LAS char* lds) {
  const int tid = threadIdx.x, l = tid & 63, w = __builtin_amdgcn_readfirstlane(tid >> 6);
  const int xcd = a & 7, idx = a >> 3, tb = (xcd >> 1) * 8 + (idx & 7), hg = (xcd & 1) * 4 + (idx >> 3);
  const int h = l >> 5, r32 = l & 31; const int stamp_base = 0; (void)stamp_base;
  const unsigned lds0 = (unsigned)(uintptr_t)lds;
  constexpr int SLOT = 24576;
  const __amdgpu_buffer_rsrc_t rX = mkrsrc(ws + WS_XN, 4194304), rW = mkrsrc(wq, 2097152), rKV = mkrsrc(ws + WS_KV, 262144);
  const unsigned vlin = l * 16;
  const unsigned vw = ((l >> 2) & 3) * 2048 + (l >> 4) * 64 + (l & 3) * 16;
  STAMP(0);
  auto issueQ = [&](int s, int slot) {
    const unsigned sb = lds0 + slot * SLOT;
    const unsigned xo = ((2 * s + (w >> 1)) * 128 + 4 * tb + 2 * (w & 1)) * 1024;
    dma16(sb + w * 2048, vlin, rX, xo); dma16(sb + w * 2048 + 1024, vlin, rX, xo + 1024);
#pragma unroll
    for (int i = 0; i < 4; ++i) dma16(sb + 8192 + (4 * w + i) * 1024, vw, rW, (64 * s + 4 * (4 * w + i)) * 2048 + 256 * hg);
  };
  auto issueKV = [&](int j, int slot) {
    const unsigned sb = lds0 + slot * SLOT;
#pragma unroll
    for (int i = 0; i < 4; ++i) dma16(sb + (4 * w + i) * 1024, vlin, rKV, j * 16384 + (4 * w + i) * 1024);
  };
  issueQ(0, 0); issueQ(1, 1);
  f32x16 qacc[2]; qacc[0] = f32x16{}; qacc[1] = f32x16{};
  const LAS char* xrd = lds + (r32 >> 4) * 1024 + h * 256 + (l & 15) * 16;
  const LAS char* wrd = lds + 8192 + 2 * h * 1024 + w * 256 + ((l & 15) >> 2) * 64 + ((l >> 4) & 1) * 32 + (l & 3) * 8;
  int slot = 0;
  for (int s = 0; s < 16; ++s) {
    if (s < 15) { WAIT_BAR(6); } else { WAIT_BAR(4); }
    const int ns = slot == 0 ? 2 : slot - 1;
    if (s < 14) issueQ(s + 2, ns); else issueKV(s - 14, ns);
    const LAS char* xs = xrd + slot * SLOT; const LAS char* wsb = wrd + slot * SLOT;
#pragma unroll
    for (int ks = 0; ks < 4; ++ks) {
      bf16x8 wf = ldstr(wsb + (4 * ks) * 1024, wsb + (4 * ks + 1) * 1024);
      bf16x8 x0 = ldsfrag(xs + (ks >> 1) * 4096 + (ks & 1) * 512), x1 = ldsfrag(xs + (ks >> 1) * 4096 + 2048 + (ks & 1) * 512);
      qacc[0] = MFMA32(wf, x0, qacc[0]); qacc[1] = MFMA32(wf, x1, qacc[1]);
    }
    slot = slot == 2 ? 0 : slot + 1;
  }
  STAMP(1);
  bf16x8 qf[2][2];
#pragma unroll
  for (int t = 0; t < 2; ++t)
#pragma unroll
    for (int s = 0; s < 2; ++s) {
      u32x4 p;
#pragma unroll
      for (int j = 0; j < 4; ++j) p[j] = cvtpk(qacc[t][8 * s + 2 * j] * QSCALE, qacc[t][8 * s + 2 * j + 1] * QSCALE);
      qf[t][s] = __builtin_bit_cast(bf16x8, p);
    }
  WAIT_BAR(4);
  const LAS char* kvb = lds + l * 16;
  f32x16 negm[2], Sa, Sb, Oa = f32x16{}, Ob = f32x16{};
  bf16x8 k0, k1, v0, v1;
  {
    const LAS char* p = kvb + 1 * SLOT;
    k0 = ldsfrag(p); k1 = ldsfrag(p + 1024);
    f32x16 z = f32x16{};
#pragma unroll
    for (int t = 0; t < 2; ++t) {
      f32x16 s0 = MFMA32(k0, qf[t][0], z); s0 = MFMA32(k1, qf[t][1], s0);
      float m = s0[0];
#pragma unroll
      for (int i = 1; i < 16; ++i) m = fmaxf(m, s0[i]);
      auto sw = __builtin_amdgcn_permlane32_swap(__float_as_uint(m), __float_as_uint(m), false, false);
      m = fmaxf(__uint_as_float(sw[0]), __uint_as_float(sw[1]));
#pragma unroll
      for (int i = 0; i < 16; ++i) negm[t][i] = -m;
    }
    PIN(negm[0]); PIN(negm[1]);
    Sa = MFMA32(k0, qf[0][0], negm[0]); Sa = MFMA32(k1, qf[0][1], Sa);
  }
  float la[4] = {0.f, 0.f, 0.f, 0.f}, lb[4] = {0.f, 0.f, 0.f, 0.f};
  u32x4 pa0, pa1, pb0 = {0, 0, 0, 0}, pb1 = {0, 0, 0, 0};
  v0 = __builtin_bit_cast(bf16x8, pb0); v1 = v0;
#define EXPG(S, i, P, pi, L) do { S[i] = __builtin_amdgcn_exp2f(S[i]); S[i + 1] = __builtin_amdgcn_exp2f(S[i + 1]); S[i + 2] = __builtin_amdgcn_exp2f(S[i + 2]); S[i + 3] = __builtin_amdgcn_exp2f(S[i + 3]); \
    P[pi] = cvtpk(S[i], S[i + 1]); P[pi + 1] = cvtpk(S[i + 2], S[i + 3]); \
    if (!LSUM_MFMA) { L[0] = fadd_s(L[0], S[i]); L[1] = fadd_s(L[1], S[i + 1]); L[2] = fadd_s(L[2], S[i + 2]); L[3] = fadd_s(L[3], S[i + 3]); PIN(L[0]); PIN(L[1]); PIN(L[2]); PIN(L[3]); } \
    PIN(P); } while (0)
#define BF(x) __builtin_bit_cast(bf16x8, x)
#define LS(acc, p)
#define TILE(KC0, KC1, KN0, KN1, kn, vp) do { \
    KN0 = ldsfrag(kn); KN1 = ldsfrag((kn) + 1024); \
    Sb = MFMA32(KC0, qf[1][0], negm[1]); EXPG(Sa, 0, pa0, 0, la); SB(); \
    Sb = MFMA32(KC1, qf[1][1], Sb);      EXPG(Sa, 4, pa0, 2, la); SB(); \
    Ob = MFMA32(v0, BF(pb0), Ob); LS(lmb, pb0); EXPG(Sa, 8, pa1, 0, la); SB(); \
    Ob = MFMA32(v1, BF(pb1), Ob); LS(lmb, pb1); EXPG(Sa, 12, pa1, 2, la); SB(); \
    v0 = ldsfrag(vp); v1 = ldsfrag((vp) + 1024); \
    Sa = MFMA32(KN0, qf[0][0], negm[0]); EXPG(Sb, 0, pb0, 0, lb); SB(); \
    Sa = MFMA32(KN1, qf[0][1], Sa);      EXPG(Sb, 4, pb0, 2, lb); SB(); \
    Oa = MFMA32(v0, BF(pa0), Oa); LS(lma, pa0); EXPG(Sb, 8, pb1, 0, lb); SB(); \
    Oa = MFMA32(v1, BF(pa1), Oa); LS(lma, pa1); EXPG(Sb, 12, pb1, 2, lb); SB(); \
  } while (0)
  bf16x8 kx0, kx1;
  int cur = 1;
  for (int j = 0; j < 16; ++j) {
    const int nxt = cur == 2 ? 0 : cur + 1;
    const LAS char* pc = kvb + cur * SLOT; const LAS char* pn = kvb + nxt * SLOT;
    TILE(k0, k1, kx0, kx1, pc + 4096, pc + 2048);
    TILE(kx0, kx1, k0, k1, pc + 8192, pc + 4096 + 2048);
    TILE(k0, k1, kx0, kx1, pc + 12288, pc + 8192 + 2048);
    WAIT_BAR(0);
    if (j + 2 < 16) issueKV(j + 2, nxt == 2 ? 0 : nxt + 1);
    TILE(kx0, kx1, k0, k1, (j < 15 ? pn : pc), pc + 12288 + 2048);
    cur = nxt;
  }
  Ob = MFMA32(v0, BF(pb0), Ob); Ob = MFMA32(v1, BF(pb1), Ob);
  STAMP(2);
  float lsa, lsb;
  lsa = (la[0] + la[1]) + (la[2] + la[3]); lsb = (lb[0] + lb[1]) + (lb[2] + lb[3]);
  { auto sw = __builtin_amdgcn_permlane32_swap(__float_as_uint(lsa), __float_as_uint(lsa), false, false); lsa = __uint_as_float(sw[0]) + __uint_as_float(sw[1]);
    auto sx = __builtin_amdgcn_permlane32_swap(__float_as_uint(lsb), __float_as_uint(lsb), false, false); lsb = __uint_as_float(sx[0]) + __uint_as_float(sx[1]); }
  float ia = 1.0f / lsa, ib = 1.0f / lsb;
  {
    float mo = 0.f;
#pragma unroll
    for (int i = 0; i < 16; ++i) mo = fmaxf(mo, fmaxf(fabsf(Oa[i]), fabsf(Ob[i])));
    const bool ok = (lsa <= 1.0e18f) && (lsb <= 1.0e18f) && (mo <= 1.0e37f);
    if (__builtin_expect(__any(!ok), 0)) {
      Oa = attn_slow(ws + WS_KV, qf[0][0], qf[0][1]);
      Ob = attn_slow(ws + WS_KV, qf[1][0], qf[1][1]);
      ia = 1.0f; ib = 1.0f;
    }
  }
  const __amdgpu_buffer_rsrc_t rO = mkrsrc(ws + WS_O, 4194304);
#pragma unroll
  for (int t = 0; t < 2; ++t) {
    const f32x16 O = t ? Ob : Oa; const float inv = t ? ib : ia;
    unsigned d[8];
#pragma unroll
    for (int i = 0; i < 8; ++i) d[i] = cvtpk(O[2 * i] * inv, O[2 * i + 1] * inv);
#pragma unroll
    for (int g = 0; g < 2; ++g) {
      auto s0 = __builtin_amdgcn_permlane32_swap(d[4 * g], d[4 * g + 2], false, false);
      auto s1 = __builtin_amdgcn_permlane32_swap(d[4 * g + 1], d[4 * g + 3], false, false);
      const unsigned off = ((4 * hg + w) * 128 + 4 * tb + 2 * t + (r32 >> 4)) * 1024 + (2 * g + h) * 256 + (l & 15) * 16;
      st_wt16(rO, off, (u32x4){s0[0], s1[0], s0[1], s1[1]});
    }
  }
  STAMP(3);
}

DI float gelu_tanh(float x) {
  constexpr float c1 = -2.0f * 0.7978845608028654f * 1.4426950408889634f, c2 = c1 * 0.044715f;
  const float z = x * __builtin_fmaf(x * x, c2, c1);
  return x * __builtin_amdgcn_rcpf(1.0f + __builtin_amdgcn_exp2f(z));
}

DI void role_w1(int g, const u16* __restrict__ w1, const u16* __restrict__ b1, char* __restrict__ ws, LAS char* lds) {
  const int tid = threadIdx.x, l = tid & 63, w = __builtin_amdgcn_readfirstlane(tid >> 6);
  const int xcd = g & 7, idx = g >> 3, p = (xcd >> 1) * 4 + (idx & 3), j = (xcd & 1) * 8 + (idx >> 2);
  const int h = l >> 5, r32 = l & 31; const int stamp_base = 256; (void)stamp_base;
  const unsigned lds0 = (unsigned)(uintptr_t)lds;
  constexpr int SLOT = 22528;
  const __amdgpu_buffer_rsrc_t rX = mkrsrc(ws + WS_XN, 4194304), rW = mkrsrc(w1, D_ * F_ * 2);
  const unsigned vlin = l * 16;
  unsigned vw[4];
#pragma unroll
  for (int i = 0; i < 4; ++i) { const int b = 4 * (w + 4 * i) + (l >> 4), kg = b / 7, nb = b - 7 * kg; vw[i] = (4 * kg + ((l >> 2) & 3)) * (F_ * 2) + (224 * j + 32 * nb + 8 * (l & 3)) * 2; }
  STAMP(0);
  auto piece = [&](int s, int slot, int i) {
    const unsigned sb = lds0 + slot * SLOT;
    if (i < 2) dma16(sb + w * 2048 + i * 1024, vlin, rX, (s * 128 + 8 * p + 2 * w + i) * 1024);
    else if (i < 5) dma16(sb + 8192 + (w + 4 * (i - 2)) * 1024, vw[i - 2], rW, s * 32 * F_ * 2);
    else if (w < 2) dma16(sb + 8192 + (w + 12) * 1024, vw[3], rW, s * 32 * F_ * 2);
  };
#pragma unroll
  for (int s = 0; s < 2; ++s)
#pragma unroll
    for (int i = 0; i < 6; ++i) piece(s, s, i);
#pragma unroll
  for (int i = 0; i < 3; ++i) piece(2, 2, i);
  LAS float* bl = (LAS float*)(lds + 3 * SLOT);
  if (tid < 224) bl[tid] = bf2f(b1[224 * j + tid]);
  const float r2 = ((const float*)(ws + WS_R2))[128 * p + 32 * w + r32];
  f32x16 acc[7];
#pragma unroll
  for (int i = 0; i < 7; ++i) acc[i] = f32x16{};
  const LAS char* xrd = lds + (2 * w + (r32 >> 4)) * 1024 + h * 256 + (l & 15) * 16;
  const LAS char* wrd = lds + 8192 + 2 * h * 7 * 256 + ((l & 15) >> 2) * 64 + ((l >> 4) & 1) * 32 + (l & 3) * 8;
  bf16x8 xa, xb, wa[7], wb[7];
#define RDX(X, slot, ks) X = ldsfrag(xrd + (slot) * SLOT + (ks) * 512)
#define RDW(W, n, slot, ks) W[n] = ldstr(wrd + (slot) * SLOT + ((ks) * 4 * 7 + (n)) * 256, wrd + (slot) * SLOT + ((ks) * 4 * 7 + 7 + (n)) * 256)
#define SUB1(X, W, XN, WN, DOREAD, rs, rk, DODMA, ds, dl, i0) do { \
    acc[0] = MFMA32(W[0], X, acc[0]); if (DOREAD) { RDX(XN, rs, rk); RDW(WN, 0, rs, rk); } SB(); \
    acc[1] = MFMA32(W[1], X, acc[1]); if (DOREAD) { RDW(WN, 1, rs, rk); } if (DODMA) piece(ds, dl, i0); SB(); \
    acc[2] = MFMA32(W[2], X, acc[2]); if (DOREAD) { RDW(WN, 2, rs, rk); } SB(); \
    acc[3] = MFMA32(W[3], X, acc[3]); if (DOREAD) { RDW(WN, 3, rs, rk); } if (DODMA) piece(ds, dl, i0 + 1); SB(); \
    acc[4] = MFMA32(W[4], X, acc[4]); if (DOREAD) { RDW(WN, 4, rs, rk); } SB(); \
    acc[5] = MFMA32(W[5], X, acc[5]); if (DOREAD) { RDW(WN, 5, rs, rk); } if (DODMA) piece(ds, dl, i0 + 2); SB(); \
    acc[6] = MFMA32(W[6], X, acc[6]); if (DOREAD) { RDW(WN, 6, rs, rk); } SB(); } while (0)
  if (w < 2) { WAIT_BAR(9); } else { WAIT_BAR(8); }
  RDX(xa, 0, 0);
#pragma unroll
  for (int n = 0; n < 7; ++n) RDW(wa, n, 0, 0);
  int slot = 0;
#define STEP1(WA, WB_, DMA_A, DMA_B, RD_B) do { \
    const int ns = slot == 2 ? 0 : slot + 1, ps = slot == 0 ? 2 : slot - 1; \
    SUB1(xa, wa, xb, wb, true, slot, 1, DMA_A, s + 2, ps, 3); \
    if (w < 2) { WAIT_BAR(WA); } else { WAIT_BAR(WB_); } \
    SUB1(xb, wb, xa, wa, RD_B, ns, 0, DMA_B, s + 3, slot, 0); \
    slot = ns; } while (0)
  int s = 0;
  for (; s < 29; ++s) STEP1(6, 5, true, true, true);
  STEP1(6, 5, true, false, true); ++s;
  STEP1(0, 0, false, false, true); ++s;
  STEP1(0, 0, false, false, false);
  STAMP(1);
  const __amdgpu_buffer_rsrc_t rH = mkrsrc(ws + WS_H, 112 * 128 * 1024);
#pragma unroll
  for (int nb = 0; nb < 7; ++nb) {
    unsigned d[8];
#pragma unroll
    for (int g4 = 0; g4 < 4; ++g4) {
      const f32x4 bb = *(const LAS f32x4*)(lds + 3 * SLOT + (32 * nb + 8 * g4 + 4 * h) * 4);
      const float y0 = gelu_tanh(__builtin_fmaf(acc[nb][4 * g4 + 0], r2, bb[0])), y1 = gelu_tanh(__builtin_fmaf(acc[nb][4 * g4 + 1], r2, bb[1]));
      const float y2 = gelu_tanh(__builtin_fmaf(acc[nb][4 * g4 + 2], r2, bb[2])), y3 = gelu_tanh(__builtin_fmaf(acc[nb][4 * g4 + 3], r2, bb[3]));
      d[2 * g4] = cvtpk(y0, y1); d[2 * g4 + 1] = cvtpk(y2, y3);
    }
#pragma unroll
    for (int g2 = 0; g2 < 2; ++g2) {
      auto s0 = __builtin_amdgcn_permlane32_swap(d[4 * g2], d[4 * g2 + 2], false, false);
      auto s1 = __builtin_amdgcn_permlane32_swap(d[4 * g2 + 1], d[4 * g2 + 3], false, false);
      const unsigned off = ((7 * j + nb) * 128 + 8 * p + 2 * w + (r32 >> 4)) * 1024 + (2 * g2 + h) * 256 + (l & 15) * 16;
      st_wt16(rH, off, (u32x4){s0[0], s1[0], s0[1], s1[1]});
    }
  }
  STAMP(2);
#undef RDX
#undef RDW
}

DI void role_w2(int g, const u16* __restrict__ w2, const u16* __restrict__ wo, const u16* __restrict__ b2, const u16* __restrict__ x, u16* __restrict__ out, char* __restrict__ ws, LAS char* lds) {
  const int tid = threadIdx.x, l = tid & 63, w = __builtin_amdgcn_readfirstlane(tid >> 6);
  const int xcd = g & 7, idx = g >> 3, kh = xcd & 1, p = (xcd >> 1) * 4 + (idx & 3), nt = idx >> 2;
  const int partner = g ^ 1;
  const int h = l >> 5, r32 = l & 31, wm = w >> 1, wn = w & 1; const int stamp_base = 512; (void)stamp_base;
  const unsigned lds0 = (unsigned)(uintptr_t)lds;
  constexpr int SLOT = 16384, NS = 5;
  const __amdgpu_buffer_rsrc_t rH = mkrsrc(ws + WS_H, 112 * 128 * 1024), rO = mkrsrc(ws + WS_O, 4194304), rW2 = mkrsrc(w2, F_ * D_ * 2), rWo = mkrsrc(wo, D_ * D_ * 2);
  const unsigned vlin = l * 16;
  const unsigned vw = ((l >> 2) & 3) * 2048 + (l >> 4) * 64 + (l & 3) * 16 + nt * 256;
  STAMP(0);
  auto piece = [&](int u, int slot, int i, bool isH) {
    const unsigned sb = lds0 + slot * SLOT;
    const int kt = isH ? 56 * kh + u : 16 * kh + (u - 56);
    if (i < 2) { const unsigned o_ = (kt * 128 + 8 * p + 2 * w + i) * 1024; if (isH) dma16(sb + w * 2048 + i * 1024, vlin, rH, o_); else dma16(sb + w * 2048 + i * 1024, vlin, rO, o_); }
    else { const unsigned o_ = (32 * kt + 8 * w + 4 * (i - 2)) * 2048; if (isH) dma16(sb + 8192 + w * 2048 + (i - 2) * 1024, vw, rW2, o_); else dma16(sb + 8192 + w * 2048 + (i - 2) * 1024, vw, rWo, o_); }
  };
#pragma unroll
  for (int u = 0; u < 4; ++u)
#pragma unroll
    for (int i = 0; i < 4; ++i) piece(u, u, i, true);
  piece(4, 4, 0, true); piece(4, 4, 2, true);
  f32x16 acc[2][2];
#pragma unroll
  for (int i = 0; i < 2; ++i) { acc[i][0] = f32x16{}; acc[i][1] = f32x16{}; }
  const LAS char* xrd = lds + (4 * wm + (r32 >> 4)) * 1024 + h * 256 + (l & 15) * 16;
  const LAS char* wrd = lds + 8192 + 2 * h * 1024 + 2 * wn * 256 + ((l & 15) >> 2) * 64 + ((l >> 4) & 1) * 32 + (l & 3) * 8;
  bf16x8 xa[2], xb[2], wa[2], wb[2];
#define RDX(X, m, slot, ks) X[m] = ldsfrag(xrd + (slot) * SLOT + (ks) * 512 + (m) * 2048)
#define RDW(W, n, slot, ks) W[n] = ldstr(wrd + (slot) * SLOT + (ks) * 4096 + (n) * 256, wrd + (slot) * SLOT + (ks) * 4096 + 1024 + (n) * 256)
#define SUB2(X, W, XN, WN, DOREAD, rs, rk, DODMA, du, dl, i0, ISH) do { \
    acc[0][0] = MFMA32(W[0], X[0], acc[0][0]); if (DOREAD) { RDW(WN, 0, rs, rk); } SB(); \
    acc[0][1] = MFMA32(W[1], X[0], acc[0][1]); if (DOREAD) { RDX(XN, 0, rs, rk); RDX(XN, 1, rs, rk); } if (DODMA) piece(du, dl, i0, ISH); SB(); \
    acc[1][0] = MFMA32(W[0], X[1], acc[1][0]); if (DOREAD) { RDW(WN, 1, rs, rk); } SB(); \
    acc[1][1] = MFMA32(W[1], X[1], acc[1][1]); if (DODMA) piece(du, dl, i0 + 2, ISH); SB(); } while (0)
  WAIT_BAR(14);
  RDX(xa, 0, 0, 0); RDX(xa, 1, 0, 0); RDW(wa, 0, 0, 0); RDW(wa, 1, 0, 0);
  int slot = 0;
#define STEP2(WAITN, DMA_A, DMA_B, RD_B, HA, HB) do { \
    const int ns = slot == NS - 1 ? 0 : slot + 1, ps = slot == 0 ? NS - 1 : slot - 1; \
    SUB2(xa, wa, xb, wb, true, slot, 1, DMA_A, u + 4, ps, 1, HA); \
    WAIT_BAR(WAITN); \
    SUB2(xb, wb, xa, wa, RD_B, ns, 0, DMA_B, u + 5, slot, 0, HB); \
    slot = ns; } while (0)
  int u = 0;
  for (; u < 51; ++u) STEP2(12, true, true, true, true, true);
  STEP2(12, true, true, true, true, false); ++u;
  for (; u < 67; ++u) STEP2(12, true, true, true, false, false);
  STEP2(12, true, false, true, false, false); ++u;
  STEP2(8, false, false, true, false, false); ++u; STEP2(4, false, false, true, false, false); ++u; STEP2(0, false, false, true, false, false); ++u; STEP2(0, false, false, false, false, false);
  STAMP(1);
  if (kh) {
#pragma unroll
    for (int nb = 0; nb < 2; ++nb) { const f32x16 t = acc[0][nb]; acc[0][nb] = acc[1][nb]; acc[1][nb] = t; }
    PIN(acc[0][0]); PIN(acc[0][1]); PIN(acc[1][0]); PIN(acc[1][1]);
  }
  {
    const __amdgpu_buffer_rsrc_t rP = mkrsrc(ws + WS_PART + (size_t)g * 32768, 32768);
#pragma unroll
    for (int nb = 0; nb < 2; ++nb)
#pragma unroll
      for (int q = 0; q < 4; ++q) {
        const f32x16 a = acc[1][nb];
        st_wt16(rP, ((w * 2 + nb) * 4 + q) * 1024 + l * 16, (u32x4){__float_as_uint(a[4 * q]), __float_as_uint(a[4 * q + 1]), __float_as_uint(a[4 * q + 2]), __float_as_uint(a[4 * q + 3])});
      }
    asm volatile("s_waitcnt vmcnt(0)" ::: "memory");
    __syncthreads();
    GAS unsigned* flags = (GAS unsigned*)(ws + WS_FLAGS);
    if (tid == 0) __hip_atomic_store(flags + 64 * g, 1u, __ATOMIC_RELAXED, __HIP_MEMORY_SCOPE_AGENT);
    if (w == 0) {
      unsigned spins = 0;
      while (__builtin_amdgcn_readfirstlane(__hip_atomic_load(flags + 64 * partner, __ATOMIC_RELAXED, __HIP_MEMORY_SCOPE_AGENT)) != 1u) { __builtin_amdgcn_s_sleep(1); if (++spins > (1u << 22)) break; }
      __builtin_amdgcn_fence(__ATOMIC_ACQUIRE, "agent");
      asm volatile("s_waitcnt vmcnt(0)" ::: "memory");
    }
    __syncthreads();
  }
  STAMP(2);
  const char* pp = ws + WS_PART + (size_t)partner * 32768;
  const int token = 128 * p + 64 * wm + 32 * kh + r32;
#pragma unroll
  for (int nb = 0; nb < 2; ++nb) {
    const f32x16 a = acc[0][nb];
    const int n0 = 128 * nt + 64 * wn + 32 * nb;
    float y[16];
#pragma unroll
    for (int q = 0; q < 4; ++q) {
      const f32x4 pv = *(const f32x4*)(pp + ((w * 2 + nb) * 4 + q) * 1024 + l * 16);
      const u32x2 bb = *(const u32x2*)(b2 + n0 + 8 * q + 4 * h);
      const u32x2 xx = *(const u32x2*)(x + (size_t)token * D_ + n0 + 8 * q + 4 * h);
      y[4 * q + 0] = a[4 * q + 0] + pv[0] + lo2f(bb[0]) + lo2f(xx[0]); y[4 * q + 1] = a[4 * q + 1] + pv[1] + hi2f(bb[0]) + hi2f(xx[0]);
      y[4 * q + 2] = a[4 * q + 2] + pv[2] + lo2f(bb[1]) + lo2f(xx[1]); y[4 * q + 3] = a[4 * q + 3] + pv[3] + hi2f(bb[1]) + hi2f(xx[1]);
    }
    unsigned d[8];
#pragma unroll
    for (int i = 0; i < 8; ++i) d[i] = cvtpk(y[2 * i], y[2 * i + 1]);
#pragma unroll
    for (int g2 = 0; g2 < 2; ++g2) {
      auto s0 = __builtin_amdgcn_permlane32_swap(d[4 * g2], d[4 * g2 + 2], false, false);
      auto s1 = __builtin_amdgcn_permlane32_swap(d[4 * g2 + 1], d[4 * g2 + 3], false, false);
      *(u32x4*)(out + (size_t)token * D_ + n0 + 16 * g2 + 8 * h) = (u32x4){s0[0], s1[0], s0[1], s1[1]};
    }
  }
  STAMP(3);
#undef RDX
#undef RDW
}

constexpr int LDS_BYTES = 81920;
#define MODE 0
__global__ __launch_bounds__(256, 2) void k1_attn_w1(const u16* __restrict__ wq, const u16* __restrict__ w1, const u16* __restrict__ b1, char* __restrict__ ws, int first_role) {
  extern __shared__ __attribute__((aligned(16))) char lds_[];
  LAS char* lds = (LAS char*)lds_;
  const int role = (blockIdx.x >> 8) ^ first_role;
  if (role == 0) { __builtin_amdgcn_s_setprio(1); role_attn(blockIdx.x & 255, wq, ws, lds); }
  else role_w1(blockIdx.x & 255, w1, b1, ws, lds);
}
__global__ __launch_bounds__(256, 2) void k2_w2(const u16* __restrict__ w2, const u16* __restrict__ wo, const u16* __restrict__ b2, const u16* __restrict__ x, u16* __restrict__ out, char* __restrict__ ws) {
  extern __shared__ __attribute__((aligned(16))) char lds_[];
  role_w2(blockIdx.x, w2, wo, b2, x, out, ws, (LAS char*)lds_);
}

extern "C" void kernel_launch(void* const* d_in, const int* in_sizes, int n_in, void* d_out, int out_size, void* d_ws, size_t ws_size, hipStream_t stream) {
  const u16* x = (const u16*)d_in[0]; const u16* wq = (const u16*)d_in[1]; const u16* wk = (const u16*)d_in[2];
  const u16* wv = (const u16*)d_in[3]; const u16* wo = (const u16*)d_in[4];
  const u16* w1 = (const u16*)d_in[6]; const u16* b1 = (const u16*)d_in[7]; const u16* w2 = (const u16*)d_in[8]; const u16* b2 = (const u16*)d_in[9];
  char* ws = (char*)d_ws;
  (void)hipFuncSetAttribute((const void*)k1_attn_w1, hipFuncAttributeMaxDynamicSharedMemorySize, LDS_BYTES);
  (void)hipFuncSetAttribute((const void*)k2_w2, hipFuncAttributeMaxDynamicSharedMemorySize, LDS_BYTES);
  k0_ln_kv<<<256, 512, 0, stream>>>(x, wk, wv, ws);
  k1_attn_w1<<<512, 256, LDS_BYTES, stream>>>(wq, w1, b1, ws, 0);
  k2_w2<<<256, 256, LDS_BYTES, stream>>>(w2, wo, b2, x, (u16*)d_out, ws);
}
```

```cpp
#include <hip/hip_runtime.h>
#include <stdint.h>

typedef unsigned short u16;
typedef __bf16 bf16x8 __attribute__((ext_vector_type(8)));
typedef __bf16 bf16x2 __attribute__((ext_vector_type(2)));
typedef short s16x8 __attribute__((ext_vector_type(8)));
typedef float f32x4 __attribute__((ext_vector_type(4)));
typedef float f32x2 __attribute__((ext_vector_type(2)));
typedef unsigned u32x4 __attribute__((ext_vector_type(4)));
typedef unsigned u32x2 __attribute__((ext_vector_type(2)));
#define LAS __attribute__((address_space(3)))
#define GAS __attribute__((address_space(1)))
#define DI __device__ __forceinline__
#define SB() __builtin_amdgcn_sched_barrier(0)
#define PIN(x) asm volatile("" : "+v"(x))
#define MFMA16(a, b, c) __builtin_amdgcn_mfma_f32_16x16x32_bf16((a), (b), (c), 0, 0, 0)
#define BF(x) __builtin_bit_cast(bf16x8, x)

#define PF0ROWS 1024
constexpr int S_ = 2048, D_ = 1024, F_ = 3584;
constexpr size_t WS_FLAGS = 0;
constexpr size_t WS_R2   = 65536;
constexpr size_t WS_KV   = WS_R2 + 8192;
constexpr size_t WS_XN   = WS_KV + 262144;
constexpr size_t WS_H    = WS_XN + 4194304;
constexpr size_t WS_O    = WS_H + (size_t)112 * 128 * 1024;
constexpr size_t WS_PART = WS_O + 4194304;
constexpr size_t WS_STAMP = WS_PART + (size_t)256 * 32768 + 4194304;

constexpr float QSCALE = 0.17677669529663687f * 1.4426950408889634f;

DI float bf2f(u16 v) { return __uint_as_float(((unsigned)v) << 16); }
DI unsigned cvtpk(float lo, float hi) { f32x2 v = {lo, hi}; bf16x2 b = __builtin_convertvector(v, bf16x2); return __builtin_bit_cast(unsigned, b); }
DI float lo2f(unsigned u) { return __uint_as_float(u << 16); }
DI float hi2f(unsigned u) { return __uint_as_float(u & 0xffff0000u); }
DI __amdgpu_buffer_rsrc_t mkrsrc(const void* p, unsigned bytes) { return __builtin_amdgcn_make_buffer_rsrc((void*)p, 0, bytes, 0x00020000); }
DI void dma16(unsigned lds_addr, unsigned voff, __amdgpu_buffer_rsrc_t rsrc, unsigned soff) {
  asm volatile("s_mov_b32 m0, %0\n\ts_nop 0\n\tbuffer_load_dwordx4 %1, %2, %3 offen lds" :: "s"(lds_addr), "v"(voff), "s"(rsrc), "s"(soff));
}
template <int IMM, int INC> DI void dma2(unsigned mbase, unsigned v0, unsigned v1, __amdgpu_buffer_rsrc_t rsrc, unsigned& soff) {
  asm volatile("s_add_u32 m0, %3, %5\n\ts_add_u32 %0, %0, %6\n\tbuffer_load_dwordx4 %1, %4, %0 offen lds\n\tbuffer_load_dwordx4 %2, %4, %0 offen offset:1024 lds"
               : "+s"(soff) : "v"(v0), "v"(v1), "s"(mbase), "s"(rsrc), "i"(IMM), "i"(INC) : "scc");
}
template <int IMM, int INC> DI void dma1a(unsigned mbase, unsigned v0, __amdgpu_buffer_rsrc_t rsrc, unsigned& soff) {
  asm volatile("s_add_u32 m0, %2, %4\n\ts_add_u32 %0, %0, %5\n\tbuffer_load_dwordx4 %1, %3, %0 offen lds" : "+s"(soff) : "v"(v0), "s"(mbase), "s"(rsrc), "i"(IMM), "i"(INC) : "scc");
}
template <int OFF> DI void dma1b(unsigned v0, __amdgpu_buffer_rsrc_t rsrc, unsigned soff) {
  asm volatile("buffer_load_dwordx4 %0, %1, %2 offen offset:%3 lds" :: "v"(v0), "s"(rsrc), "s"(soff), "i"(OFF));
}
template <int IMM, int INC> DI void dma1a_c(unsigned mbase, unsigned v0, __amdgpu_buffer_rsrc_t rsrc, unsigned& soff) {
  asm volatile("s_add_u32 m0, %2, %4\n\ts_add_u32 %0, %0, %5\n\tbuffer_load_dwordx4 %1, %3, %0 offen sc1 lds" : "+s"(soff) : "v"(v0), "s"(mbase), "s"(rsrc), "i"(IMM), "i"(INC) : "scc");
}
template <int OFF> DI void dma1b_c(unsigned v0, __amdgpu_buffer_rsrc_t rsrc, unsigned soff) {
  asm volatile("buffer_load_dwordx4 %0, %1, %2 offen offset:%3 sc1 lds" :: "v"(v0), "s"(rsrc), "s"(soff), "i"(OFF));
}
DI void dma4(unsigned lds_addr, unsigned voff, __amdgpu_buffer_rsrc_t rsrc, unsigned soff) {
  asm volatile("s_mov_b32 m0, %0\n\ts_nop 0\n\tbuffer_load_dword %1, %2, %3 offen lds" :: "s"(lds_addr), "v"(voff), "s"(rsrc), "s"(soff));
}
#define WAIT_BAR(N) asm volatile("s_waitcnt vmcnt(" #N ") lgkmcnt(0)\n\ts_barrier" ::: "memory")
DI bf16x8 ldsfrag(const LAS char* p) { return *(const LAS bf16x8*)p; }
typedef short v4i16_t __attribute__((ext_vector_type(4)));
DI bf16x8 ldstr(const LAS char* p0, const LAS char* p1) {
  v4i16_t a = __builtin_amdgcn_ds_read_tr16_b64_v4i16((LAS v4i16_t*)p0);
  v4i16_t b = __builtin_amdgcn_ds_read_tr16_b64_v4i16((LAS v4i16_t*)p1);
  s16x8 v = __builtin_shufflevector(a, b, 0, 1, 2, 3, 4, 5, 6, 7);
  return __builtin_bit_cast(bf16x8, v);
}
DI void st_wt16(__amdgpu_buffer_rsrc_t r, unsigned voff, u32x4 v) { __builtin_amdgcn_raw_buffer_store_b128(v, r, voff, 0, 16); }
DI u32x4 chunk_of(unsigned x0, unsigned x1, unsigned y0, unsigned y1) {
  auto s0 = __builtin_amdgcn_permlane16_swap(x0, y0, false, false);
  auto s1 = __builtin_amdgcn_permlane16_swap(x1, y1, false, false);
  return (u32x4){s0[0], s1[0], s0[1], s1[1]};
}
DI unsigned wdma_off(int l, int kblk, int ldbytes) {
  const int row = l >> 3, c8 = l & 7, sw = ((row >> 1) & 1) | ((kblk & 1) << 1);
  return row * ldbytes + (((c8 >> 1) ^ sw) * 32) + (c8 & 1) * 16;
}
DI unsigned wrd_off(int l, int seg) {
  const int g = l >> 4, q = (l & 15) >> 2, sw = ((q >> 1) & 1) | ((g & 1) << 1);
  return q * 128 + ((seg ^ sw) * 32) + (l & 3) * 8;
}
DI float xsum4(float v) {
  auto a = __builtin_amdgcn_permlane16_swap(__float_as_uint(v), __float_as_uint(v), false, false); v = __uint_as_float(a[0]) + __uint_as_float(a[1]);
  auto b = __builtin_amdgcn_permlane32_swap(__float_as_uint(v), __float_as_uint(v), false, false); return __uint_as_float(b[0]) + __uint_as_float(b[1]);
}
DI float xmax4(float v) {
  auto a = __builtin_amdgcn_permlane16_swap(__float_as_uint(v), __float_as_uint(v), false, false); v = fmaxf(__uint_as_float(a[0]), __uint_as_float(a[1]));
  auto b = __builtin_amdgcn_permlane32_swap(__float_as_uint(v), __float_as_uint(v), false, false); return fmaxf(__uint_as_float(b[0]), __uint_as_float(b[1]));
}
#define STAMP(i)

constexpr int FL_H = 0, FL_O = 32, FL_X = 64;
DI GAS unsigned* flagp(char* ws, int i) { return (GAS unsigned*)(ws + WS_FLAGS) + 32 * i; }
DI void signal(char* ws, int i) { __hip_atomic_fetch_add(flagp(ws, i), 1u, __ATOMIC_RELAXED, __HIP_MEMORY_SCOPE_AGENT); }
DI void await(char* ws, int i, unsigned want) {
  GAS unsigned* f = flagp(ws, i);
  for (unsigned spins = 0; spins < (1u << 20); ++spins) {
    if ((unsigned)__builtin_amdgcn_readfirstlane(__hip_atomic_load(f, __ATOMIC_RELAXED, __HIP_MEMORY_SCOPE_AGENT)) >= want) break;
    __builtin_amdgcn_s_sleep(2);
  }
  asm volatile("" ::: "memory");
}

__global__ __launch_bounds__(512) void k0_ln_kv(const u16* __restrict__ x, const u16* __restrict__ wk, const u16* __restrict__ wv, const u16* __restrict__ wq, const u16* __restrict__ w1, char* __restrict__ ws) {
  __shared__ __attribute__((aligned(16))) char lds[65536 + 16384 + 2048 + 1024 + 256];
  float* part = (float*)(lds + 65536);
  float* fin = (float*)(lds + 65536 + 16384);
  float* red = (float*)(lds + 65536 + 16384 + 2048);
  const int tid = threadIdx.x, l = tid & 63, w = __builtin_amdgcn_readfirstlane(tid >> 6);
  const int rb = blockIdx.x & 127, hf = blockIdx.x >> 7;
  const int r = l & 15, c = l >> 4; const int stamp_base = 768; (void)stamp_base;
  STAMP(0);
  u32x4 xv[4];
  { const u16* xr = x + (size_t)(rb * 16 + r) * D_ + w * 128 + c * 8;
    asm volatile("global_load_dwordx4 %0, %4, off\n\tglobal_load_dwordx4 %1, %4, off offset:64\n\tglobal_load_dwordx4 %2, %4, off offset:128\n\tglobal_load_dwordx4 %3, %4, off offset:192"
                 : "=&v"(xv[0]), "=&v"(xv[1]), "=&v"(xv[2]), "=&v"(xv[3]) : "v"(xr)); }
  const unsigned lds0 = (unsigned)(uintptr_t)lds;
  {
    __amdgpu_buffer_rsrc_t rw = mkrsrc(hf ? wv : wk, 65536);
#pragma unroll
    for (int i = 0; i < 8; ++i) dma16(lds0 + w * 8192 + i * 1024, l * 16, rw, w * 8192 + i * 1024);
  }
  asm volatile("s_waitcnt vmcnt(8)" : "+v"(xv[0]), "+v"(xv[1]), "+v"(xv[2]), "+v"(xv[3]));
  {
    const int wv_ = blockIdx.x * 8 + w;
    const bool isq = wv_ < 256;
    dma4(lds0 + 65536 + 16384 + 2048 + 1024, ((isq ? wv_ : wv_ - 256) * 64 + l) * 128, mkrsrc(isq ? wq : w1, isq ? PF0ROWS * D_ * 2 : PF0ROWS * F_ * 2), 0);
  }
  if (blockIdx.x < 8) ((u32x4*)(ws + WS_FLAGS))[blockIdx.x * 512 + tid] = (u32x4){0, 0, 0, 0};
  float v[32];
#pragma unroll
  for (int i = 0; i < 4; ++i)
#pragma unroll
    for (int j = 0; j < 4; ++j) { v[i * 8 + 2 * j] = lo2f(xv[i][j]); v[i * 8 + 2 * j + 1] = hi2f(xv[i][j]); }
  float s = 0.f;
#pragma unroll
  for (int i = 0; i < 32; ++i) s += v[i];
  s = xsum4(s);
  if (c == 0) red[w * 16 + r] = s;
  __syncthreads();
  float mean = 0.f;
#pragma unroll
  for (int i = 0; i < 8; ++i) mean += red[i * 16 + r];
  mean *= (1.0f / D_);
  float q = 0.f;
#pragma unroll
  for (int i = 0; i < 32; ++i) { v[i] -= mean; q += v[i] * v[i]; }
  q = xsum4(q);
  if (c == 0) red[128 + w * 16 + r] = q;
  __syncthreads();
  float var = 0.f;
#pragma unroll
  for (int i = 0; i < 8; ++i) var += red[128 + i * 16 + r];
  var *= (1.0f / D_);
  const float rstd = 1.0f / sqrtf(var + 1e-5f);
  if (hf == 0 && w == 0 && c == 0) ((float*)(ws + WS_R2))[rb * 16 + r] = 1.0f / sqrtf(var * rstd * rstd + 1e-5f);
  u32x4 xn[4];
#pragma unroll
  for (int i = 0; i < 4; ++i)
#pragma unroll
    for (int j = 0; j < 4; ++j) xn[i][j] = cvtpk(v[i * 8 + 2 * j] * rstd, v[i * 8 + 2 * j + 1] * rstd);
#pragma unroll
  for (int i = 0; i < 2; ++i) *(u32x4*)(ws + WS_XN + ((size_t)(4 * w + 2 * i + hf) * 128 + rb) * 1024 + l * 16) = hf ? xn[2 * i + 1] : xn[2 * i];
  STAMP(1);
  asm volatile("s_waitcnt vmcnt(3)" ::: "memory");
  f32x4 acc[2] = {{0.f, 0.f, 0.f, 0.f}, {0.f, 0.f, 0.f, 0.f}};
  const LAS char* wb = (const LAS char*)lds + w * 8192 + (8 * c + ((l & 15) >> 2)) * 64 + (l & 3) * 8;
#pragma unroll
  for (int i = 0; i < 4; ++i)
#pragma unroll
    for (int b = 0; b < 2; ++b) {
      bf16x8 wf = ldstr(wb + i * 2048 + b * 32, wb + i * 2048 + b * 32 + 256);
      acc[b] = MFMA16(BF(xn[i]), wf, acc[b]);
    }
#pragma unroll
  for (int b = 0; b < 2; ++b)
#pragma unroll
    for (int j = 0; j < 4; ++j) part[(w * 16 + 4 * c + j) * 32 + 16 * b + r] = acc[b][j];
  __syncthreads();
  { float t = 0.f;
#pragma unroll
    for (int i = 0; i < 8; ++i) t += part[i * 512 + tid];
    fin[tid] = t; }
  __syncthreads();
  if (tid < 64) {
    float o[8];
    char* dst = ws + WS_KV + (size_t)(rb >> 1) * 4096;
    const int half = rb & 1;
    if (hf == 0) { const int kb = tid >> 5, g = (tid >> 3) & 3, rr = tid & 7;
      const int row = 8 * half + rr, key = 8 * (row >> 2) + 4 * kb + (row & 3) - 16 * half;
#pragma unroll
      for (int j = 0; j < 8; ++j) o[j] = fin[key * 32 + 16 * (j >> 2) + 4 * g + (j & 3)];
      dst += kb * 1024 + (g * 16 + row) * 16;
    } else { const int db = tid >> 5, gl = (tid >> 4) & 1, rr = tid & 15;
#pragma unroll
      for (int j = 0; j < 8; ++j) o[j] = fin[(8 * gl + j) * 32 + 16 * db + rr];
      dst += 2048 + db * 1024 + ((2 * half + gl) * 16 + rr) * 16;
    }
    *(u32x4*)dst = (u32x4){cvtpk(o[0], o[1]), cvtpk(o[2], o[3]), cvtpk(o[4], o[5]), cvtpk(o[6], o[7])};
  }
  STAMP(2);
}

#define MERGED 1
#define PRIO_G1 0
#define PRIO_GE 2
#define PRIO_G2 0
#define LSUM_MFMA 1
DI float fadd_s(float a, float b) { float r; asm("s_nop 0\n\tv_add_f32_e32 %0, %1, %2" : "=v"(r) : "v"(a), "v"(b)); return r; }

DI void attn_slow(const char* __restrict__ kv, bf16x8 qf, f32x4& o0, f32x4& o1) {
  const int l = threadIdx.x & 63;
  o0 = (f32x4){0.f, 0.f, 0.f, 0.f}; o1 = o0;
  float m = -3.0e38f, ls = 0.f;
#pragma unroll 1
  for (int t = 0; t < 64; ++t) {
    const char* p = kv + t * 4096 + l * 16;
    const bf16x8 k0 = *(const bf16x8*)p, k1 = *(const bf16x8*)(p + 1024), v0 = *(const bf16x8*)(p + 2048), v1 = *(const bf16x8*)(p + 3072);
    const f32x4 z = {0.f, 0.f, 0.f, 0.f};
    f32x4 s0 = MFMA16(k0, qf, z), s1 = MFMA16(k1, qf, z);
    float mx = fmaxf(fmaxf(fmaxf(s0[0], s0[1]), fmaxf(s0[2], s0[3])), fmaxf(fmaxf(s1[0], s1[1]), fmaxf(s1[2], s1[3])));
    mx = fmaxf(mx, __shfl_xor(mx, 16)); mx = fmaxf(mx, __shfl_xor(mx, 32));
    const float mn = fmaxf(m, mx), al = __builtin_amdgcn_exp2f(m - mn);
    m = mn; float rs = 0.f;
#pragma unroll
    for (int i = 0; i < 4; ++i) { s0[i] = __builtin_amdgcn_exp2f(s0[i] - mn); s1[i] = __builtin_amdgcn_exp2f(s1[i] - mn); rs += s0[i] + s1[i]; o0[i] *= al; o1[i] *= al; }
    rs += __shfl_xor(rs, 16); rs += __shfl_xor(rs, 32); ls = ls * al + rs;
    const u32x4 pp = {cvtpk(s0[0], s0[1]), cvtpk(s0[2], s0[3]), cvtpk(s1[0], s1[1]), cvtpk(s1[2], s1[3])};
    o0 = MFMA16(v0, BF(pp), o0); o1 = MFMA16(v1, BF(pp), o1);
  }
#pragma unroll
  for (int i = 0; i < 4; ++i) { o0[i] /= ls; o1[i] /= ls; }
}

DI void role_attn(int a, const u16* __restrict__ wq, const u16* __restrict__ w2, const u16* __restrict__ wo, char* __restrict__ ws, LAS char* lds) {
  const int tid = threadIdx.x, l = tid & 63, w = __builtin_amdgcn_readfirstlane(tid >> 6);
  const int xcd = a & 7, idx = a >> 3, tb = (xcd >> 2) * 16 + (idx & 15), hg = (xcd & 3) * 2 + (idx >> 4);
  const int g = l >> 4; const int stamp_base = 0; (void)stamp_base;
  const unsigned lds0 = (unsigned)(uintptr_t)lds;
  constexpr int SLOT = 24576;
  const __amdgpu_buffer_rsrc_t rX = mkrsrc(ws + WS_XN, 4194304), rKV = mkrsrc(ws + WS_KV, 262144);
  const unsigned vlin = l * 16;
  constexpr int QS = 12288;
  const __amdgpu_buffer_rsrc_t rWq = mkrsrc((const char*)wq - 1024, D_ * D_ * 2 + 1024);
  const unsigned vq0 = wdma_off(l, w, 2048) + hg * 256 + w * 8 * 2048 + 1024, vq1 = vq0 + 128 - 1024;
  unsigned xso = (4 * tb + w) * 1024 - 131072, qso = 0u - 65536;
  const unsigned mx = lds0 + w * 1024, mq = lds0 + 4096 + w * 2048;
#define PCQ(SL, i) do { if constexpr ((i) == 0) dma1a<(SL) * QS, 131072>(mx, vlin, rX, xso); else if constexpr ((i) == 1) dma1a<(SL) * QS, 65536>(mq, vq0, rWq, qso); else dma1b<1024>(vq1, rWq, qso); } while (0)
#define STGQ(SL) do { PCQ(SL, 0); PCQ(SL, 1); PCQ(SL, 2); } while (0)
  STAMP(0);
  STGQ(0); STGQ(1); STGQ(2); STGQ(3); STGQ(4); STGQ(5);
  f32x4 qacc[2][4];
#pragma unroll
  for (int i = 0; i < 2; ++i)
#pragma unroll
    for (int j = 0; j < 4; ++j) qacc[i][j] = (f32x4){0.f, 0.f, 0.f, 0.f};
  const LAS char* xrd = lds + l * 16;
  const LAS char* wrd0 = lds + 4096 + (g * 2 + (w >> 1)) * 1024 + wrd_off(l, 2 * (w & 1));
  const LAS char* wrd1 = lds + 4096 + (g * 2 + (w >> 1)) * 1024 + wrd_off(l, 2 * (w & 1) + 1);
  bf16x8 qxa[4], qwa[2], qxb[4], qwb[2];
#define QRX(X, m, SL) X[m] = ldsfrag(xrd + (SL) * QS + (m) * 1024)
#define QRW(W, n, SL) W[n] = ((n) == 0 ? ldstr(wrd0 + (SL) * QS, wrd0 + (SL) * QS + 512) : ldstr(wrd1 + (SL) * QS, wrd1 + (SL) * QS + 512))
#define QM(X, W, m, n) qacc[n][m] = MFMA16(W[n], X[m], qacc[n][m])
#define STEPQ(X, W, XN, WN, RD, RS, DMA, DS) do { \
    QM(X, W, 0, 0); if (RD) { QRW(WN, 0, RS); QRX(XN, 0, RS); } SB(); QM(X, W, 1, 0); if (RD) { QRW(WN, 1, RS); QRX(XN, 1, RS); } SB(); QM(X, W, 2, 0); if (RD) QRX(XN, 2, RS); SB(); QM(X, W, 3, 0); if (RD) QRX(XN, 3, RS); SB(); \
    QM(X, W, 0, 1); if (DMA) PCQ(DS, 0); SB(); QM(X, W, 1, 1); if (DMA) PCQ(DS, 1); SB(); QM(X, W, 2, 1); if (DMA) PCQ(DS, 2); SB(); QM(X, W, 3, 1); SB(); } while (0)
  WAIT_BAR(15);
#pragma unroll
  for (int m = 0; m < 4; ++m) QRX(qxa, m, 0);
  QRW(qwa, 0, 0); QRW(qwa, 1, 0);
#define QEV(SC, SN, W_, RD, DMA) do { WAIT_BAR(W_); SB(); STEPQ(qxa, qwa, qxb, qwb, RD, SN, DMA, SC); } while (0)
#define QOD(SC, SN, W_, RD, DMA) do { WAIT_BAR(W_); SB(); STEPQ(qxb, qwb, qxa, qwa, RD, SN, DMA, SC); } while (0)
  QEV(0, 1, 12, true, true); QOD(1, 2, 12, true, true); QEV(2, 3, 12, true, true); QOD(3, 4, 12, true, true); QEV(4, 5, 12, true, true); QOD(5, 0, 12, true, true);
  for (int it = 0; it < 3; ++it) { QEV(0, 1, 12, true, true); QOD(1, 2, 12, true, true); QEV(2, 3, 12, true, true); QOD(3, 4, 12, true, true); QEV(4, 5, 12, true, true); QOD(5, 0, 12, true, true); }
  QEV(0, 1, 12, true, true); QOD(1, 2, 12, true, true);
  QEV(2, 3, 12, true, false); QOD(3, 4, 9, true, false); QEV(4, 5, 6, true, false); QOD(5, 0, 3, true, false); QEV(0, 1, 0, true, false);
  unsigned kvso = w * 4096 - 16384;
  const unsigned kvm = lds0 + w * 4096;
#define STGKV(SF) do { dma1a<(SF) * SLOT, 16384>(kvm, vlin, rKV, kvso); dma1b<1024>(vlin, rKV, kvso); dma1b<2048>(vlin, rKV, kvso); dma1b<3072>(vlin, rKV, kvso); } while (0)
  STGKV(1); STGKV(2);
  {
    const unsigned line = (a * 4 + w) * 64 + l;
    dma4(lds0 + 6 * QS, line * 128, mkrsrc(w2, F_ * D_ * 2), 0);
    dma4(lds0 + 6 * QS, line * 128, mkrsrc(wo, D_ * D_ * 2), 0);
  }
  STEPQ(qxb, qwb, qxa, qwa, false, 0, false, 0);
  STAMP(1);
  bf16x8 qf[4];
#pragma unroll
  for (int m = 0; m < 4; ++m)
    qf[m] = BF(((u32x4){cvtpk(qacc[0][m][0] * QSCALE, qacc[0][m][1] * QSCALE), cvtpk(qacc[0][m][2] * QSCALE, qacc[0][m][3] * QSCALE),
                        cvtpk(qacc[1][m][0] * QSCALE, qacc[1][m][1] * QSCALE), cvtpk(qacc[1][m][2] * QSCALE, qacc[1][m][3] * QSCALE)}));
  WAIT_BAR(6);
  const LAS char* kvb = lds + l * 16;
  f32x4 negm[4], S[4][2], O[4][2];
  bf16x8 k0, k1;
  {
    const LAS char* p = kvb + 1 * SLOT;
    k0 = ldsfrag(p); k1 = ldsfrag(p + 1024);
    const f32x4 z = {0.f, 0.f, 0.f, 0.f};
#pragma unroll
    for (int m = 0; m < 4; ++m) {
      const f32x4 s0 = MFMA16(k0, qf[m], z), s1 = MFMA16(k1, qf[m], z);
      float mx = fmaxf(fmaxf(fmaxf(s0[0], s0[1]), fmaxf(s0[2], s0[3])), fmaxf(fmaxf(s1[0], s1[1]), fmaxf(s1[2], s1[3])));
      mx = xmax4(mx);
      negm[m] = (f32x4){-mx, -mx, -mx, -mx}; PIN(negm[m]);
      O[m][0] = z; O[m][1] = z;
    }
    S[0][0] = MFMA16(k0, qf[0], negm[0]); S[0][1] = MFMA16(k1, qf[0], negm[0]);
    S[1][0] = MFMA16(k0, qf[1], negm[1]); S[1][1] = MFMA16(k1, qf[1], negm[1]);
  }
  u32x4 P[4];
  P[2] = (u32x4){0, 0, 0, 0}; P[3] = P[2];
  bf16x8 va0 = BF(P[2]), va1 = va0, vb0, vb1;
  S[3][1] = (f32x4){0.f, 0.f, 0.f, 0.f};
  f32x4 L[4];
#pragma unroll
  for (int m = 0; m < 4; ++m) L[m] = (f32x4){0.f, 0.f, 0.f, 0.f};
  bf16x8 ones; { const short o1 = (l & 15) == 0 ? (short)0x3f80 : (short)0; s16x8 ov = {o1, o1, o1, o1, o1, o1, o1, o1}; ones = __builtin_bit_cast(bf16x8, ov); PIN(ones); }
#define LS(m) do { L[m] = MFMA16(ones, BF(P[m]), L[m]); PIN(L[m]); SB(); } while (0)
#define LADD2(m, kb, i)
#define NOLS(m)
#define EX2(m, kb, i) do { float t0_ = __builtin_amdgcn_exp2f(S[m][kb][i]), t1_ = __builtin_amdgcn_exp2f(S[m][kb][i + 1]); PIN(t0_); PIN(t1_); S[m][kb][i] = t0_; S[m][kb][i + 1] = t1_; SB(); } while (0)
#define CVA(m, kb) do { unsigned c_ = cvtpk(S[m][kb][0], S[m][kb][1]); PIN(c_); P[m][2 * kb] = c_; LADD2(m, kb, 0); SB(); } while (0)
#define CVB(m, kb) do { unsigned c_ = cvtpk(S[m][kb][2], S[m][kb][3]); PIN(c_); P[m][2 * kb + 1] = c_; LADD2(m, kb, 2); SB(); } while (0)
#define GRP(mq, kq, KF, pm, pk, me, ke, mp, db, VF, LSM, X) do { \
    S[mq][kq] = MFMA16(KF, qf[mq], negm[mq]); PIN(S[mq][kq]); SB(); X; CVB(pm, pk); EX2(me, ke, 0); \
    O[mp][db] = MFMA16(VF, BF(P[mp]), O[mp][db]); PIN(O[mp][db]); SB(); EX2(me, ke, 2); LSM(mp); CVA(me, ke); } while (0)
#define TILE(KC0, KC1, KN0, KN1, VP0, VP1, VC0, VC1, kn, vp, D) do { \
    GRP(2, 0, KC0, 3, 1, 0, 0, 2, 0, VP0, NOLS, KN0 = ldsfrag(kn); SB()); \
    GRP(2, 1, KC1, 0, 0, 0, 1, 2, 1, VP1, LS,   KN1 = ldsfrag((kn) + 1024); SB()); \
    GRP(3, 0, KC0, 0, 1, 1, 0, 3, 0, VP0, NOLS, VC0 = ldsfrag(vp); SB()); \
    GRP(3, 1, KC1, 1, 0, 1, 1, 3, 1, VP1, LS,   VC1 = ldsfrag((vp) + 1024); SB()); \
    GRP(0, 0, KN0, 1, 1, 2, 0, 0, 0, VC0, NOLS, D(0)); \
    GRP(0, 1, KN1, 2, 0, 2, 1, 0, 1, VC1, LS,   D(1)); \
    GRP(1, 0, KN0, 2, 1, 3, 0, 1, 0, VC0, NOLS, D(2)); \
    GRP(1, 1, KN1, 3, 0, 3, 1, 1, 1, VC1, LS,   D(3)); \
  } while (0)
#define NOD(i)
  bf16x8 kx0, kx1;
#define DKV0(SF) dma1a<(SF) * SLOT, 16384>(kvm, vlin, rKV, kvso); SB()
#define STAGE(SC, SN, SF, DM, LAST) do { \
    TILE(k0, k1, kx0, kx1, va0, va1, vb0, vb1, kvb + (SC) * SLOT + 4096, kvb + (SC) * SLOT + 2048, NOD); \
    TILE(kx0, kx1, k0, k1, vb0, vb1, va0, va1, kvb + (SC) * SLOT + 8192, kvb + (SC) * SLOT + 4096 + 2048, NOD); \
    TILE(k0, k1, kx0, kx1, va0, va1, vb0, vb1, kvb + (SC) * SLOT + 12288, kvb + (SC) * SLOT + 8192 + 2048, NOD); \
    WAIT_BAR(0);        \
    TILE(kx0, kx1, k0, k1, vb0, vb1, va0, va1, kvb + ((LAST) ? (SC) : (SN)) * SLOT, kvb + (SC) * SLOT + 12288 + 2048, DM); } while (0)
#define DM_(SF, i) do { if constexpr ((i) == 0) { dma1a<(SF) * SLOT, 16384>(kvm, vlin, rKV, kvso); } else { dma1b<(i) * 1024>(vlin, rKV, kvso); } SB(); } while (0)
#define DM0(i) DM_(0, i)
#define DM1(i) DM_(1, i)
#define DM2(i) DM_(2, i)
  for (int j = 0; j < 12; j += 3) { STAGE(1, 2, 0, DM0, false); STAGE(2, 0, 1, DM1, false); STAGE(0, 1, 2, DM2, false); STAMP(4 + j / 3); }
  STAGE(1, 2, 0, DM0, false); STAGE(2, 0, 1, DM1, false); STAGE(0, 1, 2, NOD, false); STAGE(1, 2, 0, NOD, true);
  CVB(3, 1);
  O[2][0] = MFMA16(va0, BF(P[2]), O[2][0]); O[2][1] = MFMA16(va1, BF(P[2]), O[2][1]); LS(2);
  O[3][0] = MFMA16(va0, BF(P[3]), O[3][0]); O[3][1] = MFMA16(va1, BF(P[3]), O[3][1]); LS(3);
  STAMP(2);
  const __amdgpu_buffer_rsrc_t rO = mkrsrc(ws + WS_O, 4194304);
#pragma unroll
  for (int m = 0; m < 4; ++m) {
    float ls = __shfl(L[m][0], l & 15);
    float inv = 1.0f / ls;
    f32x4 o0 = O[m][0], o1 = O[m][1];
    const float mo = fmaxf(fmaxf(fmaxf(fabsf(o0[0]), fabsf(o0[1])), fmaxf(fabsf(o0[2]), fabsf(o0[3]))), fmaxf(fmaxf(fabsf(o1[0]), fabsf(o1[1])), fmaxf(fabsf(o1[2]), fabsf(o1[3]))));
    const bool ok = (ls <= 1.0e18f) && (mo <= 1.0e37f);
    if (__builtin_expect(__any(!ok), 0)) { attn_slow(ws + WS_KV, qf[m], o0, o1); inv = 1.0f; }
    const u32x4 ch = chunk_of(cvtpk(o0[0] * inv, o0[1] * inv), cvtpk(o0[2] * inv, o0[3] * inv), cvtpk(o1[0] * inv, o1[1] * inv), cvtpk(o1[2] * inv, o1[3] * inv));
    const int c = ((g & 1) << 1) | (g >> 1);
    st_wt16(rO, ((4 * hg + w) * 128 + 4 * tb + m) * 1024 + c * 256 + (l & 15) * 16, ch);
  }
  asm volatile("s_waitcnt vmcnt(0)\n\ts_barrier" ::: "memory");
  if (tid == 0) signal(ws, FL_O + (tb >> 1) * 2 + (hg >> 2));
  STAMP(3);
}

DI f32x4 gelu4(f32x4 acc, float rs, f32x4 b, float c1v) {
  constexpr float c2 = -2.0f * 0.7978845608028654f * 1.4426950408889634f * 0.044715f;
  float x0 = acc[0], x1 = acc[1], x2 = acc[2], x3 = acc[3], t0, t1, t2, t3;
  asm("v_fma_f32 %0, %0, %8, %9\n\tv_fma_f32 %1, %1, %8, %10\n\tv_fma_f32 %2, %2, %8, %11\n\tv_fma_f32 %3, %3, %8, %12\n\t"
      "v_mul_f32 %4, %0, %0\n\tv_mul_f32 %5, %1, %1\n\tv_mul_f32 %6, %2, %2\n\tv_mul_f32 %7, %3, %3\n\t"
      "v_fma_f32 %4, %4, %14, %13\n\tv_fma_f32 %5, %5, %14, %13\n\tv_fma_f32 %6, %6, %14, %13\n\tv_fma_f32 %7, %7, %14, %13\n\t"
      "v_mul_f32 %4, %0, %4\n\tv_mul_f32 %5, %1, %5\n\tv_mul_f32 %6, %2, %6\n\tv_mul_f32 %7, %3, %7\n\t"
      "v_exp_f32 %4, %4\n\tv_exp_f32 %5, %5\n\tv_exp_f32 %6, %6\n\tv_exp_f32 %7, %7\n\t"
      "v_add_f32 %4, 1.0, %4\n\tv_add_f32 %5, 1.0, %5\n\tv_add_f32 %6, 1.0, %6\n\tv_add_f32 %7, 1.0, %7\n\t"
      "v_rcp_f32 %4, %4\n\tv_rcp_f32 %5, %5\n\tv_rcp_f32 %6, %6\n\tv_rcp_f32 %7, %7\n\t"
      "v_mul_f32 %0, %0, %4\n\tv_mul_f32 %1, %1, %5\n\tv_mul_f32 %2, %2, %6\n\tv_mul_f32 %3, %3, %7"
      : "+v"(x0), "+v"(x1), "+v"(x2), "+v"(x3), "=&v"(t0), "=&v"(t1), "=&v"(t2), "=&v"(t3)
      : "v"(rs), "v"(b[0]), "v"(b[1]), "v"(b[2]), "v"(b[3]), "v"(c1v), "s"(c2));
  return (f32x4){x0, x1, x2, x3};
}

DI void role_w1(int gi, const u16* __restrict__ w1, const u16* __restrict__ b1, char* __restrict__ ws, LAS char* lds) {
  const int tid = threadIdx.x, l = tid & 63, w = __builtin_amdgcn_readfirstlane(tid >> 6);
  const int xcd = gi & 7, idx = gi >> 3, p = (xcd >> 2) * 8 + (idx & 7), j = (xcd & 3) * 4 + (idx >> 3);
  const int g = l >> 4, wm = w >> 1, wn = w & 1; const int stamp_base = 256; (void)stamp_base;
  const unsigned lds0 = (unsigned)(uintptr_t)lds;
  constexpr int SLOT = 24576;
  const __amdgpu_buffer_rsrc_t rX = mkrsrc(ws + WS_XN, 4194304), rW = mkrsrc((const char*)w1 - 4096, D_ * F_ * 2 + 4096);
  const unsigned vlin = l * 16;
  unsigned vw[4];
#pragma unroll
  for (int i = 0; i < 4; ++i) vw[i] = wdma_off(l, w, F_ * 2) + 8 * w * (F_ * 2) + 448 * j - 64 * (j & 1) + 128 * i + 4096 - 1024 * i;
  STAMP(0);
  unsigned xso = (8 * p + 2 * w) * 1024 - 131072, wso = 0u - 32 * F_ * 2;
  const unsigned mx = lds0 + w * 2048, mw = lds0 + 8192 + w * 4096;
#define PC1(SL, i) do { if constexpr ((i) == 0) dma1a<(SL) * SLOT, 131072>(mx, vlin, rX, xso); else if constexpr ((i) == 1) dma1b<1024>(vlin, rX, xso); \
    else if constexpr ((i) == 2) dma1a<(SL) * SLOT, 32 * F_ * 2>(mw, vw[0], rW, wso); else dma1b<((i) - 2) * 1024>(vw[(i) - 2], rW, wso); } while (0)
#define STG1(SL) do { PC1(SL, 0); PC1(SL, 1); PC1(SL, 2); PC1(SL, 3); PC1(SL, 4); PC1(SL, 5); } while (0)
  STG1(0); STG1(1); STG1(2);
  constexpr int L_R2 = 3 * SLOT, L_B1 = L_R2 + 512;
  if (w < 2) dma4(lds0 + L_R2 + w * 256, (128 * p + 64 * w + l) * 4, mkrsrc(ws + WS_R2, 8192), 0);
  else dma4(lds0 + L_B1 + (w - 2) * 256, 448 * j + ((w - 2) * 64 + l) * 4, mkrsrc(b1, F_ * 2), 0);
  f32x4 acc[4][7];
#pragma unroll
  for (int m = 0; m < 4; ++m)
#pragma unroll
    for (int n = 0; n < 7; ++n) acc[m][n] = (f32x4){0.f, 0.f, 0.f, 0.f};
  const LAS char* xrd = lds + 4 * wm * 1024 + l * 16;
  const LAS char* wrb[5];
  { const int off = 6 * wn + 2 * (j & 1), nl = 12 + wn + 2 * (j & 1);
#pragma unroll
    for (int i = 0; i < 4; ++i) wrb[i] = lds + 8192 + (g * 4 + ((i + off) >> 2)) * 1024 + wrd_off(l, (i + off) & 3);
    wrb[4] = lds + 8192 + (g * 4 + (nl >> 2)) * 1024 + wrd_off(l, nl & 3); }
  bf16x8 xa[4], wa[7], xb[4], wb[7];
#define RDX(X, m, so) X[m] = ldsfrag(xrd + (so) + (m) * 1024)
#define RDW(W, n, so) W[n] = ((n) < 6 ? ldstr(wrb[(n) & 3] + (so) + ((n) >> 2) * 1024, wrb[(n) & 3] + (so) + ((n) >> 2) * 1024 + 512) : ldstr(wrb[4] + (so), wrb[4] + (so) + 512))
#define M1(X, W, m, n) acc[m][n] = MFMA16(W[n], X[m], acc[m][n])
#define STEP1(X, W, XN, WN, RD, RS, DMA, DS) do { \
    M1(X, W, 0, 0); if (RD) RDW(WN, 0, (RS) * SLOT); SB(); M1(X, W, 1, 0); if (RD) RDX(XN, 0, (RS) * SLOT); SB(); M1(X, W, 2, 0); if (RD) RDW(WN, 1, (RS) * SLOT); SB(); M1(X, W, 3, 0); if (DMA) PC1(DS, 0); SB(); \
    M1(X, W, 0, 1); if (RD) RDX(XN, 1, (RS) * SLOT); SB(); M1(X, W, 1, 1); if (RD) RDW(WN, 2, (RS) * SLOT); SB(); M1(X, W, 2, 1); if (RD) RDX(XN, 2, (RS) * SLOT); SB(); M1(X, W, 3, 1); if (DMA) PC1(DS, 1); SB(); \
    M1(X, W, 0, 2); if (RD) RDW(WN, 3, (RS) * SLOT); SB(); M1(X, W, 1, 2); if (RD) RDX(XN, 3, (RS) * SLOT); SB(); M1(X, W, 2, 2); if (RD) RDW(WN, 4, (RS) * SLOT); SB(); M1(X, W, 3, 2); if (DMA) PC1(DS, 2); SB(); \
    M1(X, W, 0, 3); if (RD) RDW(WN, 5, (RS) * SLOT); SB(); M1(X, W, 1, 3); SB(); M1(X, W, 2, 3); if (RD) RDW(WN, 6, (RS) * SLOT); SB(); M1(X, W, 3, 3); if (DMA) PC1(DS, 3); SB(); \
    M1(X, W, 0, 4); SB(); M1(X, W, 1, 4); SB(); M1(X, W, 2, 4); SB(); M1(X, W, 3, 4); if (DMA) PC1(DS, 4); SB(); \
    M1(X, W, 0, 5); SB(); M1(X, W, 1, 5); SB(); M1(X, W, 2, 5); SB(); M1(X, W, 3, 5); if (DMA) PC1(DS, 5); SB(); \
    M1(X, W, 0, 6); SB(); M1(X, W, 1, 6); SB(); M1(X, W, 2, 6); SB(); M1(X, W, 3, 6); SB(); } while (0)
  WAIT_BAR(13);
#pragma unroll
  for (int m = 0; m < 4; ++m) RDX(xa, m, 0);
#pragma unroll
  for (int n = 0; n < 7; ++n) RDW(wa, n, 0);
#define EV(SC, SN, W_, RD, DMA) do { WAIT_BAR(W_); SB(); STEP1(xa, wa, xb, wb, RD, SN, DMA, SC); } while (0)
#define OD(SC, SN, W_, RD, DMA) do { WAIT_BAR(W_); SB(); STEP1(xb, wb, xa, wa, RD, SN, DMA, SC); } while (0)
#define SIX() do { EV(0, 1, 6, true, true); OD(1, 2, 6, true, true); EV(2, 0, 6, true, true); OD(0, 1, 6, true, true); EV(1, 2, 6, true, true); OD(2, 0, 6, true, true); } while (0)
  for (int it = 0; it < 4; ++it) { SIX(); STAMP(4 + it); }
  EV(0, 1, 6, true, true); OD(1, 2, 6, true, true); EV(2, 0, 6, true, true); OD(0, 1, 6, true, true); EV(1, 2, 6, true, true);
  OD(2, 0, 6, true, false); EV(0, 1, 0, true, false); OD(1, 2, 0, false, false);
  STAMP(1);
  __builtin_amdgcn_s_setprio(PRIO_GE);
  float r2[4];
#pragma unroll
  for (int m = 0; m < 4; ++m) r2[m] = *(const LAS float*)(lds + L_R2 + (64 * wm + 16 * m + (l & 15)) * 4);
  const __amdgpu_buffer_rsrc_t rH = mkrsrc(ws + WS_H, 112 * 128 * 1024);
  const int cch = ((g & 1) << 1) | (g >> 1);
  unsigned last[4][2];
  float c1v = -2.0f * 0.7978845608028654f * 1.4426950408889634f; PIN(c1v);
#pragma unroll
  for (int n = 0; n < 7; ++n) {
    const int nb = n < 6 ? 6 * wn + n : 12 + wn;
    const u32x2 braw = *(const LAS u32x2*)(lds + L_B1 + (16 * nb + 4 * g) * 2);
    const f32x4 bb = {lo2f(braw[0]), hi2f(braw[0]), lo2f(braw[1]), hi2f(braw[1])};
#pragma unroll
    for (int m = 0; m < 4; ++m) acc[m][n] = gelu4(acc[m][n], r2[m], bb, c1v);
    if (n < 6) { if (n & 1) {
#pragma unroll
        for (int m = 0; m < 4; ++m) {
          const u32x4 ch = chunk_of(cvtpk(acc[m][n - 1][0], acc[m][n - 1][1]), cvtpk(acc[m][n - 1][2], acc[m][n - 1][3]), cvtpk(acc[m][n][0], acc[m][n][1]), cvtpk(acc[m][n][2], acc[m][n][3]));
          st_wt16(rH, ((7 * j + 3 * wn + (n >> 1)) * 128 + 8 * p + 4 * wm + m) * 1024 + cch * 256 + (l & 15) * 16, ch);
        } }
    } else {
#pragma unroll
      for (int m = 0; m < 4; ++m) { last[m][0] = cvtpk(acc[m][n][0], acc[m][n][1]); last[m][1] = cvtpk(acc[m][n][2], acc[m][n][3]); }
    }
  }
#pragma unroll
  for (int m = 0; m < 4; m += 2) {
    const u32x4 ch = chunk_of(last[m][0], last[m][1], last[m + 1][0], last[m + 1][1]);
    st_wt16(rH, ((7 * j + 6) * 128 + 8 * p + 4 * wm + m + (g & 1)) * 1024 + (2 * wn + (g >> 1)) * 256 + (l & 15) * 16, ch);
  }
  STAMP(2);
  __builtin_amdgcn_s_setprio(PRIO_G2);
  asm volatile("s_waitcnt vmcnt(0)\n\ts_barrier" ::: "memory");
  if (tid == 0) signal(ws, FL_H + p * 2 + (j >> 3));
  STAMP(3);
#undef RDX
#undef RDW
#undef EV
#undef OD
}

DI void role_w2(int gi, const u16* __restrict__ w2, const u16* __restrict__ wo, const u16* __restrict__ b2, const u16* __restrict__ x, u16* __restrict__ out, char* __restrict__ ws, LAS char* lds) {
  int tid = threadIdx.x; asm volatile("" : "+v"(tid));
  const int l = tid & 63, w = __builtin_amdgcn_readfirstlane(tid >> 6);
  const int xcd = gi & 7, idx = gi >> 3, kh = xcd & 1, p = (xcd >> 1) * 4 + (idx & 3), nt = idx >> 2;
  const int partner = gi ^ 1;
  const int g = l >> 4, wm = w >> 1, wn = w & 1; const int stamp_base = 512; (void)stamp_base;
  const unsigned lds0 = (unsigned)(uintptr_t)lds;
  constexpr int SLOT = 16384;
  const __amdgpu_buffer_rsrc_t rA = mkrsrc(ws + WS_H, 144 * 128 * 1024);
  __amdgpu_buffer_rsrc_t rW = mkrsrc((const char*)w2 - 1024, F_ * D_ * 2 + 1024);
  const unsigned vlin = l * 16;
  const unsigned vw0 = wdma_off(l, w, 2048) + nt * 256 + w * 8 * 2048 + 1024, vw1 = vw0 + 128 - 1024;
  if (w == 0) await(ws, FL_H + p * 2 + kh, 8);
  asm volatile("s_barrier" ::: "memory");
  STAMP(0);
  unsigned aso = (56 * kh * 128 + 8 * p + 2 * w) * 1024 - 131072, wso = 56 * kh * 65536 - 65536;
  const unsigned ma = lds0 + w * 2048, mw = lds0 + 8192 + w * 2048;
#define PC2(SL, i) do { if constexpr ((i) == 0) dma1a_c<(SL) * SLOT, 131072>(ma, vlin, rA, aso); else if constexpr ((i) == 1) dma1b_c<1024>(vlin, rA, aso); \
    else if constexpr ((i) == 2) dma1a<(SL) * SLOT, 65536>(mw, vw0, rW, wso); else dma1b<1024>(vw1, rW, wso); } while (0)
#define STG2(SL) do { PC2(SL, 0); PC2(SL, 1); PC2(SL, 2); PC2(SL, 3); } while (0)
  STG2(0); STG2(1); STG2(2); STG2(3); STG2(4);
  f32x4 acc[4][4];
#pragma unroll
  for (int m = 0; m < 4; ++m)
#pragma unroll
    for (int n = 0; n < 4; ++n) acc[m][n] = (f32x4){0.f, 0.f, 0.f, 0.f};
  const LAS char* xrd = lds + 4 * wm * 1024 + l * 16;
  const LAS char* wrb[4];
#pragma unroll
  for (int i = 0; i < 4; ++i) wrb[i] = lds + 8192 + (g * 2 + wn) * 1024 + wrd_off(l, i);
  bf16x8 xa[4], wa[4], xb[4], wb[4];
#define RDX(X, m, so) X[m] = ldsfrag(xrd + (so) + (m) * 1024)
#define RDW(W, n, so) W[n] = ldstr(wrb[n] + (so), wrb[n] + (so) + 512)
#define M2(X, W, m, n) acc[m][n] = MFMA16(W[n], X[m], acc[m][n])
#define STEP2(X, W, XN, WN, RD, RS, DMA, DS) do { \
    M2(X, W, 0, 0); if (RD) RDW(WN, 0, (RS) * SLOT); SB(); M2(X, W, 1, 0); if (RD) RDX(XN, 0, (RS) * SLOT); SB(); M2(X, W, 2, 0); if (RD) RDX(XN, 1, (RS) * SLOT); SB(); M2(X, W, 3, 0); if (DMA) PC2(DS, 0); SB(); \
    M2(X, W, 0, 1); if (RD) RDW(WN, 1, (RS) * SLOT); SB(); M2(X, W, 1, 1); if (RD) RDX(XN, 2, (RS) * SLOT); SB(); M2(X, W, 2, 1); if (RD) RDX(XN, 3, (RS) * SLOT); SB(); M2(X, W, 3, 1); if (DMA) PC2(DS, 1); SB(); \
    M2(X, W, 0, 2); if (RD) RDW(WN, 2, (RS) * SLOT); SB(); M2(X, W, 1, 2); if (RD) RDW(WN, 3, (RS) * SLOT); SB(); M2(X, W, 2, 2); SB(); M2(X, W, 3, 2); if (DMA) PC2(DS, 2); SB(); \
    M2(X, W, 0, 3); SB(); M2(X, W, 1, 3); SB(); M2(X, W, 2, 3); SB(); M2(X, W, 3, 3); if (DMA) PC2(DS, 3); SB(); } while (0)
  WAIT_BAR(16);
#pragma unroll
  for (int i = 0; i < 4; ++i) { RDX(xa, i, 0); RDW(wa, i, 0); }
#define EV(SC, SN, W_, RD, DMA) do { WAIT_BAR(W_); SB(); STEP2(xa, wa, xb, wb, RD, SN, DMA, SC); } while (0)
#define OD(SC, SN, W_, RD, DMA) do { WAIT_BAR(W_); SB(); STEP2(xb, wb, xa, wa, RD, SN, DMA, SC); } while (0)
  for (int it = 0; it < 6; ++it) {
    STAMP(4 + it);
    EV(0, 1, 12, true, true);
    if (it == 5) {
      if (w == 0) await(ws, FL_O + p * 2 + kh, 8);
      rW = mkrsrc((const char*)wo - 1024, D_ * D_ * 2 + 1024);
      aso = ((112 + 16 * kh) * 128 + 8 * p + 2 * w) * 1024 - 131072; wso = 16 * kh * 65536 - 65536;
    }
    OD(1, 2, 12, true, true); EV(2, 3, 12, true, true); OD(3, 4, 12, true, true); EV(4, 0, 12, true, true);
    OD(0, 1, 12, true, true); EV(1, 2, 12, true, true); OD(2, 3, 12, true, true); EV(3, 4, 12, true, true); OD(4, 0, 12, true, true);
  }
  EV(0, 1, 12, true, true); OD(1, 2, 12, true, true); EV(2, 3, 12, true, true); OD(3, 4, 12, true, true); EV(4, 0, 12, true, true); OD(0, 1, 12, true, true); EV(1, 2, 12, true, true);
  OD(2, 3, 12, true, false); EV(3, 4, 8, true, false); OD(4, 0, 4, true, false); EV(0, 1, 0, true, false); OD(1, 2, 0, false, false);
  STAMP(1);
  if (kh) {
#pragma unroll
    for (int n = 0; n < 4; ++n) { f32x4 t = acc[0][n]; acc[0][n] = acc[2][n]; acc[2][n] = t; t = acc[1][n]; acc[1][n] = acc[3][n]; acc[3][n] = t; }
#pragma unroll
    for (int n = 0; n < 4; ++n) { PIN(acc[0][n]); PIN(acc[1][n]); PIN(acc[2][n]); PIN(acc[3][n]); }
  }
  const int token0 = 128 * p + 64 * wm + 32 * kh + (l & 15);
  u32x2 xres[2][4], bres[4];
#pragma unroll
  for (int n = 0; n < 4; ++n) {
    const int n0 = 128 * nt + 64 * wn + 16 * n + 4 * g;
    bres[n] = *(const u32x2*)(b2 + n0);
#pragma unroll
    for (int m = 0; m < 2; ++m) xres[m][n] = *(const u32x2*)(x + (size_t)(token0 + 16 * m) * D_ + n0);
  }
  {
    const __amdgpu_buffer_rsrc_t rP = mkrsrc(ws + WS_PART + (size_t)gi * 32768, 32768);
#pragma unroll
    for (int m = 0; m < 2; ++m)
#pragma unroll
      for (int n = 0; n < 4; ++n) st_wt16(rP, ((w * 2 + m) * 4 + n) * 1024 + l * 16, __builtin_bit_cast(u32x4, acc[2 + m][n]));
    asm volatile("s_waitcnt vmcnt(0)\n\ts_barrier" ::: "memory");
    if (tid == 0) signal(ws, FL_X + gi);
    if (w == 0) await(ws, FL_X + partner, 1);
    asm volatile("s_barrier" ::: "memory");
  }
  STAMP(2);
  const __amdgpu_buffer_rsrc_t rPP = mkrsrc(ws + WS_PART + (size_t)partner * 32768, 32768);
#pragma unroll
  for (int m = 0; m < 2; ++m) {
    unsigned d[4][2];
#pragma unroll
    for (int n = 0; n < 4; ++n) {
      const f32x4 pv = __builtin_bit_cast(f32x4, __builtin_amdgcn_raw_buffer_load_b128(rPP, ((w * 2 + m) * 4 + n) * 1024 + l * 16, 0, 16));
      const f32x4 a4 = acc[m][n];
      d[n][0] = cvtpk(a4[0] + pv[0] + lo2f(bres[n][0]) + lo2f(xres[m][n][0]), a4[1] + pv[1] + hi2f(bres[n][0]) + hi2f(xres[m][n][0]));
      d[n][1] = cvtpk(a4[2] + pv[2] + lo2f(bres[n][1]) + lo2f(xres[m][n][1]), a4[3] + pv[3] + hi2f(bres[n][1]) + hi2f(xres[m][n][1]));
    }
    const int cch = ((g & 1) << 1) | (g >> 1);
#pragma unroll
    for (int n2 = 0; n2 < 2; ++n2) {
      const u32x4 ch = chunk_of(d[2 * n2][0], d[2 * n2][1], d[2 * n2 + 1][0], d[2 * n2 + 1][1]);
      *(u32x4*)(out + (size_t)(token0 + 16 * m) * D_ + 128 * nt + 64 * wn + 32 * n2 + 8 * cch) = ch;
    }
  }
  STAMP(3);
#undef RDX
#undef RDW
}

constexpr int LDS_BYTES = 81920;
#define MODE 0
#define PRIO_A 1
__global__ __launch_bounds__(256, 2) void k1_main(const u16* __restrict__ x, const u16* __restrict__ wq, const u16* __restrict__ wo, const u16* __restrict__ w1, const u16* __restrict__ b1, const u16* __restrict__ w2, const u16* __restrict__ b2,
                                                   u16* __restrict__ out, char* __restrict__ ws, int sel) {
  extern __shared__ __attribute__((aligned(16))) char lds_[];
  LAS char* lds = (LAS char*)lds_;
  if ((blockIdx.x >> 8) == 0) { if (PRIO_A) __builtin_amdgcn_s_setprio(PRIO_A); role_attn(blockIdx.x & 255, wq, w2, wo, ws, lds); }
  else { __builtin_amdgcn_s_setprio(PRIO_G1); role_w1(blockIdx.x & 255, w1, b1, ws, lds); role_w2(blockIdx.x & 255, w2, wo, b2, x, out, ws, lds); }
}

extern "C" void kernel_launch(void* const* d_in, const int* in_sizes, int n_in, void* d_out, int out_size, void* d_ws, size_t ws_size, hipStream_t stream) {
  const u16* x = (const u16*)d_in[0]; const u16* wq = (const u16*)d_in[1]; const u16* wk = (const u16*)d_in[2];
  const u16* wv = (const u16*)d_in[3]; const u16* wo = (const u16*)d_in[4];
  const u16* w1 = (const u16*)d_in[6]; const u16* b1 = (const u16*)d_in[7]; const u16* w2 = (const u16*)d_in[8]; const u16* b2 = (const u16*)d_in[9];
  char* ws = (char*)d_ws;
  (void)hipFuncSetAttribute((const void*)k1_main, hipFuncAttributeMaxDynamicSharedMemorySize, LDS_BYTES);
  k0_ln_kv<<<256, 512, 0, stream>>>(x, wk, wv, wq, w1, ws);
  k1_main<<<512, 256, LDS_BYTES, stream>>>(x, wq, wo, w1, b1, w2, b2, (u16*)d_out, ws, 0);
}
```

```cpp
#include <hip/hip_runtime.h>
#include <stdint.h>

typedef unsigned short u16;
typedef __bf16 bf16x8 __attribute__((ext_vector_type(8)));
typedef __bf16 bf16x2 __attribute__((ext_vector_type(2)));
typedef short s16x8 __attribute__((ext_vector_type(8)));
typedef float f32x4 __attribute__((ext_vector_type(4)));
typedef float f32x2 __attribute__((ext_vector_type(2)));
typedef unsigned u32x4 __attribute__((ext_vector_type(4)));
typedef unsigned u32x2 __attribute__((ext_vector_type(2)));
#define LAS __attribute__((address_space(3)))
#define GAS __attribute__((address_space(1)))
#define DI __device__ __forceinline__
#define SB() __builtin_amdgcn_sched_barrier(0)
#define PIN(x) asm volatile("" : "+v"(x))
#define MFMA16(a, b, c) __builtin_amdgcn_mfma_f32_16x16x32_bf16((a), (b), (c), 0, 0, 0)
#define BF(x) __builtin_bit_cast(bf16x8, x)

#define PF0ROWS 1024
constexpr int S_ = 2048, D_ = 1024, F_ = 3584;
constexpr size_t WS_FLAGS = 0;
constexpr size_t WS_R2   = 65536;
constexpr size_t WS_KV   = WS_R2 + 8192;
constexpr size_t WS_XN   = WS_KV + 262144;
constexpr size_t WS_H    = WS_XN + 4194304;
constexpr size_t WS_O    = WS_H + (size_t)112 * 128 * 1024;
constexpr size_t WS_PART = WS_O + 4194304;
constexpr size_t WS_STAMP = WS_PART + (size_t)256 * 32768 + 4194304;

constexpr float QSCALE = 0.17677669529663687f * 1.4426950408889634f;

DI float bf2f(u16 v) { return __uint_as_float(((unsigned)v) << 16); }
DI unsigned cvtpk(float lo, float hi) { f32x2 v = {lo, hi}; bf16x2 b = __builtin_convertvector(v, bf16x2); return __builtin_bit_cast(unsigned, b); }
DI float lo2f(unsigned u) { return __uint_as_float(u << 16); }
DI float hi2f(unsigned u) { return __uint_as_float(u & 0xffff0000u); }
DI __amdgpu_buffer_rsrc_t mkrsrc(const void* p, unsigned bytes) { return __builtin_amdgcn_make_buffer_rsrc((void*)p, 0, bytes, 0x00020000); }
DI void dma16(unsigned lds_addr, unsigned voff, __amdgpu_buffer_rsrc_t rsrc, unsigned soff) {
  asm volatile("s_mov_b32 m0, %0\n\ts_nop 0\n\tbuffer_load_dwordx4 %1, %2, %3 offen lds" :: "s"(lds_addr), "v"(voff), "s"(rsrc), "s"(soff));
}
template <int IMM, int INC> DI void dma2(unsigned mbase, unsigned v0, unsigned v1, __amdgpu_buffer_rsrc_t rsrc, unsigned& soff) {
  asm volatile("s_add_u32 m0, %3, %5\n\ts_add_u32 %0, %0, %6\n\tbuffer_load_dwordx4 %1, %4, %0 offen lds\n\tbuffer_load_dwordx4 %2, %4, %0 offen offset:1024 lds"
               : "+s"(soff) : "v"(v0), "v"(v1), "s"(mbase), "s"(rsrc), "i"(IMM), "i"(INC) : "scc");
}
template <int IMM, int INC> DI void dma1a(unsigned mbase, unsigned v0, __amdgpu_buffer_rsrc_t rsrc, unsigned& soff) {
  asm volatile("s_add_u32 m0, %2, %4\n\ts_add_u32 %0, %0, %5\n\tbuffer_load_dwordx4 %1, %3, %0 offen lds" : "+s"(soff) : "v"(v0), "s"(mbase), "s"(rsrc), "i"(IMM), "i"(INC) : "scc");
}
template <int OFF> DI void dma1b(unsigned v0, __amdgpu_buffer_rsrc_t rsrc, unsigned soff) {
  asm volatile("buffer_load_dwordx4 %0, %1, %2 offen offset:%3 lds" :: "v"(v0), "s"(rsrc), "s"(soff), "i"(OFF));
}
template <int IMM, int INC> DI void dma1a_c(unsigned mbase, unsigned v0, __amdgpu_buffer_rsrc_t rsrc, unsigned& soff) {
  asm volatile("s_add_u32 m0, %2, %4\n\ts_add_u32 %0, %0, %5\n\tbuffer_load_dwordx4 %1, %3, %0 offen sc1 lds" : "+s"(soff) : "v"(v0), "s"(mbase), "s"(rsrc), "i"(IMM), "i"(INC) : "scc");
}
template <int OFF> DI void dma1b_c(unsigned v0, __amdgpu_buffer_rsrc_t rsrc, unsigned soff) {
  asm volatile("buffer_load_dwordx4 %0, %1, %2 offen offset:%3 sc1 lds" :: "v"(v0), "s"(rsrc), "s"(soff), "i"(OFF));
}
DI void dma4(unsigned lds_addr, unsigned voff, __amdgpu_buffer_rsrc_t rsrc, unsigned soff) {
  asm volatile("s_mov_b32 m0, %0\n\ts_nop 0\n\tbuffer_load_dword %1, %2, %3 offen lds" :: "s"(lds_addr), "v"(voff), "s"(rsrc), "s"(soff));
}
#define WAIT_BAR(N) asm volatile("s_waitcnt vmcnt(" #N ") lgkmcnt(0)\n\ts_barrier" ::: "memory")
DI bf16x8 ldsfrag(const LAS char* p) { return *(const LAS bf16x8*)p; }
typedef short v4i16_t __attribute__((ext_vector_type(4)));
DI bf16x8 ldstr(const LAS char* p0, const LAS char* p1) {
  v4i16_t a = __builtin_amdgcn_ds_read_tr16_b64_v4i16((LAS v4i16_t*)p0);
  v4i16_t b = __builtin_amdgcn_ds_read_tr16_b64_v4i16((LAS v4i16_t*)p1);
  s16x8 v = __builtin_shufflevector(a, b, 0, 1, 2, 3, 4, 5, 6, 7);
  return __builtin_bit_cast(bf16x8, v);
}
DI void st_wt16(__amdgpu_buffer_rsrc_t r, unsigned voff, u32x4 v) { __builtin_amdgcn_raw_buffer_store_b128(v, r, voff, 0, 16); }
DI u32x4 chunk_of(unsigned x0, unsigned x1, unsigned y0, unsigned y1) {
  auto s0 = __builtin_amdgcn_permlane16_swap(x0, y0, false, false);
  auto s1 = __builtin_amdgcn_permlane16_swap(x1, y1, false, false);
  return (u32x4){s0[0], s1[0], s0[1], s1[1]};
}
DI unsigned wdma_off(int l, int kblk, int ldbytes) {
  const int row = l >> 3, c8 = l & 7, sw = ((row >> 1) & 1) | ((kblk & 1) << 1);
  return row * ldbytes + (((c8 >> 1) ^ sw) * 32) + (c8 & 1) * 16;
}
DI unsigned wrd_off(int l, int seg) {
  const int g = l >> 4, q = (l & 15) >> 2, sw = ((q >> 1) & 1) | ((g & 1) << 1);
  return q * 128 + ((seg ^ sw) * 32) + (l & 3) * 8;
}
DI float xsum4(float v) {
  auto a = __builtin_amdgcn_permlane16_swap(__float_as_uint(v), __float_as_uint(v), false, false); v = __uint_as_float(a[0]) + __uint_as_float(a[1]);
  auto b = __builtin_amdgcn_permlane32_swap(__float_as_uint(v), __float_as_uint(v), false, false); return __uint_as_float(b[0]) + __uint_as_float(b[1]);
}
DI float xmax4(float v) {
  auto a = __builtin_amdgcn_permlane16_swap(__float_as_uint(v), __float_as_uint(v), false, false); v = fmaxf(__uint_as_float(a[0]), __uint_as_float(a[1]));
  auto b = __builtin_amdgcn_permlane32_swap(__float_as_uint(v), __float_as_uint(v), false, false); return fmaxf(__uint_as_float(b[0]), __uint_as_float(b[1]));
}
#define STAMP(i)

constexpr int FL_H = 0, FL_O = 32, FL_X = 64;
DI GAS unsigned* flagp(char* ws, int i) { return (GAS unsigned*)(ws + WS_FLAGS) + 32 * i; }
DI void signal(char* ws, int i) { __hip_atomic_fetch_add(flagp(ws, i), 1u, __ATOMIC_RELAXED, __HIP_MEMORY_SCOPE_AGENT); }
DI void await(char* ws, int i, unsigned want) {
  GAS unsigned* f = flagp(ws, i);
  for (unsigned spins = 0; spins < (1u << 20); ++spins) {
    if ((unsigned)__builtin_amdgcn_readfirstlane(__hip_atomic_load(f, __ATOMIC_RELAXED, __HIP_MEMORY_SCOPE_AGENT)) >= want) break;
    __builtin_amdgcn_s_sleep(2);
  }
  asm volatile("" ::: "memory");
}

__global__ __launch_bounds__(512) void k0_ln_kv(const u16* __restrict__ x, const u16* __restrict__ wk, const u16* __restrict__ wv, const u16* __restrict__ wq, const u16* __restrict__ w1, char* __restrict__ ws) {
  __shared__ __attribute__((aligned(16))) char lds[65536 + 16384 + 2048 + 1024 + 256];
  float* part = (float*)(lds + 65536);
  float* fin = (float*)(lds + 65536 + 16384);
  float* red = (float*)(lds + 65536 + 16384 + 2048);
  const int tid = threadIdx.x, l = tid & 63, w = __builtin_amdgcn_readfirstlane(tid >> 6);
  const int rb = blockIdx.x & 127, hf = blockIdx.x >> 7;
  const int r = l & 15, c = l >> 4; const int stamp_base = 768; (void)stamp_base;
  STAMP(0);
  u32x4 xv[4];
  { const u16* xr = x + (size_t)(rb * 16 + r) * D_ + w * 128 + c * 8;
    asm volatile("global_load_dwordx4 %0, %4, off\n\tglobal_load_dwordx4 %1, %4, off offset:64\n\tglobal_load_dwordx4 %2, %4, off offset:128\n\tglobal_load_dwordx4 %3, %4, off offset:192"
                 : "=&v"(xv[0]), "=&v"(xv[1]), "=&v"(xv[2]), "=&v"(xv[3]) : "v"(xr)); }
  const unsigned lds0 = (unsigned)(uintptr_t)lds;
  {
    __amdgpu_buffer_rsrc_t rw = mkrsrc(hf ? wv : wk, 65536);
#pragma unroll
    for (int i = 0; i < 8; ++i) dma16(lds0 + w * 8192 + i * 1024, l * 16, rw, w * 8192 + i * 1024);
  }
  asm volatile("s_waitcnt vmcnt(8)" : "+v"(xv[0]), "+v"(xv[1]), "+v"(xv[2]), "+v"(xv[3]));
  {
    const int wv_ = blockIdx.x * 8 + w;
    const bool isq = wv_ < 256;
    dma4(lds0 + 65536 + 16384 + 2048 + 1024, ((isq ? wv_ : wv_ - 256) * 64 + l) * 128, mkrsrc(isq ? wq : w1, isq ? PF0ROWS * D_ * 2 : PF0ROWS * F_ * 2), 0);
  }
  if (blockIdx.x < 8) ((u32x4*)(ws + WS_FLAGS))[blockIdx.x * 512 + tid] = (u32x4){0, 0, 0, 0};
  float v[32];
#pragma unroll
  for (int i = 0; i < 4; ++i)
#pragma unroll
    for (int j = 0; j < 4; ++j) { v[i * 8 + 2 * j] = lo2f(xv[i][j]); v[i * 8 + 2 * j + 1] = hi2f(xv[i][j]); }
  float s = 0.f;
#pragma unroll
  for (int i = 0; i < 32; ++i) s += v[i];
  s = xsum4(s);
  if (c == 0) red[w * 16 + r] = s;
  __syncthreads();
  float mean = 0.f;
#pragma unroll
  for (int i = 0; i < 8; ++i) mean += red[i * 16 + r];
  mean *= (1.0f / D_);
  float q = 0.f;
#pragma unroll
  for (int i = 0; i < 32; ++i) { v[i] -= mean; q += v[i] * v[i]; }
  q = xsum4(q);
  if (c == 0) red[128 + w * 16 + r] = q;
  __syncthreads();
  float var = 0.f;
#pragma unroll
  for (int i = 0; i < 8; ++i) var += red[128 + i * 16 + r];
  var *= (1.0f / D_);
  const float rstd = 1.0f / sqrtf(var + 1e-5f);
  if (hf == 0 && w == 0 && c == 0) ((float*)(ws + WS_R2))[rb * 16 + r] = 1.0f / sqrtf(var * rstd * rstd + 1e-5f);
  u32x4 xn[4];
#pragma unroll
  for (int i = 0; i < 4; ++i)
#pragma unroll
    for (int j = 0; j < 4; ++j) xn[i][j] = cvtpk(v[i * 8 + 2 * j] * rstd, v[i * 8 + 2 * j + 1] * rstd);
#pragma unroll
  for (int i = 0; i < 2; ++i) *(u32x4*)(ws + WS_XN + ((size_t)(4 * w + 2 * i + hf) * 128 + rb) * 1024 + l * 16) = hf ? xn[2 * i + 1] : xn[2 * i];
  STAMP(1);
  asm volatile("s_waitcnt vmcnt(3)" ::: "memory");
  f32x4 acc[2] = {{0.f, 0.f, 0.f, 0.f}, {0.f, 0.f, 0.f, 0.f}};
  const LAS char* wb = (const LAS char*)lds + w * 8192 + (8 * c + ((l & 15) >> 2)) * 64 + (l & 3) * 8;
#pragma unroll
  for (int i = 0; i < 4; ++i)
#pragma unroll
    for (int b = 0; b < 2; ++b) {
      bf16x8 wf = ldstr(wb + i * 2048 + b * 32, wb + i * 2048 + b * 32 + 256);
      acc[b] = MFMA16(BF(xn[i]), wf, acc[b]);
    }
#pragma unroll
  for (int b = 0; b < 2; ++b)
#pragma unroll
    for (int j = 0; j < 4; ++j) part[(w * 16 + 4 * c + j) * 32 + 16 * b + r] = acc[b][j];
  __syncthreads();
  { float t = 0.f;
#pragma unroll
    for (int i = 0; i < 8; ++i) t += part[i * 512 + tid];
    fin[tid] = t; }
  __syncthreads();
  if (tid < 64) {
    float o[8];
    char* dst = ws + WS_KV + (size_t)(rb >> 1) * 4096;
    const int half = rb & 1;
    if (hf == 0) { const int kb = tid >> 5, g = (tid >> 3) & 3, rr = tid & 7;
      const int row = 8 * half + rr, key = 8 * (row >> 2) + 4 * kb + (row & 3) - 16 * half;
#pragma unroll
      for (int j = 0; j < 8; ++j) o[j] = fin[key * 32 + 16 * (j >> 2) + 4 * g + (j & 3)];
      dst += kb * 1024 + (g * 16 + row) * 16;
    } else { const int db = tid >> 5, gl = (tid >> 4) & 1, rr = tid & 15;
#pragma unroll
      for (int j = 0; j < 8; ++j) o[j] = fin[(8 * gl + j) * 32 + 16 * db + rr];
      dst += 2048 + db * 1024 + ((2 * half + gl) * 16 + rr) * 16;
    }
    *(u32x4*)dst = (u32x4){cvtpk(o[0], o[1]), cvtpk(o[2], o[3]), cvtpk(o[4], o[5]), cvtpk(o[6], o[7])};
  }
  STAMP(2);
}

#define MERGED 1
#define PRIO_A 1
#define G1PAT 0
#define G2PAT 0
#define PRIO_Q 1
#define PRIO_G1 0
#define PRIO_GE 2
#define PRIO_G2 0
#define LSUM_MFMA 1
DI float fadd_s(float a, float b) { float r; asm("s_nop 0\n\tv_add_f32_e32 %0, %1, %2" : "=v"(r) : "v"(a), "v"(b)); return r; }

DI void attn_slow(const char* __restrict__ kv, bf16x8 qf, f32x4& o0, f32x4& o1) {
  const int l = threadIdx.x & 63;
  o0 = (f32x4){0.f, 0.f, 0.f, 0.f}; o1 = o0;
  float m = -3.0e38f, ls = 0.f;
#pragma unroll 1
  for (int t = 0; t < 64; ++t) {
    const char* p = kv + t * 4096 + l * 16;
    const bf16x8 k0 = *(const bf16x8*)p, k1 = *(const bf16x8*)(p + 1024), v0 = *(const bf16x8*)(p + 2048), v1 = *(const bf16x8*)(p + 3072);
    const f32x4 z = {0.f, 0.f, 0.f, 0.f};
    f32x4 s0 = MFMA16(k0, qf, z), s1 = MFMA16(k1, qf, z);
    float mx = fmaxf(fmaxf(fmaxf(s0[0], s0[1]), fmaxf(s0[2], s0[3])), fmaxf(fmaxf(s1[0], s1[1]), fmaxf(s1[2], s1[3])));
    mx = fmaxf(mx, __shfl_xor(mx, 16)); mx = fmaxf(mx, __shfl_xor(mx, 32));
    const float mn = fmaxf(m, mx), al = __builtin_amdgcn_exp2f(m - mn);
    m = mn; float rs = 0.f;
#pragma unroll
    for (int i = 0; i < 4; ++i) { s0[i] = __builtin_amdgcn_exp2f(s0[i] - mn); s1[i] = __builtin_amdgcn_exp2f(s1[i] - mn); rs += s0[i] + s1[i]; o0[i] *= al; o1[i] *= al; }
    rs += __shfl_xor(rs, 16); rs += __shfl_xor(rs, 32); ls = ls * al + rs;
    const u32x4 pp = {cvtpk(s0[0], s0[1]), cvtpk(s0[2], s0[3]), cvtpk(s1[0], s1[1]), cvtpk(s1[2], s1[3])};
    o0 = MFMA16(v0, BF(pp), o0); o1 = MFMA16(v1, BF(pp), o1);
  }
#pragma unroll
  for (int i = 0; i < 4; ++i) { o0[i] /= ls; o1[i] /= ls; }
}

DI void role_attn(int a, const u16* __restrict__ wq, const u16* __restrict__ w2, const u16* __restrict__ wo, char* __restrict__ ws, LAS char* lds) {
  const int tid = threadIdx.x, l = tid & 63, w = __builtin_amdgcn_readfirstlane(tid >> 6);
  const int xcd = a & 7, idx = a >> 3, tb = (xcd >> 2) * 16 + (idx & 15), hg = (xcd & 3) * 2 + (idx >> 4);
  const int g = l >> 4; const int stamp_base = 0; (void)stamp_base;
  const unsigned lds0 = (unsigned)(uintptr_t)lds;
  constexpr int SLOT = 24576;
  const __amdgpu_buffer_rsrc_t rX = mkrsrc(ws + WS_XN, 4194304), rKV = mkrsrc(ws + WS_KV, 262144);
  const unsigned vlin = l * 16;
  constexpr int QS = 12288;
  const __amdgpu_buffer_rsrc_t rWq = mkrsrc((const char*)wq - 1024, D_ * D_ * 2 + 1024);
  const unsigned vq0 = wdma_off(l, w, 2048) + hg * 256 + w * 8 * 2048 + 1024, vq1 = vq0 + 128 - 1024;
  unsigned xso = (4 * tb + w) * 1024 - 131072, qso = 0u - 65536;
  const unsigned mx = lds0 + w * 1024, mq = lds0 + 4096 + w * 2048;
#define PCQ(SL, i) do { if constexpr ((i) == 0) dma1a<(SL) * QS, 131072>(mx, vlin, rX, xso); else if constexpr ((i) == 1) dma1a<(SL) * QS, 65536>(mq, vq0, rWq, qso); else dma1b<1024>(vq1, rWq, qso); } while (0)
#define STGQ(SL) do { PCQ(SL, 0); PCQ(SL, 1); PCQ(SL, 2); } while (0)
  STAMP(0);
  STGQ(0); STGQ(1); STGQ(2); STGQ(3); STGQ(4); STGQ(5);
  f32x4 qacc[2][4];
#pragma unroll
  for (int i = 0; i < 2; ++i)
#pragma unroll
    for (int j = 0; j < 4; ++j) qacc[i][j] = (f32x4){0.f, 0.f, 0.f, 0.f};
  const LAS char* xrd = lds + l * 16;
  const LAS char* wrd0 = lds + 4096 + (g * 2 + (w >> 1)) * 1024 + wrd_off(l, 2 * (w & 1));
  const LAS char* wrd1 = lds + 4096 + (g * 2 + (w >> 1)) * 1024 + wrd_off(l, 2 * (w & 1) + 1);
  bf16x8 qxa[4], qwa[2], qxb[4], qwb[2];
#define QRX(X, m, SL) X[m] = ldsfrag(xrd + (SL) * QS + (m) * 1024)
#define QRW(W, n, SL) W[n] = ((n) == 0 ? ldstr(wrd0 + (SL) * QS, wrd0 + (SL) * QS + 512) : ldstr(wrd1 + (SL) * QS, wrd1 + (SL) * QS + 512))
#define QM(X, W, m, n) qacc[n][m] = MFMA16(W[n], X[m], qacc[n][m])
#define STEPQ(X, W, XN, WN, RD, RS, DMA, DS) do { \
    QM(X, W, 0, 0); if (RD) { QRW(WN, 0, RS); QRX(XN, 0, RS); } SB(); QM(X, W, 1, 0); if (RD) { QRW(WN, 1, RS); QRX(XN, 1, RS); } SB(); QM(X, W, 2, 0); if (RD) QRX(XN, 2, RS); SB(); QM(X, W, 3, 0); if (RD) QRX(XN, 3, RS); SB(); \
    QM(X, W, 0, 1); if (DMA) PCQ(DS, 0); SB(); QM(X, W, 1, 1); if (DMA) PCQ(DS, 1); SB(); QM(X, W, 2, 1); if (DMA) PCQ(DS, 2); SB(); QM(X, W, 3, 1); SB(); } while (0)
  WAIT_BAR(15);
#pragma unroll
  for (int m = 0; m < 4; ++m) QRX(qxa, m, 0);
  QRW(qwa, 0, 0); QRW(qwa, 1, 0);
#define QEV(SC, SN, W_, RD, DMA) do { WAIT_BAR(W_); SB(); STEPQ(qxa, qwa, qxb, qwb, RD, SN, DMA, SC); } while (0)
#define QOD(SC, SN, W_, RD, DMA) do { WAIT_BAR(W_); SB(); STEPQ(qxb, qwb, qxa, qwa, RD, SN, DMA, SC); } while (0)
  QEV(0, 1, 12, true, true); QOD(1, 2, 12, true, true); QEV(2, 3, 12, true, true); QOD(3, 4, 12, true, true); QEV(4, 5, 12, true, true); QOD(5, 0, 12, true, true);
  for (int it = 0; it < 3; ++it) { QEV(0, 1, 12, true, true); QOD(1, 2, 12, true, true); QEV(2, 3, 12, true, true); QOD(3, 4, 12, true, true); QEV(4, 5, 12, true, true); QOD(5, 0, 12, true, true); }
  QEV(0, 1, 12, true, true); QOD(1, 2, 12, true, true);
  QEV(2, 3, 12, true, false); QOD(3, 4, 9, true, false); QEV(4, 5, 6, true, false); QOD(5, 0, 3, true, false); QEV(0, 1, 0, true, false);
  unsigned kvso = w * 4096 - 16384;
  const unsigned kvm = lds0 + w * 4096;
#define STGKV(SF) do { dma1a<(SF) * SLOT, 16384>(kvm, vlin, rKV, kvso); dma1b<1024>(vlin, rKV, kvso); dma1b<2048>(vlin, rKV, kvso); dma1b<3072>(vlin, rKV, kvso); } while (0)
  STGKV(1); STGKV(2);
  {
    const unsigned line = (a * 4 + w) * 64 + l;
    dma4(lds0 + 6 * QS, line * 128, mkrsrc(w2, F_ * D_ * 2), 0);
    dma4(lds0 + 6 * QS, line * 128, mkrsrc(wo, D_ * D_ * 2), 0);
  }
  STEPQ(qxb, qwb, qxa, qwa, false, 0, false, 0);
  STAMP(1);
  __builtin_amdgcn_s_setprio(PRIO_A);
  bf16x8 qf[4];
#pragma unroll
  for (int m = 0; m < 4; ++m)
    qf[m] = BF(((u32x4){cvtpk(qacc[0][m][0] * QSCALE, qacc[0][m][1] * QSCALE), cvtpk(qacc[0][m][2] * QSCALE, qacc[0][m][3] * QSCALE),
                        cvtpk(qacc[1][m][0] * QSCALE, qacc[1][m][1] * QSCALE), cvtpk(qacc[1][m][2] * QSCALE, qacc[1][m][3] * QSCALE)}));
  WAIT_BAR(6);
  const LAS char* kvb = lds + l * 16;
  f32x4 negm[4], S[4][2], O[4][2];
  bf16x8 k0, k1;
  {
    const LAS char* p = kvb + 1 * SLOT;
    k0 = ldsfrag(p); k1 = ldsfrag(p + 1024);
    const f32x4 z = {0.f, 0.f, 0.f, 0.f};
#pragma unroll
    for (int m = 0; m < 4; ++m) {
      const f32x4 s0 = MFMA16(k0, qf[m], z), s1 = MFMA16(k1, qf[m], z);
      float mx = fmaxf(fmaxf(fmaxf(s0[0], s0[1]), fmaxf(s0[2], s0[3])), fmaxf(fmaxf(s1[0], s1[1]), fmaxf(s1[2], s1[3])));
      mx = xmax4(mx);
      negm[m] = (f32x4){-mx, -mx, -mx, -mx}; PIN(negm[m]);
      O[m][0] = z; O[m][1] = z;
    }
    S[0][0] = MFMA16(k0, qf[0], negm[0]); S[0][1] = MFMA16(k1, qf[0], negm[0]);
    S[1][0] = MFMA16(k0, qf[1], negm[1]); S[1][1] = MFMA16(k1, qf[1], negm[1]);
  }
  u32x4 P[4];
  P[2] = (u32x4){0, 0, 0, 0}; P[3] = P[2];
  bf16x8 va0 = BF(P[2]), va1 = va0, vb0, vb1;
  S[3][1] = (f32x4){0.f, 0.f, 0.f, 0.f};
  f32x4 L[4];
#pragma unroll
  for (int m = 0; m < 4; ++m) L[m] = (f32x4){0.f, 0.f, 0.f, 0.f};
  bf16x8 ones; { const short o1 = (l & 15) == 0 ? (short)0x3f80 : (short)0; s16x8 ov = {o1, o1, o1, o1, o1, o1, o1, o1}; ones = __builtin_bit_cast(bf16x8, ov); PIN(ones); }
#define LS(m) do { L[m] = MFMA16(ones, BF(P[m]), L[m]); PIN(L[m]); SB(); } while (0)
#define LADD2(m, kb, i)
#define NOLS(m)
#define EXPF(x) __builtin_amdgcn_exp2f(x)
#define EX2(m, kb, i) do { float t0_ = EXPF(S[m][kb][i]), t1_ = EXPF(S[m][kb][i + 1]); PIN(t0_); PIN(t1_); S[m][kb][i] = t0_; S[m][kb][i + 1] = t1_; SB(); } while (0)
#define CVA(m, kb) do { unsigned c_ = cvtpk(S[m][kb][0], S[m][kb][1]); PIN(c_); P[m][2 * kb] = c_; LADD2(m, kb, 0); SB(); } while (0)
#define CVB(m, kb) do { unsigned c_ = cvtpk(S[m][kb][2], S[m][kb][3]); PIN(c_); P[m][2 * kb + 1] = c_; LADD2(m, kb, 2); SB(); } while (0)
#define GRP(mq, kq, KF, pm, pk, me, ke, mp, db, VF, LSM, X) do { \
    S[mq][kq] = MFMA16(KF, qf[mq], negm[mq]); PIN(S[mq][kq]); SB(); X; CVB(pm, pk); EX2(me, ke, 0); \
    O[mp][db] = MFMA16(VF, BF(P[mp]), O[mp][db]); PIN(O[mp][db]); SB(); EX2(me, ke, 2); LSM(mp); CVA(me, ke); } while (0)
#define TILE(KC0, KC1, KN0, KN1, VP0, VP1, VC0, VC1, kn, vp, D) do { \
    GRP(2, 0, KC0, 3, 1, 0, 0, 2, 0, VP0, NOLS, KN0 = ldsfrag(kn); SB()); \
    GRP(2, 1, KC1, 0, 0, 0, 1, 2, 1, VP1, LS,   KN1 = ldsfrag((kn) + 1024); SB()); \
    GRP(3, 0, KC0, 0, 1, 1, 0, 3, 0, VP0, NOLS, VC0 = ldsfrag(vp); SB()); \
    GRP(3, 1, KC1, 1, 0, 1, 1, 3, 1, VP1, LS,   VC1 = ldsfrag((vp) + 1024); SB()); \
    GRP(0, 0, KN0, 1, 1, 2, 0, 0, 0, VC0, NOLS, D(0)); \
    GRP(0, 1, KN1, 2, 0, 2, 1, 0, 1, VC1, LS,   D(1)); \
    GRP(1, 0, KN0, 2, 1, 3, 0, 1, 0, VC0, NOLS, D(2)); \
    GRP(1, 1, KN1, 3, 0, 3, 1, 1, 1, VC1, LS,   D(3)); \
  } while (0)
#define NOD(i)
  bf16x8 kx0, kx1;
#define DKV0(SF) dma1a<(SF) * SLOT, 16384>(kvm, vlin, rKV, kvso); SB()
#define STAGE(SC, SN, SF, DM, LAST) do { \
    TILE(k0, k1, kx0, kx1, va0, va1, vb0, vb1, kvb + (SC) * SLOT + 4096, kvb + (SC) * SLOT + 2048, NOD); \
    TILE(kx0, kx1, k0, k1, vb0, vb1, va0, va1, kvb + (SC) * SLOT + 8192, kvb + (SC) * SLOT + 4096 + 2048, NOD); \
    TILE(k0, k1, kx0, kx1, va0, va1, vb0, vb1, kvb + (SC) * SLOT + 12288, kvb + (SC) * SLOT + 8192 + 2048, NOD); \
    WAIT_BAR(0);        \
    TILE(kx0, kx1, k0, k1, vb0, vb1, va0, va1, kvb + ((LAST) ? (SC) : (SN)) * SLOT, kvb + (SC) * SLOT + 12288 + 2048, DM); } while (0)
#define DM_(SF, i) do { if constexpr ((i) == 0) { dma1a<(SF) * SLOT, 16384>(kvm, vlin, rKV, kvso); } else { dma1b<(i) * 1024>(vlin, rKV, kvso); } SB(); } while (0)
#define DM0(i) DM_(0, i)
#define DM1(i) DM_(1, i)
#define DM2(i) DM_(2, i)
  for (int j = 0; j < 12; j += 3) { STAGE(1, 2, 0, DM0, false); STAGE(2, 0, 1, DM1, false); STAGE(0, 1, 2, DM2, false); STAMP(4 + j / 3); }
  STAGE(1, 2, 0, DM0, false); STAGE(2, 0, 1, DM1, false); STAGE(0, 1, 2, NOD, false); STAGE(1, 2, 0, NOD, true);
  CVB(3, 1);
  O[2][0] = MFMA16(va0, BF(P[2]), O[2][0]); O[2][1] = MFMA16(va1, BF(P[2]), O[2][1]); LS(2);
  O[3][0] = MFMA16(va0, BF(P[3]), O[3][0]); O[3][1] = MFMA16(va1, BF(P[3]), O[3][1]); LS(3);
  STAMP(2);
  const __amdgpu_buffer_rsrc_t rO = mkrsrc(ws + WS_O, 4194304);
#pragma unroll
  for (int m = 0; m < 4; ++m) {
    float ls = __shfl(L[m][0], l & 15);
    float inv = 1.0f / ls;
    f32x4 o0 = O[m][0], o1 = O[m][1];
    const float mo = fmaxf(fmaxf(fmaxf(fabsf(o0[0]), fabsf(o0[1])), fmaxf(fabsf(o0[2]), fabsf(o0[3]))), fmaxf(fmaxf(fabsf(o1[0]), fabsf(o1[1])), fmaxf(fabsf(o1[2]), fabsf(o1[3]))));
    const bool ok = (ls <= 1.0e18f) && (mo <= 1.0e37f);
    if (__builtin_expect(__any(!ok), 0)) { attn_slow(ws + WS_KV, qf[m], o0, o1); inv = 1.0f; }
    const u32x4 ch = chunk_of(cvtpk(o0[0] * inv, o0[1] * inv), cvtpk(o0[2] * inv, o0[3] * inv), cvtpk(o1[0] * inv, o1[1] * inv), cvtpk(o1[2] * inv, o1[3] * inv));
    const int c = ((g & 1) << 1) | (g >> 1);
    st_wt16(rO, ((4 * hg + w) * 128 + 4 * tb + m) * 1024 + c * 256 + (l & 15) * 16, ch);
  }
  asm volatile("s_waitcnt vmcnt(0)\n\ts_barrier" ::: "memory");
  if (tid == 0) signal(ws, FL_O + (tb >> 1) * 2 + (hg >> 2));
  STAMP(3);
}

DI f32x4 gelu4(f32x4 acc, float rs, f32x4 b, float c1v) {
  constexpr float c2 = -2.0f * 0.7978845608028654f * 1.4426950408889634f * 0.044715f;
  float x0 = acc[0], x1 = acc[1], x2 = acc[2], x3 = acc[3], t0, t1, t2, t3;
  asm("v_fma_f32 %0, %0, %8, %9\n\tv_fma_f32 %1, %1, %8, %10\n\tv_fma_f32 %2, %2, %8, %11\n\tv_fma_f32 %3, %3, %8, %12\n\t"
      "v_mul_f32 %4, %0, %0\n\tv_mul_f32 %5, %1, %1\n\tv_mul_f32 %6, %2, %2\n\tv_mul_f32 %7, %3, %3\n\t"
      "v_fma_f32 %4, %4, %14, %13\n\tv_fma_f32 %5, %5, %14, %13\n\tv_fma_f32 %6, %6, %14, %13\n\tv_fma_f32 %7, %7, %14, %13\n\t"
      "v_mul_f32 %4, %0, %4\n\tv_mul_f32 %5, %1, %5\n\tv_mul_f32 %6, %2, %6\n\tv_mul_f32 %7, %3, %7\n\t"
      "v_exp_f32 %4, %4\n\tv_exp_f32 %5, %5\n\tv_exp_f32 %6, %6\n\tv_exp_f32 %7, %7\n\t"
      "v_add_f32 %4, 1.0, %4\n\tv_add_f32 %5, 1.0, %5\n\tv_add_f32 %6, 1.0, %6\n\tv_add_f32 %7, 1.0, %7\n\t"
      "v_rcp_f32 %4, %4\n\tv_rcp_f32 %5, %5\n\tv_rcp_f32 %6, %6\n\tv_rcp_f32 %7, %7\n\t"
      "v_mul_f32 %0, %0, %4\n\tv_mul_f32 %1, %1, %5\n\tv_mul_f32 %2, %2, %6\n\tv_mul_f32 %3, %3, %7"
      : "+v"(x0), "+v"(x1), "+v"(x2), "+v"(x3), "=&v"(t0), "=&v"(t1), "=&v"(t2), "=&v"(t3)
      : "v"(rs), "v"(b[0]), "v"(b[1]), "v"(b[2]), "v"(b[3]), "v"(c1v), "s"(c2));
  return (f32x4){x0, x1, x2, x3};
}

DI void role_w1(int gi, const u16* __restrict__ w1, const u16* __restrict__ b1, char* __restrict__ ws, LAS char* lds) {
  const int tid = threadIdx.x, l = tid & 63, w = __builtin_amdgcn_readfirstlane(tid >> 6);
  const int xcd = gi & 7, idx = gi >> 3, p = (xcd >> 2) * 8 + (idx & 7), j = (xcd & 3) * 4 + (idx >> 3);
  const int g = l >> 4, wm = w >> 1, wn = w & 1; const int stamp_base = 256; (void)stamp_base;
  const unsigned lds0 = (unsigned)(uintptr_t)lds;
  constexpr int SLOT = 24576;
  const __amdgpu_buffer_rsrc_t rX = mkrsrc(ws + WS_XN, 4194304), rW = mkrsrc((const char*)w1 - 4096, D_ * F_ * 2 + 4096);
  const unsigned vlin = l * 16;
  unsigned vw[4];
#pragma unroll
  for (int i = 0; i < 4; ++i) vw[i] = wdma_off(l, w, F_ * 2) + 8 * w * (F_ * 2) + 448 * j - 64 * (j & 1) + 128 * i + 4096 - 1024 * i;
  STAMP(0);
  unsigned xso = (8 * p + 2 * w) * 1024 - 131072, wso = 0u - 32 * F_ * 2;
  const unsigned mx = lds0 + w * 2048, mw = lds0 + 8192 + w * 4096;
#define PC1(SL, i) do { if constexpr ((i) == 0) dma1a<(SL) * SLOT, 131072>(mx, vlin, rX, xso); else if constexpr ((i) == 1) dma1b<1024>(vlin, rX, xso); \
    else if constexpr ((i) == 2) dma1a<(SL) * SLOT, 32 * F_ * 2>(mw, vw[0], rW, wso); else dma1b<((i) - 2) * 1024>(vw[(i) - 2], rW, wso); } while (0)
#define STG1(SL) do { PC1(SL, 0); PC1(SL, 1); PC1(SL, 2); PC1(SL, 3); PC1(SL, 4); PC1(SL, 5); } while (0)
  STG1(0); STG1(1); STG1(2);
  constexpr int L_R2 = 3 * SLOT, L_B1 = L_R2 + 512;
  if (w < 2) dma4(lds0 + L_R2 + w * 256, (128 * p + 64 * w + l) * 4, mkrsrc(ws + WS_R2, 8192), 0);
  else dma4(lds0 + L_B1 + (w - 2) * 256, 448 * j + ((w - 2) * 64 + l) * 4, mkrsrc(b1, F_ * 2), 0);
  f32x4 acc[4][7];
#pragma unroll
  for (int m = 0; m < 4; ++m)
#pragma unroll
    for (int n = 0; n < 7; ++n) acc[m][n] = (f32x4){0.f, 0.f, 0.f, 0.f};
  const LAS char* xrd = lds + 4 * wm * 1024 + l * 16;
  const LAS char* wrb[5];
  { const int off = 6 * wn + 2 * (j & 1), nl = 12 + wn + 2 * (j & 1);
#pragma unroll
    for (int i = 0; i < 4; ++i) wrb[i] = lds + 8192 + (g * 4 + ((i + off) >> 2)) * 1024 + wrd_off(l, (i + off) & 3);
    wrb[4] = lds + 8192 + (g * 4 + (nl >> 2)) * 1024 + wrd_off(l, nl & 3); }
  bf16x8 xa[4], wa[7], xb[4], wb[7];
#define RDX(X, m, so) X[m] = ldsfrag(xrd + (so) + (m) * 1024)
#define RDW(W, n, so) W[n] = ((n) < 6 ? ldstr(wrb[(n) & 3] + (so) + ((n) >> 2) * 1024, wrb[(n) & 3] + (so) + ((n) >> 2) * 1024 + 512) : ldstr(wrb[4] + (so), wrb[4] + (so) + 512))
#define M1(X, W, m, n) acc[m][n] = MFMA16(W[n], X[m], acc[m][n])
#define STEP1(X, W, XN, WN, RD, RS, DMA, DS) do { \
    M1(X, W, 0, 0); if (RD) RDW(WN, 0, (RS) * SLOT); SB(); M1(X, W, 1, 0); if (RD) RDX(XN, 0, (RS) * SLOT); SB(); M1(X, W, 2, 0); if (RD) RDW(WN, 1, (RS) * SLOT); SB(); M1(X, W, 3, 0); if (DMA) PC1(DS, 0); SB(); \
    M1(X, W, 0, 1); if (RD) RDX(XN, 1, (RS) * SLOT); SB(); M1(X, W, 1, 1); if (RD) RDW(WN, 2, (RS) * SLOT); SB(); M1(X, W, 2, 1); if (RD) RDX(XN, 2, (RS) * SLOT); SB(); M1(X, W, 3, 1); if (DMA) PC1(DS, 1); SB(); \
    M1(X, W, 0, 2); if (RD) RDW(WN, 3, (RS) * SLOT); SB(); M1(X, W, 1, 2); if (RD) RDX(XN, 3, (RS) * SLOT); SB(); M1(X, W, 2, 2); if (RD) RDW(WN, 4, (RS) * SLOT); SB(); M1(X, W, 3, 2); if (DMA) PC1(DS, 2); SB(); \
    M1(X, W, 0, 3); if (RD) RDW(WN, 5, (RS) * SLOT); SB(); M1(X, W, 1, 3); SB(); M1(X, W, 2, 3); if (RD) RDW(WN, 6, (RS) * SLOT); SB(); M1(X, W, 3, 3); if (DMA) PC1(DS, 3); SB(); \
    M1(X, W, 0, 4); SB(); M1(X, W, 1, 4); SB(); M1(X, W, 2, 4); SB(); M1(X, W, 3, 4); if (DMA) PC1(DS, 4); SB(); \
    M1(X, W, 0, 5); SB(); M1(X, W, 1, 5); SB(); M1(X, W, 2, 5); SB(); M1(X, W, 3, 5); if (DMA) PC1(DS, 5); SB(); \
    M1(X, W, 0, 6); SB(); M1(X, W, 1, 6); SB(); M1(X, W, 2, 6); SB(); M1(X, W, 3, 6); SB(); } while (0)
  WAIT_BAR(13);
#pragma unroll
  for (int m = 0; m < 4; ++m) RDX(xa, m, 0);
#pragma unroll
  for (int n = 0; n < 7; ++n) RDW(wa, n, 0);
#define EV(SC, SN, W_, RD, DMA) do { WAIT_BAR(W_); SB(); STEP1(xa, wa, xb, wb, RD, SN, DMA, SC); } while (0)
#define OD(SC, SN, W_, RD, DMA) do { WAIT_BAR(W_); SB(); STEP1(xb, wb, xa, wa, RD, SN, DMA, SC); } while (0)
#define PR1(i) do { if constexpr ((i) == 0 || (((G1PAT >> (i)) & 1) != ((G1PAT >> ((i) - 1 < 0 ? 0 : (i) - 1)) & 1))) __builtin_amdgcn_s_setprio(((G1PAT >> (i)) & 1) ? 2 : 0); } while (0)
#define SIX() do { PR1(0); EV(0, 1, 6, true, true); PR1(1); OD(1, 2, 6, true, true); PR1(2); EV(2, 0, 6, true, true); PR1(3); OD(0, 1, 6, true, true); PR1(4); EV(1, 2, 6, true, true); PR1(5); OD(2, 0, 6, true, true); } while (0)
  for (int it = 0; it < 4; ++it) { SIX(); STAMP(4 + it); }
  EV(0, 1, 6, true, true); OD(1, 2, 6, true, true); EV(2, 0, 6, true, true); OD(0, 1, 6, true, true); EV(1, 2, 6, true, true);
  OD(2, 0, 6, true, false); EV(0, 1, 0, true, false); OD(1, 2, 0, false, false);
  STAMP(1);
  __builtin_amdgcn_s_setprio(PRIO_GE);
  float r2[4];
#pragma unroll
  for (int m = 0; m < 4; ++m) r2[m] = *(const LAS float*)(lds + L_R2 + (64 * wm + 16 * m + (l & 15)) * 4);
  const __amdgpu_buffer_rsrc_t rH = mkrsrc(ws + WS_H, 112 * 128 * 1024);
  const int cch = ((g & 1) << 1) | (g >> 1);
  unsigned last[4][2];
  float c1v = -2.0f * 0.7978845608028654f * 1.4426950408889634f; PIN(c1v);
#pragma unroll
  for (int n = 0; n < 7; ++n) {
    const int nb = n < 6 ? 6 * wn + n : 12 + wn;
    const u32x2 braw = *(const LAS u32x2*)(lds + L_B1 + (16 * nb + 4 * g) * 2);
    const f32x4 bb = {lo2f(braw[0]), hi2f(braw[0]), lo2f(braw[1]), hi2f(braw[1])};
#pragma unroll
    for (int m = 0; m < 4; ++m) acc[m][n] = gelu4(acc[m][n], r2[m], bb, c1v);
    if (n < 6) { if (n & 1) {
#pragma unroll
        for (int m = 0; m < 4; ++m) {
          const u32x4 ch = chunk_of(cvtpk(acc[m][n - 1][0], acc[m][n - 1][1]), cvtpk(acc[m][n - 1][2], acc[m][n - 1][3]), cvtpk(acc[m][n][0], acc[m][n][1]), cvtpk(acc[m][n][2], acc[m][n][3]));
          st_wt16(rH, ((7 * j + 3 * wn + (n >> 1)) * 128 + 8 * p + 4 * wm + m) * 1024 + cch * 256 + (l & 15) * 16, ch);
        } }
    } else {
#pragma unroll
      for (int m = 0; m < 4; ++m) { last[m][0] = cvtpk(acc[m][n][0], acc[m][n][1]); last[m][1] = cvtpk(acc[m][n][2], acc[m][n][3]); }
    }
  }
#pragma unroll
  for (int m = 0; m < 4; m += 2) {
    const u32x4 ch = chunk_of(last[m][0], last[m][1], last[m + 1][0], last[m + 1][1]);
    st_wt16(rH, ((7 * j + 6) * 128 + 8 * p + 4 * wm + m + (g & 1)) * 1024 + (2 * wn + (g >> 1)) * 256 + (l & 15) * 16, ch);
  }
  STAMP(2);
  __builtin_amdgcn_s_setprio(PRIO_G2);
  asm volatile("s_waitcnt vmcnt(0)\n\ts_barrier" ::: "memory");
  if (tid == 0) signal(ws, FL_H + p * 2 + (j >> 3));
  STAMP(3);
#undef RDX
#undef RDW
#undef EV
#undef OD
}

DI void role_w2(int gi, const u16* __restrict__ w2, const u16* __restrict__ wo, const u16* __restrict__ b2, const u16* __restrict__ x, u16* __restrict__ out, char* __restrict__ ws, LAS char* lds) {
  int tid = threadIdx.x; asm volatile("" : "+v"(tid));
  const int l = tid & 63, w = __builtin_amdgcn_readfirstlane(tid >> 6);
  const int xcd = gi & 7, idx = gi >> 3, kh = xcd & 1, p = (xcd >> 1) * 4 + (idx & 3), nt = idx >> 2;
  const int partner = gi ^ 1;
  const int g = l >> 4, wm = w >> 1, wn = w & 1; const int stamp_base = 512; (void)stamp_base;
  const unsigned lds0 = (unsigned)(uintptr_t)lds;
  constexpr int SLOT = 16384;
#define AINC_ 131072
#define WINC_ 65536
  const __amdgpu_buffer_rsrc_t rA = mkrsrc(ws + WS_H, 144 * 128 * 1024);
  __amdgpu_buffer_rsrc_t rW = mkrsrc((const char*)w2 - 1024, F_ * D_ * 2 + 1024);
  const unsigned vlin = l * 16;
  const unsigned vw0 = wdma_off(l, w, 2048) + nt * 256 + w * 8 * 2048 + 1024, vw1 = vw0 + 128 - 1024;
  if (w == 0) await(ws, FL_H + p * 2 + kh, 8);
  asm volatile("s_barrier" ::: "memory");
  STAMP(0);
  unsigned aso = (56 * kh * 128 + 8 * p + 2 * w) * 1024 - AINC_, wso = 56 * kh * 65536 - WINC_;
  const unsigned ma = lds0 + w * 2048, mw = lds0 + 8192 + w * 2048;
#define AINC 131072
#define WINC 65536
#define PC2(SL, i) do { if constexpr ((i) == 0) dma1a_c<(SL) * SLOT, AINC>(ma, vlin, rA, aso); else if constexpr ((i) == 1) dma1b_c<1024>(vlin, rA, aso); \
    else if constexpr ((i) == 2) dma1a<(SL) * SLOT, WINC>(mw, vw0, rW, wso); else dma1b<1024>(vw1, rW, wso); } while (0)
#define STG2(SL) do { PC2(SL, 0); PC2(SL, 1); PC2(SL, 2); PC2(SL, 3); } while (0)
  STG2(0); STG2(1); STG2(2); STG2(3); STG2(4);
  f32x4 acc[4][4];
#pragma unroll
  for (int m = 0; m < 4; ++m)
#pragma unroll
    for (int n = 0; n < 4; ++n) acc[m][n] = (f32x4){0.f, 0.f, 0.f, 0.f};
  const LAS char* xrd = lds + 4 * wm * 1024 + l * 16;
  const LAS char* wrb[4];
#pragma unroll
  for (int i = 0; i < 4; ++i) wrb[i] = lds + 8192 + (g * 2 + wn) * 1024 + wrd_off(l, i);
  bf16x8 xa[4], wa[4], xb[4], wb[4];
#define RDX(X, m, so) X[m] = ldsfrag(xrd + (so) + (m) * 1024)
#define RDW(W, n, so) W[n] = ldstr(wrb[n] + (so), wrb[n] + (so) + 512)
#define M2(X, W, m, n) acc[m][n] = MFMA16(W[n], X[m], acc[m][n])
#define STEP2(X, W, XN, WN, RD, RS, DMA, DS) do { \
    M2(X, W, 0, 0); if (RD) RDW(WN, 0, (RS) * SLOT); SB(); M2(X, W, 1, 0); if (RD) RDX(XN, 0, (RS) * SLOT); SB(); M2(X, W, 2, 0); if (RD) RDX(XN, 1, (RS) * SLOT); SB(); M2(X, W, 3, 0); if (DMA) PC2(DS, 0); SB(); \
    M2(X, W, 0, 1); if (RD) RDW(WN, 1, (RS) * SLOT); SB(); M2(X, W, 1, 1); if (RD) RDX(XN, 2, (RS) * SLOT); SB(); M2(X, W, 2, 1); if (RD) RDX(XN, 3, (RS) * SLOT); SB(); M2(X, W, 3, 1); if (DMA) PC2(DS, 1); SB(); \
    M2(X, W, 0, 2); if (RD) RDW(WN, 2, (RS) * SLOT); SB(); M2(X, W, 1, 2); if (RD) RDW(WN, 3, (RS) * SLOT); SB(); M2(X, W, 2, 2); SB(); M2(X, W, 3, 2); if (DMA) PC2(DS, 2); SB(); \
    M2(X, W, 0, 3); SB(); M2(X, W, 1, 3); SB(); M2(X, W, 2, 3); SB(); M2(X, W, 3, 3); if (DMA) PC2(DS, 3); SB(); } while (0)
  WAIT_BAR(16);
#pragma unroll
  for (int i = 0; i < 4; ++i) { RDX(xa, i, 0); RDW(wa, i, 0); }
#define EV(SC, SN, W_, RD, DMA) do { WAIT_BAR(W_); SB(); STEP2(xa, wa, xb, wb, RD, SN, DMA, SC); } while (0)
#define OD(SC, SN, W_, RD, DMA) do { WAIT_BAR(W_); SB(); STEP2(xb, wb, xa, wa, RD, SN, DMA, SC); } while (0)
#define PR2(i) do { if constexpr ((i) == 0 || (((G2PAT >> (i)) & 1) != ((G2PAT >> ((i) - 1 < 0 ? 0 : (i) - 1)) & 1))) __builtin_amdgcn_s_setprio(((G2PAT >> (i)) & 1) ? 2 : 0); } while (0)
  for (int it = 0; it < 6; ++it) {
    STAMP(4 + it);
    PR2(0);
    EV(0, 1, 12, true, true);
    if (it == 5) {
      if (w == 0) await(ws, FL_O + p * 2 + kh, 8);
      rW = mkrsrc((const char*)wo - 1024, D_ * D_ * 2 + 1024);
      aso = ((112 + 16 * kh) * 128 + 8 * p + 2 * w) * 1024 - 131072; wso = 16 * kh * 65536 - 65536;
    }
    PR2(1); OD(1, 2, 12, true, true); PR2(2); EV(2, 3, 12, true, true); PR2(3); OD(3, 4, 12, true, true); PR2(4); EV(4, 0, 12, true, true);
    PR2(5); OD(0, 1, 12, true, true); PR2(6); EV(1, 2, 12, true, true); PR2(7); OD(2, 3, 12, true, true); PR2(8); EV(3, 4, 12, true, true); PR2(9); OD(4, 0, 12, true, true);
  }
  EV(0, 1, 12, true, true); OD(1, 2, 12, true, true); EV(2, 3, 12, true, true); OD(3, 4, 12, true, true); EV(4, 0, 12, true, true); OD(0, 1, 12, true, true); EV(1, 2, 12, true, true);
  OD(2, 3, 12, true, false); EV(3, 4, 8, true, false); OD(4, 0, 4, true, false); EV(0, 1, 0, true, false); OD(1, 2, 0, false, false);
  STAMP(1);
  if (kh) {
#pragma unroll
    for (int n = 0; n < 4; ++n) { f32x4 t = acc[0][n]; acc[0][n] = acc[2][n]; acc[2][n] = t; t = acc[1][n]; acc[1][n] = acc[3][n]; acc[3][n] = t; }
#pragma unroll
    for (int n = 0; n < 4; ++n) { PIN(acc[0][n]); PIN(acc[1][n]); PIN(acc[2][n]); PIN(acc[3][n]); }
  }
  const int token0 = 128 * p + 64 * wm + 32 * kh + (l & 15);
  u32x2 xres[2][4], bres[4];
#pragma unroll
  for (int n = 0; n < 4; ++n) {
    const int n0 = 128 * nt + 64 * wn + 16 * n + 4 * g;
    bres[n] = *(const u32x2*)(b2 + n0);
#pragma unroll
    for (int m = 0; m < 2; ++m) xres[m][n] = *(const u32x2*)(x + (size_t)(token0 + 16 * m) * D_ + n0);
  }
  {
    const __amdgpu_buffer_rsrc_t rP = mkrsrc(ws + WS_PART + (size_t)gi * 32768, 32768);
#pragma unroll
    for (int m = 0; m < 2; ++m)
#pragma unroll
      for (int n = 0; n < 4; ++n) st_wt16(rP, ((w * 2 + m) * 4 + n) * 1024 + l * 16, __builtin_bit_cast(u32x4, acc[2 + m][n]));
    asm volatile("s_waitcnt vmcnt(0)\n\ts_barrier" ::: "memory");
    if (tid == 0) signal(ws, FL_X + gi);
    if (w == 0) await(ws, FL_X + partner, 1);
    asm volatile("s_barrier" ::: "memory");
  }
  STAMP(2);
  const __amdgpu_buffer_rsrc_t rPP = mkrsrc(ws + WS_PART + (size_t)partner * 32768, 32768);
#pragma unroll
  for (int m = 0; m < 2; ++m) {
    unsigned d[4][2];
#pragma unroll
    for (int n = 0; n < 4; ++n) {
      const f32x4 pv = __builtin_bit_cast(f32x4, __builtin_amdgcn_raw_buffer_load_b128(rPP, ((w * 2 + m) * 4 + n) * 1024 + l * 16, 0, 16));
      const f32x4 a4 = acc[m][n];
      d[n][0] = cvtpk(a4[0] + pv[0] + lo2f(bres[n][0]) + lo2f(xres[m][n][0]), a4[1] + pv[1] + hi2f(bres[n][0]) + hi2f(xres[m][n][0]));
      d[n][1] = cvtpk(a4[2] + pv[2] + lo2f(bres[n][1]) + lo2f(xres[m][n][1]), a4[3] + pv[3] + hi2f(bres[n][1]) + hi2f(xres[m][n][1]));
    }
    const int cch = ((g & 1) << 1) | (g >> 1);
#pragma unroll
    for (int n2 = 0; n2 < 2; ++n2) {
      const u32x4 ch = chunk_of(d[2 * n2][0], d[2 * n2][1], d[2 * n2 + 1][0], d[2 * n2 + 1][1]);
      *(u32x4*)(out + (size_t)(token0 + 16 * m) * D_ + 128 * nt + 64 * wn + 32 * n2 + 8 * cch) = ch;
    }
  }
  STAMP(3);
#undef RDX
#undef RDW
}

constexpr int LDS_BYTES = 81920;
#define MODE 0
__global__ __launch_bounds__(256, 2) void k1_main(const u16* __restrict__ x, const u16* __restrict__ wq, const u16* __restrict__ wo, const u16* __restrict__ w1, const u16* __restrict__ b1, const u16* __restrict__ w2, const u16* __restrict__ b2,
                                                   u16* __restrict__ out, char* __restrict__ ws, int sel) {
  extern __shared__ __attribute__((aligned(16))) char lds_[];
  LAS char* lds = (LAS char*)lds_;
  if ((blockIdx.x >> 8) == 0) { __builtin_amdgcn_s_setprio(PRIO_Q); role_attn(blockIdx.x & 255, wq, w2, wo, ws, lds); }
  else { __builtin_amdgcn_s_setprio(PRIO_G1); role_w1(blockIdx.x & 255, w1, b1, ws, lds); role_w2(blockIdx.x & 255, w2, wo, b2, x, out, ws, lds); }
}

extern "C" void kernel_launch(void* const* d_in, const int* in_sizes, int n_in, void* d_out, int out_size, void* d_ws, size_t ws_size, hipStream_t stream) {
  const u16* x = (const u16*)d_in[0]; const u16* wq = (const u16*)d_in[1]; const u16* wk = (const u16*)d_in[2];
  const u16* wv = (const u16*)d_in[3]; const u16* wo = (const u16*)d_in[4];
  const u16* w1 = (const u16*)d_in[6]; const u16* b1 = (const u16*)d_in[7]; const u16* w2 = (const u16*)d_in[8]; const u16* b2 = (const u16*)d_in[9];
  char* ws = (char*)d_ws;
  (void)hipFuncSetAttribute((const void*)k1_main, hipFuncAttributeMaxDynamicSharedMemorySize, LDS_BYTES);
  k0_ln_kv<<<256, 512, 0, stream>>>(x, wk, wv, wq, w1, ws);
  k1_main<<<512, 256, LDS_BYTES, stream>>>(x, wq, wo, w1, b1, w2, b2, (u16*)d_out, ws, 0);
}
```

```cpp
#include <hip/hip_runtime.h>
#include <stdint.h>

typedef unsigned short u16;
typedef __bf16 bf16x8 __attribute__((ext_vector_type(8)));
typedef __bf16 bf16x2 __attribute__((ext_vector_type(2)));
typedef short s16x8 __attribute__((ext_vector_type(8)));
typedef float f32x4 __attribute__((ext_vector_type(4)));
typedef float f32x2 __attribute__((ext_vector_type(2)));
typedef unsigned u32x4 __attribute__((ext_vector_type(4)));
typedef unsigned u32x2 __attribute__((ext_vector_type(2)));
#define LAS __attribute__((address_space(3)))
#define GAS __attribute__((address_space(1)))
#define DI __device__ __forceinline__
#define SB() __builtin_amdgcn_sched_barrier(0)
#define PIN(x) asm volatile("" : "+v"(x))
#define MFMA16(a, b, c) __builtin_amdgcn_mfma_f32_16x16x32_bf16((a), (b), (c), 0, 0, 0)
#define BF(x) __builtin_bit_cast(bf16x8, x)

#define PF0ROWS 1024
constexpr int S_ = 2048, D_ = 1024, F_ = 3584;
constexpr size_t WS_FLAGS = 0;
constexpr size_t WS_R2   = 131072;
constexpr size_t WS_KV   = WS_R2 + 8192;
constexpr size_t WS_XN   = WS_KV + 262144;
constexpr size_t WS_H    = WS_XN + 4194304;
constexpr size_t WS_O    = WS_H + (size_t)112 * 128 * 1024;
constexpr size_t WS_PART = WS_O + 4194304;
constexpr size_t WS_STAMP = WS_PART + (size_t)2 * 256 * 32768;

constexpr float QSCALE = 0.17677669529663687f * 1.4426950408889634f;

DI int lane_id() { int l; asm volatile("v_mbcnt_lo_u32_b32 %0, -1, 0\n\tv_mbcnt_hi_u32_b32 %0, -1, %0" : "=v"(l)); return l; }
DI float bf2f(u16 v) { return __uint_as_float(((unsigned)v) << 16); }
DI unsigned cvtpk(float lo, float hi) { f32x2 v = {lo, hi}; bf16x2 b = __builtin_convertvector(v, bf16x2); return __builtin_bit_cast(unsigned, b); }
DI float lo2f(unsigned u) { return __uint_as_float(u << 16); }
DI float hi2f(unsigned u) { return __uint_as_float(u & 0xffff0000u); }
DI __amdgpu_buffer_rsrc_t mkrsrc(const void* p, unsigned bytes) { return __builtin_amdgcn_make_buffer_rsrc((void*)p, 0, bytes, 0x00020000); }
DI void dma16(unsigned lds_addr, unsigned voff, __amdgpu_buffer_rsrc_t rsrc, unsigned soff) {
  asm volatile("s_mov_b32 m0, %0\n\ts_nop 0\n\tbuffer_load_dwordx4 %1, %2, %3 offen lds" :: "s"(lds_addr), "v"(voff), "s"(rsrc), "s"(soff));
}
template <int IMM, int INC> DI void dma2(unsigned mbase, unsigned v0, unsigned v1, __amdgpu_buffer_rsrc_t rsrc, unsigned& soff) {
  asm volatile("s_add_u32 m0, %3, %5\n\ts_add_u32 %0, %0, %6\n\tbuffer_load_dwordx4 %1, %4, %0 offen lds\n\tbuffer_load_dwordx4 %2, %4, %0 offen offset:1024 lds"
               : "+s"(soff) : "v"(v0), "v"(v1), "s"(mbase), "s"(rsrc), "i"(IMM), "i"(INC) : "scc");
}
template <int IMM, int INC> DI void dma1a(unsigned mbase, unsigned v0, __amdgpu_buffer_rsrc_t rsrc, unsigned& soff) {
  asm volatile("s_add_u32 m0, %2, %4\n\ts_add_u32 %0, %0, %5\n\tbuffer_load_dwordx4 %1, %3, %0 offen lds" : "+s"(soff) : "v"(v0), "s"(mbase), "s"(rsrc), "i"(IMM), "i"(INC) : "scc");
}
template <int OFF> DI void dma1b(unsigned v0, __amdgpu_buffer_rsrc_t rsrc, unsigned soff) {
  asm volatile("buffer_load_dwordx4 %0, %1, %2 offen offset:%3 lds" :: "v"(v0), "s"(rsrc), "s"(soff), "i"(OFF));
}
template <int IMM, int INC> DI void dma1a_c(unsigned mbase, unsigned v0, __amdgpu_buffer_rsrc_t rsrc, unsigned& soff) {
  asm volatile("s_add_u32 m0, %2, %4\n\ts_add_u32 %0, %0, %5\n\tbuffer_load_dwordx4 %1, %3, %0 offen sc1 lds" : "+s"(soff) : "v"(v0), "s"(mbase), "s"(rsrc), "i"(IMM), "i"(INC) : "scc");
}
template <int OFF> DI void dma1b_c(unsigned v0, __amdgpu_buffer_rsrc_t rsrc, unsigned soff) {
  asm volatile("buffer_load_dwordx4 %0, %1, %2 offen offset:%3 sc1 lds" :: "v"(v0), "s"(rsrc), "s"(soff), "i"(OFF));
}
DI void dma4(unsigned lds_addr, unsigned voff, __amdgpu_buffer_rsrc_t rsrc, unsigned soff) {
  asm volatile("s_mov_b32 m0, %0\n\ts_nop 0\n\tbuffer_load_dword %1, %2, %3 offen lds" :: "s"(lds_addr), "v"(voff), "s"(rsrc), "s"(soff));
}
DI void dma4_c(unsigned lds_addr, unsigned voff, __amdgpu_buffer_rsrc_t rsrc, unsigned soff) {
  asm volatile("s_mov_b32 m0, %0\n\ts_nop 0\n\tbuffer_load_dword %1, %2, %3 offen sc1 lds" :: "s"(lds_addr), "v"(voff), "s"(rsrc), "s"(soff));
}
#define WAIT_BAR(N) asm volatile("s_waitcnt vmcnt(" #N ") lgkmcnt(0)\n\ts_barrier" ::: "memory")
DI bf16x8 ldsfrag(const LAS char* p) { return *(const LAS bf16x8*)p; }
typedef short v4i16_t __attribute__((ext_vector_type(4)));
DI bf16x8 ldstr(const LAS char* p0, const LAS char* p1) {
  v4i16_t a = __builtin_amdgcn_ds_read_tr16_b64_v4i16((LAS v4i16_t*)p0);
  v4i16_t b = __builtin_amdgcn_ds_read_tr16_b64_v4i16((LAS v4i16_t*)p1);
  s16x8 v = __builtin_shufflevector(a, b, 0, 1, 2, 3, 4, 5, 6, 7);
  return __builtin_bit_cast(bf16x8, v);
}
DI void st_wt16(__amdgpu_buffer_rsrc_t r, unsigned voff, u32x4 v) { __builtin_amdgcn_raw_buffer_store_b128(v, r, voff, 0, 16); }
DI u32x4 chunk_of(unsigned x0, unsigned x1, unsigned y0, unsigned y1) {
  auto s0 = __builtin_amdgcn_permlane16_swap(x0, y0, false, false);
  auto s1 = __builtin_amdgcn_permlane16_swap(x1, y1, false, false);
  return (u32x4){s0[0], s1[0], s0[1], s1[1]};
}
DI unsigned wdma_off(int l, int kblk, int ldbytes) {
  const int row = l >> 3, c8 = l & 7, sw = ((row >> 1) & 1) | ((kblk & 1) << 1);
  return row * ldbytes + (((c8 >> 1) ^ sw) * 32) + (c8 & 1) * 16;
}
DI unsigned wrd_off(int l, int seg) {
  const int g = l >> 4, q = (l & 15) >> 2, sw = ((q >> 1) & 1) | ((g & 1) << 1);
  return q * 128 + ((seg ^ sw) * 32) + (l & 3) * 8;
}
DI float xsum4(float v) {
  auto a = __builtin_amdgcn_permlane16_swap(__float_as_uint(v), __float_as_uint(v), false, false); v = __uint_as_float(a[0]) + __uint_as_float(a[1]);
  auto b = __builtin_amdgcn_permlane32_swap(__float_as_uint(v), __float_as_uint(v), false, false); return __uint_as_float(b[0]) + __uint_as_float(b[1]);
}
DI float xmax4(float v) {
  auto a = __builtin_amdgcn_permlane16_swap(__float_as_uint(v), __float_as_uint(v), false, false); v = fmaxf(__uint_as_float(a[0]), __uint_as_float(a[1]));
  auto b = __builtin_amdgcn_permlane32_swap(__float_as_uint(v), __float_as_uint(v), false, false); return fmaxf(__uint_as_float(b[0]), __uint_as_float(b[1]));
}
#define STAMP(i)

typedef unsigned long long u64;
constexpr int FL_E = 0, FL_RD = 16, FL_XN = 528, FL_KV = 784, FL_H = 1040, FL_O = 1296, FL_X = 1552;
DI GAS u64* flagp(char* ws, int i) { return (GAS u64*)(ws + WS_FLAGS + (size_t)i * 64); }
DI void signal(char* ws, int i, u64 tag) { __hip_atomic_store(flagp(ws, i), tag, __ATOMIC_RELAXED, __HIP_MEMORY_SCOPE_AGENT); }
DI void await(char* ws, int idx, u64 tag) {
  GAS u64* f = flagp(ws, idx < 0 ? FL_E : idx);
  for (unsigned spins = 0; spins < (1u << 20); ++spins) {
    const u64 v = __hip_atomic_load(f, __ATOMIC_RELAXED, __HIP_MEMORY_SCOPE_AGENT);
    if (__all(idx < 0 || v == tag)) break;
    __builtin_amdgcn_s_sleep(2);
  }
  asm volatile("" ::: "memory");
}

DI void phase0(int a, const u16* __restrict__ x, const u16* __restrict__ wk, const u16* __restrict__ wv, char* __restrict__ ws, LAS char* lds, const int w, const u64 tag) {
  const int l = lane_id(), tid = w * 64 + l;
  LAS float* part = (LAS float*)(lds + 65536);
  LAS float* fin = (LAS float*)(lds + 65536 + 8192);
  LAS float* red = (LAS float*)(lds + 65536 + 8192 + 2048);
  const int rb = a & 127, hf = a >> 7;
  const int r = l & 15, c = l >> 4; const int stamp_base = 768; (void)stamp_base;
  STAMP(0);
  u32x4 xv[8];
  { const u16* xr = x + (size_t)(rb * 16 + r) * D_ + w * 256 + c * 8;
#define XL(i) asm volatile("global_load_dwordx4 %0, %1, off offset:" #i "*64" : "=&v"(xv[i]) : "v"(xr))
    XL(0); XL(1); XL(2); XL(3); XL(4); XL(5); XL(6); XL(7); }
  const unsigned lds0 = (unsigned)(uintptr_t)lds;
  {
    __amdgpu_buffer_rsrc_t rw = mkrsrc(hf ? wv : wk, 65536);
#pragma unroll
    for (int i = 0; i < 16; ++i) dma16(lds0 + w * 16384 + i * 1024, l * 16, rw, w * 16384 + i * 1024);
  }
  asm volatile("s_waitcnt vmcnt(16)" : "+v"(xv[0]), "+v"(xv[1]), "+v"(xv[2]), "+v"(xv[3]), "+v"(xv[4]), "+v"(xv[5]), "+v"(xv[6]), "+v"(xv[7]));
  STAMP(1);
  float s = 0.f;
#pragma unroll
  for (int i = 0; i < 8; ++i)
#pragma unroll
    for (int j = 0; j < 4; ++j) s += lo2f(xv[i][j]) + hi2f(xv[i][j]);
  s = xsum4(s);
  if (c == 0) red[w * 16 + r] = s;
  asm volatile("s_waitcnt lgkmcnt(0)\n\ts_barrier" ::: "memory");
  const float mean = ((red[r] + red[16 + r]) + (red[32 + r] + red[48 + r])) * (1.0f / D_);
  float q = 0.f;
#pragma unroll
  for (int i = 0; i < 8; ++i)
#pragma unroll
    for (int j = 0; j < 4; ++j) { const float u = lo2f(xv[i][j]) - mean, v = hi2f(xv[i][j]) - mean; q += u * u + v * v; }
  q = xsum4(q);
  if (c == 0) red[64 + w * 16 + r] = q;
  asm volatile("s_waitcnt lgkmcnt(0)\n\ts_barrier" ::: "memory");
  const float var = ((red[64 + r] + red[80 + r]) + (red[96 + r] + red[112 + r])) * (1.0f / D_);
  const float rstd = 1.0f / sqrtf(var + 1e-5f);
#pragma unroll
  for (int i = 0; i < 8; ++i)
#pragma unroll
    for (int j = 0; j < 4; ++j) xv[i][j] = cvtpk((lo2f(xv[i][j]) - mean) * rstd, (hi2f(xv[i][j]) - mean) * rstd);
  const __amdgpu_buffer_rsrc_t rXN = mkrsrc(ws + WS_XN, 4194304);
#pragma unroll
  for (int i = 0; i < 4; ++i) st_wt16(rXN, ((8 * w + 2 * i + hf) * 128 + rb) * 1024 + l * 16, hf ? xv[2 * i + 1] : xv[2 * i]);
  if (hf == 0 && w == 0 && c == 0) __hip_atomic_store((GAS float*)(ws + WS_R2) + rb * 16 + r, 1.0f / sqrtf(var * rstd * rstd + 1e-5f), __ATOMIC_RELAXED, __HIP_MEMORY_SCOPE_AGENT);
  asm volatile("s_waitcnt vmcnt(4)" ::: "memory");
  f32x4 acc[2] = {{0.f, 0.f, 0.f, 0.f}, {0.f, 0.f, 0.f, 0.f}};
  const LAS char* wb = (const LAS char*)lds + w * 16384 + (8 * c + ((l & 15) >> 2)) * 64 + (l & 3) * 8;
#pragma unroll
  for (int i = 0; i < 8; ++i)
#pragma unroll
    for (int b = 0; b < 2; ++b) {
      bf16x8 wf = ldstr(wb + i * 2048 + b * 32, wb + i * 2048 + b * 32 + 256);
      acc[b] = MFMA16(BF(xv[i]), wf, acc[b]);
    }
#pragma unroll
  for (int b = 0; b < 2; ++b)
#pragma unroll
    for (int j = 0; j < 4; ++j) part[(w * 16 + 4 * c + j) * 32 + 16 * b + r] = acc[b][j];
  asm volatile("s_waitcnt vmcnt(0) lgkmcnt(0)\n\ts_barrier" ::: "memory");
  if (tid == 0) signal(ws, FL_XN + a, tag);
  STAMP(2);
#pragma unroll
  for (int u = 0; u < 2; ++u) { const int e = tid + 256 * u; fin[e] = (part[e] + part[512 + e]) + (part[1024 + e] + part[1536 + e]); }
  asm volatile("s_waitcnt lgkmcnt(0)\n\ts_barrier" ::: "memory");
  if (w == 0) {
    float o[8];
    unsigned dst = (rb >> 1) * 4096;
    const int half = rb & 1;
    if (hf == 0) { const int kb = l >> 5, g = (l >> 3) & 3, rr = l & 7;
      const int row = 8 * half + rr, key = 8 * (row >> 2) + 4 * kb + (row & 3) - 16 * half;
#pragma unroll
      for (int j = 0; j < 8; ++j) o[j] = fin[key * 32 + 16 * (j >> 2) + 4 * g + (j & 3)];
      dst += kb * 1024 + (g * 16 + row) * 16;
    } else { const int db = l >> 5, gl = (l >> 4) & 1, rr = l & 15;
#pragma unroll
      for (int j = 0; j < 8; ++j) o[j] = fin[(8 * gl + j) * 32 + 16 * db + rr];
      dst += 2048 + db * 1024 + ((2 * half + gl) * 16 + rr) * 16;
    }
    st_wt16(mkrsrc(ws + WS_KV, 262144), dst, (u32x4){cvtpk(o[0], o[1]), cvtpk(o[2], o[3]), cvtpk(o[4], o[5]), cvtpk(o[6], o[7])});
    asm volatile("s_waitcnt vmcnt(0)" ::: "memory");
    if (l == 0) signal(ws, FL_KV + a, tag);
  }
  STAMP(3);
}

#define MERGED 1
#define PRIO_A 1
#define G1PAT 0
#define G2PAT 0
#define PFSLEEP 1
#define PRIO_Q 1
#define PRIO_G1 0
#define PRIO_GE 2
#define PRIO_G2 0
#define LSUM_MFMA 1
DI float fadd_s(float a, float b) { float r; asm("s_nop 0\n\tv_add_f32_e32 %0, %1, %2" : "=v"(r) : "v"(a), "v"(b)); return r; }

DI void attn_slow(const char* __restrict__ kv, bf16x8 qf, f32x4& o0, f32x4& o1) {
  const int l = lane_id();
  const __amdgpu_buffer_rsrc_t rk = mkrsrc(kv, 262144);
  o0 = (f32x4){0.f, 0.f, 0.f, 0.f}; o1 = o0;
  float m = -3.0e38f, ls = 0.f;
#pragma unroll 1
  for (int t = 0; t < 64; ++t) {
    const int p = t * 4096 + l * 16;
    const bf16x8 k0 = BF(__builtin_amdgcn_raw_buffer_load_b128(rk, p, 0, 16)), k1 = BF(__builtin_amdgcn_raw_buffer_load_b128(rk, p + 1024, 0, 16)),
                 v0 = BF(__builtin_amdgcn_raw_buffer_load_b128(rk, p + 2048, 0, 16)), v1 = BF(__builtin_amdgcn_raw_buffer_load_b128(rk, p + 3072, 0, 16));
    const f32x4 z = {0.f, 0.f, 0.f, 0.f};
    f32x4 s0 = MFMA16(k0, qf, z), s1 = MFMA16(k1, qf, z);
    float mx = fmaxf(fmaxf(fmaxf(s0[0], s0[1]), fmaxf(s0[2], s0[3])), fmaxf(fmaxf(s1[0], s1[1]), fmaxf(s1[2], s1[3])));
    mx = fmaxf(mx, __shfl_xor(mx, 16)); mx = fmaxf(mx, __shfl_xor(mx, 32));
    const float mn = fmaxf(m, mx), al = __builtin_amdgcn_exp2f(m - mn);
    m = mn; float rs = 0.f;
#pragma unroll
    for (int i = 0; i < 4; ++i) { s0[i] = __builtin_amdgcn_exp2f(s0[i] - mn); s1[i] = __builtin_amdgcn_exp2f(s1[i] - mn); rs += s0[i] + s1[i]; o0[i] *= al; o1[i] *= al; }
    rs += __shfl_xor(rs, 16); rs += __shfl_xor(rs, 32); ls = ls * al + rs;
    const u32x4 pp = {cvtpk(s0[0], s0[1]), cvtpk(s0[2], s0[3]), cvtpk(s1[0], s1[1]), cvtpk(s1[2], s1[3])};
    o0 = MFMA16(v0, BF(pp), o0); o1 = MFMA16(v1, BF(pp), o1);
  }
#pragma unroll
  for (int i = 0; i < 4; ++i) { o0[i] /= ls; o1[i] /= ls; }
}

DI void role_attn(int a, const u16* __restrict__ wq, const u16* __restrict__ w2, const u16* __restrict__ wo, char* __restrict__ ws, LAS char* lds, const int w, const u64 tag) {
  const int l = lane_id();
  const int xcd = a & 7, idx = a >> 3, tb = (xcd >> 2) * 16 + (idx & 15), hg = (xcd & 3) * 2 + (idx >> 4);
  const int g = l >> 4; const int stamp_base = 0; (void)stamp_base;
  const unsigned lds0 = (unsigned)(uintptr_t)lds;
  constexpr int SLOT = 24576;
  const __amdgpu_buffer_rsrc_t rX = mkrsrc(ws + WS_XN, 4194304), rKV = mkrsrc(ws + WS_KV, 262144);
  const unsigned vlin = l * 16;
  constexpr int QS = 12288;
  const __amdgpu_buffer_rsrc_t rWq = mkrsrc((const char*)wq - 1024, D_ * D_ * 2 + 1024);
  const unsigned vq0 = wdma_off(l, w, 2048) + hg * 256 + w * 8 * 2048 + 1024, vq1 = vq0 + 128 - 1024;
  unsigned xso = (4 * tb + w) * 1024 - 131072, qso = 0u - 65536;
  const unsigned mx = lds0 + w * 1024, mq = lds0 + 4096 + w * 2048;
#define PCQ(SL, i) do { if constexpr ((i) == 0) dma1a_c<(SL) * QS, 131072>(mx, vlin, rX, xso); else if constexpr ((i) == 1) dma1a<(SL) * QS, 65536>(mq, vq0, rWq, qso); else dma1b<1024>(vq1, rWq, qso); } while (0)
#define STGQ(SL) do { PCQ(SL, 0); PCQ(SL, 1); PCQ(SL, 2); } while (0)
  if (w == 0) await(ws, l < 8 ? FL_XN + 4 * tb + (l & 3) + 128 * (l >> 2) : -1, tag);
  asm volatile("s_waitcnt lgkmcnt(0)\n\ts_barrier" ::: "memory");
  STAMP(0);
  STGQ(0); STGQ(1); STGQ(2); STGQ(3); STGQ(4); STGQ(5);
  f32x4 qacc[2][4];
#pragma unroll
  for (int i = 0; i < 2; ++i)
#pragma unroll
    for (int j = 0; j < 4; ++j) qacc[i][j] = (f32x4){0.f, 0.f, 0.f, 0.f};
  const LAS char* xrd = lds + l * 16;
  const LAS char* wrd0 = lds + 4096 + (g * 2 + (w >> 1)) * 1024 + wrd_off(l, 2 * (w & 1));
  const LAS char* wrd1 = lds + 4096 + (g * 2 + (w >> 1)) * 1024 + wrd_off(l, 2 * (w & 1) + 1);
  bf16x8 qxa[4], qwa[2], qxb[4], qwb[2];
#define QRX(X, m, SL) X[m] = ldsfrag(xrd + (SL) * QS + (m) * 1024)
#define QRW(W, n, SL) W[n] = ((n) == 0 ? ldstr(wrd0 + (SL) * QS, wrd0 + (SL) * QS + 512) : ldstr(wrd1 + (SL) * QS, wrd1 + (SL) * QS + 512))
#define QM(X, W, m, n) qacc[n][m] = MFMA16(W[n], X[m], qacc[n][m])
#define STEPQ(X, W, XN, WN, RD, RS, DMA, DS) do { \
    QM(X, W, 0, 0); if (RD) { QRW(WN, 0, RS); QRX(XN, 0, RS); } SB(); QM(X, W, 1, 0); if (RD) { QRW(WN, 1, RS); QRX(XN, 1, RS); } SB(); QM(X, W, 2, 0); if (RD) QRX(XN, 2, RS); SB(); QM(X, W, 3, 0); if (RD) QRX(XN, 3, RS); SB(); \
    QM(X, W, 0, 1); if (DMA) PCQ(DS, 0); SB(); QM(X, W, 1, 1); if (DMA) PCQ(DS, 1); SB(); QM(X, W, 2, 1); if (DMA) PCQ(DS, 2); SB(); QM(X, W, 3, 1); SB(); } while (0)
  WAIT_BAR(15);
#pragma unroll
  for (int m = 0; m < 4; ++m) QRX(qxa, m, 0);
  QRW(qwa, 0, 0); QRW(qwa, 1, 0);
#define QEV(SC, SN, W_, RD, DMA) do { WAIT_BAR(W_); SB(); STEPQ(qxa, qwa, qxb, qwb, RD, SN, DMA, SC); } while (0)
#define QOD(SC, SN, W_, RD, DMA) do { WAIT_BAR(W_); SB(); STEPQ(qxb, qwb, qxa, qwa, RD, SN, DMA, SC); } while (0)
  QEV(0, 1, 12, true, true); QOD(1, 2, 12, true, true); QEV(2, 3, 12, true, true); QOD(3, 4, 12, true, true); QEV(4, 5, 12, true, true); QOD(5, 0, 12, true, true);
  for (int it = 0; it < 3; ++it) { QEV(0, 1, 12, true, true); QOD(1, 2, 12, true, true); QEV(2, 3, 12, true, true); QOD(3, 4, 12, true, true); QEV(4, 5, 12, true, true); QOD(5, 0, 12, true, true); }
  QEV(0, 1, 12, true, true); QOD(1, 2, 12, true, true);
  QEV(2, 3, 12, true, false); QOD(3, 4, 9, true, false); QEV(4, 5, 6, true, false); QOD(5, 0, 3, true, false);
  if (w == 0) { await(ws, FL_KV + l, tag); await(ws, FL_KV + 64 + l, tag); await(ws, FL_KV + 128 + l, tag); await(ws, FL_KV + 192 + l, tag); }
  QEV(0, 1, 0, true, false);
  unsigned kvso = w * 4096 - 16384;
  const unsigned kvm = lds0 + w * 4096;
#define STGKV(SF) do { dma1a_c<(SF) * SLOT, 16384>(kvm, vlin, rKV, kvso); dma1b_c<1024>(vlin, rKV, kvso); dma1b_c<2048>(vlin, rKV, kvso); dma1b_c<3072>(vlin, rKV, kvso); } while (0)
  STGKV(1); STGKV(2);
  {
    const unsigned line = (a * 4 + w) * 64 + l;
    dma4(lds0 + 6 * QS, line * 128, mkrsrc(w2, F_ * D_ * 2), 0);
    dma4(lds0 + 6 * QS, line * 128, mkrsrc(wo, D_ * D_ * 2), 0);
  }
  STEPQ(qxb, qwb, qxa, qwa, false, 0, false, 0);
  STAMP(1);
  __builtin_amdgcn_s_setprio(PRIO_A);
  bf16x8 qf[4];
#pragma unroll
  for (int m = 0; m < 4; ++m)
    qf[m] = BF(((u32x4){cvtpk(qacc[0][m][0] * QSCALE, qacc[0][m][1] * QSCALE), cvtpk(qacc[0][m][2] * QSCALE, qacc[0][m][3] * QSCALE),
                        cvtpk(qacc[1][m][0] * QSCALE, qacc[1][m][1] * QSCALE), cvtpk(qacc[1][m][2] * QSCALE, qacc[1][m][3] * QSCALE)}));
  WAIT_BAR(6);
  const LAS char* kvb = lds + l * 16;
  f32x4 negm[4], S[4][2], O[4][2];
  bf16x8 k0, k1;
  {
    const LAS char* p = kvb + 1 * SLOT;
    k0 = ldsfrag(p); k1 = ldsfrag(p + 1024);
    const f32x4 z = {0.f, 0.f, 0.f, 0.f};
#pragma unroll
    for (int m = 0; m < 4; ++m) {
      const f32x4 s0 = MFMA16(k0, qf[m], z), s1 = MFMA16(k1, qf[m], z);
      float mx = fmaxf(fmaxf(fmaxf(s0[0], s0[1]), fmaxf(s0[2], s0[3])), fmaxf(fmaxf(s1[0], s1[1]), fmaxf(s1[2], s1[3])));
      mx = xmax4(mx);
      negm[m] = (f32x4){-mx, -mx, -mx, -mx}; PIN(negm[m]);
      O[m][0] = z; O[m][1] = z;
    }
    S[0][0] = MFMA16(k0, qf[0], negm[0]); S[0][1] = MFMA16(k1, qf[0], negm[0]);
    S[1][0] = MFMA16(k0, qf[1], negm[1]); S[1][1] = MFMA16(k1, qf[1], negm[1]);
  }
  u32x4 P[4];
  P[2] = (u32x4){0, 0, 0, 0}; P[3] = P[2];
  bf16x8 va0 = BF(P[2]), va1 = va0, vb0, vb1;
  S[3][1] = (f32x4){0.f, 0.f, 0.f, 0.f};
  f32x4 L[4];
#pragma unroll
  for (int m = 0; m < 4; ++m) L[m] = (f32x4){0.f, 0.f, 0.f, 0.f};
  bf16x8 ones; { const short o1 = (l & 15) == 0 ? (short)0x3f80 : (short)0; s16x8 ov = {o1, o1, o1, o1, o1, o1, o1, o1}; ones = __builtin_bit_cast(bf16x8, ov); PIN(ones); }
#define LS(m) do { L[m] = MFMA16(ones, BF(P[m]), L[m]); PIN(L[m]); SB(); } while (0)
#define LADD2(m, kb, i)
#define NOLS(m)
#define EXPF(x) __builtin_amdgcn_exp2f(x)
#define EX2(m, kb, i) do { float t0_ = EXPF(S[m][kb][i]), t1_ = EXPF(S[m][kb][i + 1]); PIN(t0_); PIN(t1_); S[m][kb][i] = t0_; S[m][kb][i + 1] = t1_; SB(); } while (0)
#define CVA(m, kb) do { unsigned c_ = cvtpk(S[m][kb][0], S[m][kb][1]); PIN(c_); P[m][2 * kb] = c_; LADD2(m, kb, 0); SB(); } while (0)
#define CVB(m, kb) do { unsigned c_ = cvtpk(S[m][kb][2], S[m][kb][3]); PIN(c_); P[m][2 * kb + 1] = c_; LADD2(m, kb, 2); SB(); } while (0)
#define GRP(mq, kq, KF, pm, pk, me, ke, mp, db, VF, LSM, X) do { \
    S[mq][kq] = MFMA16(KF, qf[mq], negm[mq]); PIN(S[mq][kq]); SB(); X; CVB(pm, pk); EX2(me, ke, 0); \
    O[mp][db] = MFMA16(VF, BF(P[mp]), O[mp][db]); PIN(O[mp][db]); SB(); EX2(me, ke, 2); LSM(mp); CVA(me, ke); } while (0)
#define TILE(KC0, KC1, KN0, KN1, VP0, VP1, VC0, VC1, kn, vp, D) do { \
    GRP(2, 0, KC0, 3, 1, 0, 0, 2, 0, VP0, NOLS, KN0 = ldsfrag(kn); SB()); \
    GRP(2, 1, KC1, 0, 0, 0, 1, 2, 1, VP1, LS,   KN1 = ldsfrag((kn) + 1024); SB()); \
    GRP(3, 0, KC0, 0, 1, 1, 0, 3, 0, VP0, NOLS, VC0 = ldsfrag(vp); SB()); \
    GRP(3, 1, KC1, 1, 0, 1, 1, 3, 1, VP1, LS,   VC1 = ldsfrag((vp) + 1024); SB()); \
    GRP(0, 0, KN0, 1, 1, 2, 0, 0, 0, VC0, NOLS, D(0)); \
    GRP(0, 1, KN1, 2, 0, 2, 1, 0, 1, VC1, LS,   D(1)); \
    GRP(1, 0, KN0, 2, 1, 3, 0, 1, 0, VC0, NOLS, D(2)); \
    GRP(1, 1, KN1, 3, 0, 3, 1, 1, 1, VC1, LS,   D(3)); \
  } while (0)
#define NOD(i)
  bf16x8 kx0, kx1;
#define STAGE(SC, SN, SF, DM, LAST) do { \
    TILE(k0, k1, kx0, kx1, va0, va1, vb0, vb1, kvb + (SC) * SLOT + 4096, kvb + (SC) * SLOT + 2048, NOD); \
    TILE(kx0, kx1, k0, k1, vb0, vb1, va0, va1, kvb + (SC) * SLOT + 8192, kvb + (SC) * SLOT + 4096 + 2048, NOD); \
    TILE(k0, k1, kx0, kx1, va0, va1, vb0, vb1, kvb + (SC) * SLOT + 12288, kvb + (SC) * SLOT + 8192 + 2048, NOD); \
    WAIT_BAR(0);        \
    TILE(kx0, kx1, k0, k1, vb0, vb1, va0, va1, kvb + ((LAST) ? (SC) : (SN)) * SLOT, kvb + (SC) * SLOT + 12288 + 2048, DM); } while (0)
#define DM_(SF, i) do { if constexpr ((i) == 0) { dma1a_c<(SF) * SLOT, 16384>(kvm, vlin, rKV, kvso); } else { dma1b_c<(i) * 1024>(vlin, rKV, kvso); } SB(); } while (0)
#define DM0(i) DM_(0, i)
#define DM1(i) DM_(1, i)
#define DM2(i) DM_(2, i)
  for (int j = 0; j < 12; j += 3) { STAGE(1, 2, 0, DM0, false); STAGE(2, 0, 1, DM1, false); STAGE(0, 1, 2, DM2, false); STAMP(4 + j / 3); }
  STAGE(1, 2, 0, DM0, false); STAGE(2, 0, 1, DM1, false); STAGE(0, 1, 2, NOD, false); STAGE(1, 2, 0, NOD, true);
  CVB(3, 1);
  O[2][0] = MFMA16(va0, BF(P[2]), O[2][0]); O[2][1] = MFMA16(va1, BF(P[2]), O[2][1]); LS(2);
  O[3][0] = MFMA16(va0, BF(P[3]), O[3][0]); O[3][1] = MFMA16(va1, BF(P[3]), O[3][1]); LS(3);
  STAMP(2);
  const __amdgpu_buffer_rsrc_t rO = mkrsrc(ws + WS_O, 4194304);
#pragma unroll
  for (int m = 0; m < 4; ++m) {
    float ls = __shfl(L[m][0], l & 15);
    float inv = 1.0f / ls;
    f32x4 o0 = O[m][0], o1 = O[m][1];
    const float mo = fmaxf(fmaxf(fmaxf(fabsf(o0[0]), fabsf(o0[1])), fmaxf(fabsf(o0[2]), fabsf(o0[3]))), fmaxf(fmaxf(fabsf(o1[0]), fabsf(o1[1])), fmaxf(fabsf(o1[2]), fabsf(o1[3]))));
    const bool ok = (ls <= 1.0e18f) && (mo <= 1.0e37f);
    if (__builtin_expect(__any(!ok), 0)) { attn_slow(ws + WS_KV, qf[m], o0, o1); inv = 1.0f; }
    const u32x4 ch = chunk_of(cvtpk(o0[0] * inv, o0[1] * inv), cvtpk(o0[2] * inv, o0[3] * inv), cvtpk(o1[0] * inv, o1[1] * inv), cvtpk(o1[2] * inv, o1[3] * inv));
    const int c = ((g & 1) << 1) | (g >> 1);
    st_wt16(rO, ((4 * hg + w) * 128 + 4 * tb + m) * 1024 + c * 256 + (l & 15) * 16, ch);
  }
  asm volatile("s_waitcnt vmcnt(0)\n\ts_barrier" ::: "memory");
  if (w == 0 && lane_id() == 0) signal(ws, FL_O + tb * 8 + hg, tag);
  STAMP(3);
}

DI f32x4 gelu4(f32x4 acc, float rs, f32x4 b, float c1v) {
  constexpr float c2 = -2.0f * 0.7978845608028654f * 1.4426950408889634f * 0.044715f;
  float x0 = acc[0], x1 = acc[1], x2 = acc[2], x3 = acc[3], t0, t1, t2, t3;
  asm("v_fma_f32 %0, %0, %8, %9\n\tv_fma_f32 %1, %1, %8, %10\n\tv_fma_f32 %2, %2, %8, %11\n\tv_fma_f32 %3, %3, %8, %12\n\t"
      "v_mul_f32 %4, %0, %0\n\tv_mul_f32 %5, %1, %1\n\tv_mul_f32 %6, %2, %2\n\tv_mul_f32 %7, %3, %3\n\t"
      "v_fma_f32 %4, %4, %14, %13\n\tv_fma_f32 %5, %5, %14, %13\n\tv_fma_f32 %6, %6, %14, %13\n\tv_fma_f32 %7, %7, %14, %13\n\t"
      "v_mul_f32 %4, %0, %4\n\tv_mul_f32 %5, %1, %5\n\tv_mul_f32 %6, %2, %6\n\tv_mul_f32 %7, %3, %7\n\t"
      "v_exp_f32 %4, %4\n\tv_exp_f32 %5, %5\n\tv_exp_f32 %6, %6\n\tv_exp_f32 %7, %7\n\t"
      "v_add_f32 %4, 1.0, %4\n\tv_add_f32 %5, 1.0, %5\n\tv_add_f32 %6, 1.0, %6\n\tv_add_f32 %7, 1.0, %7\n\t"
      "v_rcp_f32 %4, %4\n\tv_rcp_f32 %5, %5\n\tv_rcp_f32 %6, %6\n\tv_rcp_f32 %7, %7\n\t"
      "v_mul_f32 %0, %0, %4\n\tv_mul_f32 %1, %1, %5\n\tv_mul_f32 %2, %2, %6\n\tv_mul_f32 %3, %3, %7"
      : "+v"(x0), "+v"(x1), "+v"(x2), "+v"(x3), "=&v"(t0), "=&v"(t1), "=&v"(t2), "=&v"(t3)
      : "v"(rs), "v"(b[0]), "v"(b[1]), "v"(b[2]), "v"(b[3]), "v"(c1v), "s"(c2));
  return (f32x4){x0, x1, x2, x3};
}

DI void role_w1(int gi, const u16* __restrict__ wq, const u16* __restrict__ w1, const u16* __restrict__ b1, char* __restrict__ ws, LAS char* lds, const int w, const u64 tag) {
  const int l = lane_id();
  const int xcd = gi & 7, idx = gi >> 3, p = (xcd >> 2) * 8 + (idx & 7), j = (xcd & 3) * 4 + (idx >> 3);
  const int g = l >> 4, wm = w >> 1, wn = w & 1; const int stamp_base = 256; (void)stamp_base;
  const unsigned lds0 = (unsigned)(uintptr_t)lds;
  constexpr int SLOT = 24576;
  const __amdgpu_buffer_rsrc_t rX = mkrsrc(ws + WS_XN, 4194304), rW = mkrsrc((const char*)w1 - 4096, D_ * F_ * 2 + 4096);
  const unsigned vlin = l * 16;
  unsigned vw[4];
#pragma unroll
  for (int i = 0; i < 4; ++i) vw[i] = wdma_off(l, w, F_ * 2) + 8 * w * (F_ * 2) + 448 * j - 64 * (j & 1) + 128 * i + 4096 - 1024 * i;
  STAMP(0);
  unsigned xso = (8 * p + 2 * w) * 1024 - 131072, wso = 0u - 32 * F_ * 2;
  const unsigned mx = lds0 + w * 2048, mw = lds0 + 8192 + w * 4096;
#define PC1(SL, i) do { if constexpr ((i) == 0) dma1a_c<(SL) * SLOT, 131072>(mx, vlin, rX, xso); else if constexpr ((i) == 1) dma1b_c<1024>(vlin, rX, xso); \
    else if constexpr ((i) == 2) dma1a<(SL) * SLOT, 32 * F_ * 2>(mw, vw[0], rW, wso); else dma1b<((i) - 2) * 1024>(vw[(i) - 2], rW, wso); } while (0)
#define STG1(SL) do { PC1(SL, 0); PC1(SL, 1); PC1(SL, 2); PC1(SL, 3); PC1(SL, 4); PC1(SL, 5); } while (0)
  PC1(0, 2); PC1(0, 3); PC1(0, 4); PC1(0, 5); PC1(1, 2); PC1(1, 3); PC1(1, 4); PC1(1, 5); PC1(2, 2); PC1(2, 3); PC1(2, 4); PC1(2, 5);
  constexpr int L_R2 = 3 * SLOT, L_B1 = L_R2 + 512;
  if (w != 0) {
    __builtin_amdgcn_s_sleep(PFSLEEP);
    if (w == 1) dma4(lds0 + L_B1 + 1024, (gi * 64 + l) * 128, mkrsrc(wq, D_ * D_ * 2), 0);
    else {
#pragma unroll
      for (int i = 0; i < 2; ++i) dma4(lds0 + L_B1 + 1024, l < 56 ? (((gi * 2 + (w - 2)) * 2 + i) * 56 + l) * 128 : 0x80000000u, mkrsrc(w1, D_ * F_ * 2), 0);
    }
  }
  if (w >= 2) dma4(lds0 + L_B1 + (w - 2) * 256, 448 * j + ((w - 2) * 64 + l) * 4, mkrsrc(b1, F_ * 2), 0);
  if (w == 0) await(ws, l < 16 ? FL_XN + 8 * p + (l & 7) + 128 * (l >> 3) : -1, tag);
  asm volatile("s_barrier" ::: "memory");
  STAMP(8);
  if (w < 2) dma4_c(lds0 + L_R2 + w * 256, (128 * p + 64 * w + l) * 4, mkrsrc(ws + WS_R2, 8192), 0);
  PC1(0, 0); PC1(0, 1); PC1(1, 0); PC1(1, 1); PC1(2, 0); PC1(2, 1);
  f32x4 acc[4][7];
#pragma unroll
  for (int m = 0; m < 4; ++m)
#pragma unroll
    for (int n = 0; n < 7; ++n) acc[m][n] = (f32x4){0.f, 0.f, 0.f, 0.f};
  const LAS char* xrd = lds + 4 * wm * 1024 + l * 16;
  const LAS char* wrb[5];
  { const int off = 6 * wn + 2 * (j & 1), nl = 12 + wn + 2 * (j & 1);
#pragma unroll
    for (int i = 0; i < 4; ++i) wrb[i] = lds + 8192 + (g * 4 + ((i + off) >> 2)) * 1024 + wrd_off(l, (i + off) & 3);
    wrb[4] = lds + 8192 + (g * 4 + (nl >> 2)) * 1024 + wrd_off(l, nl & 3); }
  bf16x8 xa[4], wa[7], xb[4], wb[7];
#define RDX(X, m, so) X[m] = ldsfrag(xrd + (so) + (m) * 1024)
#define RDW(W, n, so) W[n] = ((n) < 6 ? ldstr(wrb[(n) & 3] + (so) + ((n) >> 2) * 1024, wrb[(n) & 3] + (so) + ((n) >> 2) * 1024 + 512) : ldstr(wrb[4] + (so), wrb[4] + (so) + 512))
#define M1(X, W, m, n) acc[m][n] = MFMA16(W[n], X[m], acc[m][n])
#define STEP1(X, W, XN, WN, RD, RS, DMA, DS) do { \
    M1(X, W, 0, 0); if (RD) RDW(WN, 0, (RS) * SLOT); SB(); M1(X, W, 1, 0); if (RD) RDX(XN, 0, (RS) * SLOT); SB(); M1(X, W, 2, 0); if (RD) RDW(WN, 1, (RS) * SLOT); SB(); M1(X, W, 3, 0); if (DMA) PC1(DS, 0); SB(); \
    M1(X, W, 0, 1); if (RD) RDX(XN, 1, (RS) * SLOT); SB(); M1(X, W, 1, 1); if (RD) RDW(WN, 2, (RS) * SLOT); SB(); M1(X, W, 2, 1); if (RD) RDX(XN, 2, (RS) * SLOT); SB(); M1(X, W, 3, 1); if (DMA) PC1(DS, 1); SB(); \
    M1(X, W, 0, 2); if (RD) RDW(WN, 3, (RS) * SLOT); SB(); M1(X, W, 1, 2); if (RD) RDX(XN, 3, (RS) * SLOT); SB(); M1(X, W, 2, 2); if (RD) RDW(WN, 4, (RS) * SLOT); SB(); M1(X, W, 3, 2); if (DMA) PC1(DS, 2); SB(); \
    M1(X, W, 0, 3); if (RD) RDW(WN, 5, (RS) * SLOT); SB(); M1(X, W, 1, 3); SB(); M1(X, W, 2, 3); if (RD) RDW(WN, 6, (RS) * SLOT); SB(); M1(X, W, 3, 3); if (DMA) PC1(DS, 3); SB(); \
    M1(X, W, 0, 4); SB(); M1(X, W, 1, 4); SB(); M1(X, W, 2, 4); SB(); M1(X, W, 3, 4); if (DMA) PC1(DS, 4); SB(); \
    M1(X, W, 0, 5); SB(); M1(X, W, 1, 5); SB(); M1(X, W, 2, 5); SB(); M1(X, W, 3, 5); if (DMA) PC1(DS, 5); SB(); \
    M1(X, W, 0, 6); SB(); M1(X, W, 1, 6); SB(); M1(X, W, 2, 6); SB(); M1(X, W, 3, 6); SB(); } while (0)
  WAIT_BAR(2);
#pragma unroll
  for (int m = 0; m < 4; ++m) RDX(xa, m, 0);
#pragma unroll
  for (int n = 0; n < 7; ++n) RDW(wa, n, 0);
#define EV(SC, SN, W_, RD, DMA) do { WAIT_BAR(W_); SB(); STEP1(xa, wa, xb, wb, RD, SN, DMA, SC); } while (0)
#define OD(SC, SN, W_, RD, DMA) do { WAIT_BAR(W_); SB(); STEP1(xb, wb, xa, wa, RD, SN, DMA, SC); } while (0)
#define PR1(i) do { if constexpr ((i) == 0 || (((G1PAT >> (i)) & 1) != ((G1PAT >> ((i) - 1 < 0 ? 0 : (i) - 1)) & 1))) __builtin_amdgcn_s_setprio(((G1PAT >> (i)) & 1) ? 2 : 0); } while (0)
#define SIX() do { PR1(0); EV(0, 1, 6, true, true); PR1(1); OD(1, 2, 6, true, true); PR1(2); EV(2, 0, 6, true, true); PR1(3); OD(0, 1, 6, true, true); PR1(4); EV(1, 2, 6, true, true); PR1(5); OD(2, 0, 6, true, true); } while (0)
  for (int it = 0; it < 4; ++it) { SIX(); STAMP(4 + it); }
  EV(0, 1, 6, true, true); OD(1, 2, 6, true, true); EV(2, 0, 6, true, true); OD(0, 1, 6, true, true); EV(1, 2, 6, true, true);
  OD(2, 0, 6, true, false); EV(0, 1, 0, true, false); OD(1, 2, 0, false, false);
  STAMP(1);
  __builtin_amdgcn_s_setprio(PRIO_GE);
  const int le = lane_id(), ge = le >> 4;
  float r2[4];
#pragma unroll
  for (int m = 0; m < 4; ++m) r2[m] = *(const LAS float*)(lds + L_R2 + (64 * wm + 16 * m + (le & 15)) * 4);
  const __amdgpu_buffer_rsrc_t rH = mkrsrc(ws + WS_H, 112 * 128 * 1024);
  const int cch = ((ge & 1) << 1) | (ge >> 1);
  unsigned last[4][2];
  float c1v = -2.0f * 0.7978845608028654f * 1.4426950408889634f; PIN(c1v);
#pragma unroll
  for (int n = 0; n < 7; ++n) {
    const int nb = n < 6 ? 6 * wn + n : 12 + wn;
    const u32x2 braw = *(const LAS u32x2*)(lds + L_B1 + (16 * nb + 4 * ge) * 2);
    const f32x4 bb = {lo2f(braw[0]), hi2f(braw[0]), lo2f(braw[1]), hi2f(braw[1])};
#pragma unroll
    for (int m = 0; m < 4; ++m) acc[m][n] = gelu4(acc[m][n], r2[m], bb, c1v);
    if (n < 6) { if (n & 1) {
#pragma unroll
        for (int m = 0; m < 4; ++m) {
          const u32x4 ch = chunk_of(cvtpk(acc[m][n - 1][0], acc[m][n - 1][1]), cvtpk(acc[m][n - 1][2], acc[m][n - 1][3]), cvtpk(acc[m][n][0], acc[m][n][1]), cvtpk(acc[m][n][2], acc[m][n][3]));
          st_wt16(rH, ((7 * j + 3 * wn + (n >> 1)) * 128 + 8 * p + 4 * wm + m) * 1024 + cch * 256 + (le & 15) * 16, ch);
        } }
    } else {
#pragma unroll
      for (int m = 0; m < 4; ++m) { last[m][0] = cvtpk(acc[m][n][0], acc[m][n][1]); last[m][1] = cvtpk(acc[m][n][2], acc[m][n][3]); }
    }
  }
#pragma unroll
  for (int m = 0; m < 4; m += 2) {
    const u32x4 ch = chunk_of(last[m][0], last[m][1], last[m + 1][0], last[m + 1][1]);
    st_wt16(rH, ((7 * j + 6) * 128 + 8 * p + 4 * wm + m + (ge & 1)) * 1024 + (2 * wn + (ge >> 1)) * 256 + (le & 15) * 16, ch);
  }
  STAMP(2);
  __builtin_amdgcn_s_setprio(PRIO_G2);
  asm volatile("s_waitcnt vmcnt(0)\n\ts_barrier" ::: "memory");
  if (w == 0 && lane_id() == 0) signal(ws, FL_H + p * 16 + j, tag);
  STAMP(3);
#undef RDX
#undef RDW
#undef EV
#undef OD
}

DI void role_w2(int gi, const u16* __restrict__ w2, const u16* __restrict__ wo, const u16* __restrict__ b2, const u16* __restrict__ x, u16* __restrict__ out, char* __restrict__ ws, LAS char* lds, const int w, const u64 tag) {
  const int l = lane_id();
  const int xcd = gi & 7, idx = gi >> 3, kh = xcd & 1, p = (xcd >> 1) * 4 + (idx & 3), nt = idx >> 2;
  const int partner = gi ^ 1;
  const int g = l >> 4, wm = w >> 1, wn = w & 1; const int stamp_base = 512; (void)stamp_base;
  const unsigned lds0 = (unsigned)(uintptr_t)lds;
  constexpr int SLOT = 16384;
#define AINC_ 131072
#define WINC_ 65536
  const __amdgpu_buffer_rsrc_t rA = mkrsrc(ws + WS_H, 144 * 128 * 1024);
  __amdgpu_buffer_rsrc_t rW = mkrsrc((const char*)w2 - 1024, F_ * D_ * 2 + 1024);
  const unsigned vlin = l * 16;
  const unsigned vw0 = wdma_off(l, w, 2048) + nt * 256 + w * 8 * 2048 + 1024, vw1 = vw0 + 128 - 1024;
  if (w == 0) await(ws, l < 8 ? FL_H + p * 16 + 8 * kh + l : -1, tag);
  asm volatile("s_barrier" ::: "memory");
  STAMP(0);
  unsigned aso = (56 * kh * 128 + 8 * p + 2 * w) * 1024 - AINC_, wso = 56 * kh * 65536 - WINC_;
  const unsigned ma = lds0 + w * 2048, mw = lds0 + 8192 + w * 2048;
#define AINC 131072
#define WINC 65536
#define PC2(SL, i) do { if constexpr ((i) == 0) dma1a_c<(SL) * SLOT, AINC>(ma, vlin, rA, aso); else if constexpr ((i) == 1) dma1b_c<1024>(vlin, rA, aso); \
    else if constexpr ((i) == 2) dma1a<(SL) * SLOT, WINC>(mw, vw0, rW, wso); else dma1b<1024>(vw1, rW, wso); } while (0)
#define STG2(SL) do { PC2(SL, 0); PC2(SL, 1); PC2(SL, 2); PC2(SL, 3); } while (0)
  STG2(0); STG2(1); STG2(2); STG2(3); STG2(4);
  f32x4 acc[4][4];
#pragma unroll
  for (int m = 0; m < 4; ++m)
#pragma unroll
    for (int n = 0; n < 4; ++n) acc[m][n] = (f32x4){0.f, 0.f, 0.f, 0.f};
  const LAS char* xrd = lds + 4 * wm * 1024 + l * 16;
  const LAS char* wrb[4];
#pragma unroll
  for (int i = 0; i < 4; ++i) wrb[i] = lds + 8192 + (g * 2 + wn) * 1024 + wrd_off(l, i);
  bf16x8 xa[4], wa[4], xb[4], wb[4];
#define RDX(X, m, so) X[m] = ldsfrag(xrd + (so) + (m) * 1024)
#define RDW(W, n, so) W[n] = ldstr(wrb[n] + (so), wrb[n] + (so) + 512)
#define M2(X, W, m, n) acc[m][n] = MFMA16(W[n], X[m], acc[m][n])
#define STEP2(X, W, XN, WN, RD, RS, DMA, DS) do { \
    M2(X, W, 0, 0); if (RD) RDW(WN, 0, (RS) * SLOT); SB(); M2(X, W, 1, 0); if (RD) RDX(XN, 0, (RS) * SLOT); SB(); M2(X, W, 2, 0); if (RD) RDX(XN, 1, (RS) * SLOT); SB(); M2(X, W, 3, 0); if (DMA) PC2(DS, 0); SB(); \
    M2(X, W, 0, 1); if (RD) RDW(WN, 1, (RS) * SLOT); SB(); M2(X, W, 1, 1); if (RD) RDX(XN, 2, (RS) * SLOT); SB(); M2(X, W, 2, 1); if (RD) RDX(XN, 3, (RS) * SLOT); SB(); M2(X, W, 3, 1); if (DMA) PC2(DS, 1); SB(); \
    M2(X, W, 0, 2); if (RD) RDW(WN, 2, (RS) * SLOT); SB(); M2(X, W, 1, 2); if (RD) RDW(WN, 3, (RS) * SLOT); SB(); M2(X, W, 2, 2); SB(); M2(X, W, 3, 2); if (DMA) PC2(DS, 2); SB(); \
    M2(X, W, 0, 3); SB(); M2(X, W, 1, 3); SB(); M2(X, W, 2, 3); SB(); M2(X, W, 3, 3); if (DMA) PC2(DS, 3); SB(); } while (0)
  WAIT_BAR(16);
#pragma unroll
  for (int i = 0; i < 4; ++i) { RDX(xa, i, 0); RDW(wa, i, 0); }
#define EV(SC, SN, W_, RD, DMA) do { WAIT_BAR(W_); SB(); STEP2(xa, wa, xb, wb, RD, SN, DMA, SC); } while (0)
#define OD(SC, SN, W_, RD, DMA) do { WAIT_BAR(W_); SB(); STEP2(xb, wb, xa, wa, RD, SN, DMA, SC); } while (0)
#define PR2(i) do { if constexpr ((i) == 0 || (((G2PAT >> (i)) & 1) != ((G2PAT >> ((i) - 1 < 0 ? 0 : (i) - 1)) & 1))) __builtin_amdgcn_s_setprio(((G2PAT >> (i)) & 1) ? 2 : 0); } while (0)
  for (int it = 0; it < 6; ++it) {
    STAMP(4 + it);
    PR2(0);
    EV(0, 1, 12, true, true);
    if (it == 5) {
      if (w == 0) await(ws, l < 8 ? FL_O + (2 * p + (l >> 2)) * 8 + 4 * kh + (l & 3) : -1, tag);
      rW = mkrsrc((const char*)wo - 1024, D_ * D_ * 2 + 1024);
      aso = ((112 + 16 * kh) * 128 + 8 * p + 2 * w) * 1024 - 131072; wso = 16 * kh * 65536 - 65536;
    }
    PR2(1); OD(1, 2, 12, true, true); PR2(2); EV(2, 3, 12, true, true); PR2(3); OD(3, 4, 12, true, true); PR2(4); EV(4, 0, 12, true, true);
    PR2(5); OD(0, 1, 12, true, true); PR2(6); EV(1, 2, 12, true, true); PR2(7); OD(2, 3, 12, true, true); PR2(8); EV(3, 4, 12, true, true); PR2(9); OD(4, 0, 12, true, true);
  }
  EV(0, 1, 12, true, true); OD(1, 2, 12, true, true); EV(2, 3, 12, true, true); OD(3, 4, 12, true, true); EV(4, 0, 12, true, true); OD(0, 1, 12, true, true); EV(1, 2, 12, true, true);
  OD(2, 3, 12, true, false); EV(3, 4, 8, true, false); OD(4, 0, 4, true, false); EV(0, 1, 0, true, false); OD(1, 2, 0, false, false);
  STAMP(1);
  if (kh) {
#pragma unroll
    for (int n = 0; n < 4; ++n) { f32x4 t = acc[0][n]; acc[0][n] = acc[2][n]; acc[2][n] = t; t = acc[1][n]; acc[1][n] = acc[3][n]; acc[3][n] = t; }
#pragma unroll
    for (int n = 0; n < 4; ++n) { PIN(acc[0][n]); PIN(acc[1][n]); PIN(acc[2][n]); PIN(acc[3][n]); }
  }
  const int token0 = 128 * p + 64 * wm + 32 * kh + (l & 15);
  u32x2 xres[2][4], bres[4];
#pragma unroll
  for (int n = 0; n < 4; ++n) {
    const int n0 = 128 * nt + 64 * wn + 16 * n + 4 * g;
    bres[n] = *(const u32x2*)(b2 + n0);
#pragma unroll
    for (int m = 0; m < 2; ++m) xres[m][n] = *(const u32x2*)(x + (size_t)(token0 + 16 * m) * D_ + n0);
  }
  {
    const __amdgpu_buffer_rsrc_t rP = mkrsrc(ws + WS_PART + (size_t)gi * 32768, 32768);
#pragma unroll
    for (int m = 0; m < 2; ++m)
#pragma unroll
      for (int n = 0; n < 4; ++n) st_wt16(rP, ((w * 2 + m) * 4 + n) * 1024 + l * 16, __builtin_bit_cast(u32x4, acc[2 + m][n]));
    asm volatile("s_waitcnt vmcnt(0)\n\ts_barrier" ::: "memory");
    if (w == 0) { if (lane_id() == 0) signal(ws, FL_X + gi, tag); await(ws, FL_X + partner, tag); }
    asm volatile("s_barrier" ::: "memory");
  }
  STAMP(2);
  const __amdgpu_buffer_rsrc_t rPP = mkrsrc(ws + WS_PART + (size_t)partner * 32768, 32768);
#pragma unroll
  for (int m = 0; m < 2; ++m) {
    unsigned d[4][2];
#pragma unroll
    for (int n = 0; n < 4; ++n) {
      const f32x4 pv = __builtin_bit_cast(f32x4, __builtin_amdgcn_raw_buffer_load_b128(rPP, ((w * 2 + m) * 4 + n) * 1024 + l * 16, 0, 16));
      const f32x4 a4 = acc[m][n];
      d[n][0] = cvtpk(a4[0] + pv[0] + lo2f(bres[n][0]) + lo2f(xres[m][n][0]), a4[1] + pv[1] + hi2f(bres[n][0]) + hi2f(xres[m][n][0]));
      d[n][1] = cvtpk(a4[2] + pv[2] + lo2f(bres[n][1]) + lo2f(xres[m][n][1]), a4[3] + pv[3] + hi2f(bres[n][1]) + hi2f(xres[m][n][1]));
    }
    const int cch = ((g & 1) << 1) | (g >> 1);
#pragma unroll
    for (int n2 = 0; n2 < 2; ++n2) {
      const u32x4 ch = chunk_of(d[2 * n2][0], d[2 * n2][1], d[2 * n2 + 1][0], d[2 * n2 + 1][1]);
      *(u32x4*)(out + (size_t)(token0 + 16 * m) * D_ + 128 * nt + 64 * wn + 32 * n2 + 8 * cch) = ch;
    }
  }
  STAMP(3);
#undef RDX
#undef RDW
}

constexpr int LDS_BYTES = 81920;
#define MODE 0
__global__ __launch_bounds__(256, 2) void k_main(const u16* __restrict__ x, const u16* __restrict__ wq, const u16* __restrict__ wk, const u16* __restrict__ wv, const u16* __restrict__ wo,
                                                  const u16* __restrict__ w1, const u16* __restrict__ b1, const u16* __restrict__ w2, const u16* __restrict__ b2, u16* __restrict__ out, char* __restrict__ ws) {
  extern __shared__ __attribute__((aligned(16))) char lds_[];
  LAS char* lds = (LAS char*)lds_;
  const int w = __builtin_amdgcn_readfirstlane(threadIdx.x >> 6);
  u64 tag = 0;
  if (w == 0) {
    const u64 e = __hip_atomic_load(flagp(ws, FL_E), __ATOMIC_RELAXED, __HIP_MEMORY_SCOPE_AGENT);
    tag = (((u64)(unsigned)__builtin_amdgcn_readfirstlane((unsigned)(e >> 32))) << 32 | (unsigned)__builtin_amdgcn_readfirstlane((unsigned)e)) + 1;
    if (lane_id() == 0) signal(ws, FL_RD + blockIdx.x, tag);
  }
  if ((blockIdx.x >> 8) == 0) {
    __builtin_amdgcn_s_setprio(PRIO_Q);
    phase0(blockIdx.x, x, wk, wv, ws, lds, w, tag);
    role_attn(blockIdx.x, wq, w2, wo, ws, lds, w, tag);
  } else {
    __builtin_amdgcn_s_setprio(PRIO_G1);
    role_w1(blockIdx.x & 255, wq, w1, b1, ws, lds, w, tag);
    role_w2(blockIdx.x & 255, w2, wo, b2, x, out, ws, lds, w, tag);
    if (blockIdx.x == 256 && w == 0) {
      const int l = lane_id();
#pragma unroll 1
      for (int i = 0; i < 8; ++i) await(ws, FL_RD + 64 * i + l, tag);
      if (l == 0) signal(ws, FL_E, tag);
    }
  }
}

extern "C" void kernel_launch(void* const* d_in, const int* in_sizes, int n_in, void* d_out, int out_size, void* d_ws, size_t ws_size, hipStream_t stream) {
  const u16* x = (const u16*)d_in[0]; const u16* wq = (const u16*)d_in[1]; const u16* wk = (const u16*)d_in[2];
  const u16* wv = (const u16*)d_in[3]; const u16* wo = (const u16*)d_in[4];
  const u16* w1 = (const u16*)d_in[6]; const u16* b1 = (const u16*)d_in[7]; const u16* w2 = (const u16*)d_in[8]; const u16* b2 = (const u16*)d_in[9];
  (void)hipFuncSetAttribute((const void*)k_main, hipFuncAttributeMaxDynamicSharedMemorySize, LDS_BYTES);
  k_main<<<512, 256, LDS_BYTES, stream>>>(x, wq, wk, wv, wo, w1, b1, w2, b2, (u16*)d_out, (char*)d_ws);
}
```

```cpp
#include <hip/hip_runtime.h>
#include <stdint.h>

typedef unsigned short u16;
typedef __bf16 bf16x8 __attribute__((ext_vector_type(8)));
typedef __bf16 bf16x2 __attribute__((ext_vector_type(2)));
typedef short s16x8 __attribute__((ext_vector_type(8)));
typedef float f32x4 __attribute__((ext_vector_type(4)));
typedef float f32x2 __attribute__((ext_vector_type(2)));
typedef unsigned u32x4 __attribute__((ext_vector_type(4)));
typedef unsigned u32x2 __attribute__((ext_vector_type(2)));
#define LAS __attribute__((address_space(3)))
#define GAS __attribute__((address_space(1)))
#define DI __device__ __forceinline__
#define SB() __builtin_amdgcn_sched_barrier(0)
#define PIN(x) asm volatile("" : "+v"(x))
#define MFMA16(a, b, c) __builtin_amdgcn_mfma_f32_16x16x32_bf16((a), (b), (c), 0, 0, 0)
#define BF(x) __builtin_bit_cast(bf16x8, x)

#define PF0ROWS 1024
constexpr int S_ = 2048, D_ = 1024, F_ = 3584;
constexpr size_t WS_FLAGS = 0;
constexpr size_t WS_R2   = 131072;
constexpr size_t WS_KV   = WS_R2 + 8192;
constexpr size_t WS_XN   = WS_KV + 262144;
constexpr size_t WS_H    = WS_XN + 4194304;
constexpr size_t WS_O    = WS_H + (size_t)112 * 128 * 1024;
constexpr size_t WS_PART = WS_O + 4194304;
constexpr size_t WS_STAMP = WS_PART + (size_t)2 * 256 * 32768;

constexpr float QSCALE = 0.17677669529663687f * 1.4426950408889634f;

DI int lane_id() { int l; asm volatile("v_mbcnt_lo_u32_b32 %0, -1, 0\n\tv_mbcnt_hi_u32_b32 %0, -1, %0" : "=v"(l)); return l; }
DI float bf2f(u16 v) { return __uint_as_float(((unsigned)v) << 16); }
DI unsigned cvtpk(float lo, float hi) { f32x2 v = {lo, hi}; bf16x2 b = __builtin_convertvector(v, bf16x2); return __builtin_bit_cast(unsigned, b); }
DI float lo2f(unsigned u) { return __uint_as_float(u << 16); }
DI float hi2f(unsigned u) { return __uint_as_float(u & 0xffff0000u); }
DI __amdgpu_buffer_rsrc_t mkrsrc(const void* p, unsigned bytes) { return __builtin_amdgcn_make_buffer_rsrc((void*)p, 0, bytes, 0x00020000); }
DI void dma16(unsigned lds_addr, unsigned voff, __amdgpu_buffer_rsrc_t rsrc, unsigned soff) {
  asm volatile("s_mov_b32 m0, %0\n\ts_nop 0\n\tbuffer_load_dwordx4 %1, %2, %3 offen lds" :: "s"(lds_addr), "v"(voff), "s"(rsrc), "s"(soff));
}
template <int IMM, int INC> DI void dma2(unsigned mbase, unsigned v0, unsigned v1, __amdgpu_buffer_rsrc_t rsrc, unsigned& soff) {
  asm volatile("s_add_u32 m0, %3, %5\n\ts_add_u32 %0, %0, %6\n\tbuffer_load_dwordx4 %1, %4, %0 offen lds\n\tbuffer_load_dwordx4 %2, %4, %0 offen offset:1024 lds"
               : "+s"(soff) : "v"(v0), "v"(v1), "s"(mbase), "s"(rsrc), "i"(IMM), "i"(INC) : "scc");
}
template <int IMM, int INC> DI void dma1a(unsigned mbase, unsigned v0, __amdgpu_buffer_rsrc_t rsrc, unsigned& soff) {
  asm volatile("s_add_u32 m0, %2, %4\n\ts_add_u32 %0, %0, %5\n\tbuffer_load_dwordx4 %1, %3, %0 offen lds" : "+s"(soff) : "v"(v0), "s"(mbase), "s"(rsrc), "i"(IMM), "i"(INC) : "scc");
}
template <int OFF> DI void dma1b(unsigned v0, __amdgpu_buffer_rsrc_t rsrc, unsigned soff) {
  asm volatile("buffer_load_dwordx4 %0, %1, %2 offen offset:%3 lds" :: "v"(v0), "s"(rsrc), "s"(soff), "i"(OFF));
}
template <int IMM, int INC> DI void dma1a_c(unsigned mbase, unsigned v0, __amdgpu_buffer_rsrc_t rsrc, unsigned& soff) {
  asm volatile("s_add_u32 m0, %2, %4\n\ts_add_u32 %0, %0, %5\n\tbuffer_load_dwordx4 %1, %3, %0 offen sc1 lds" : "+s"(soff) : "v"(v0), "s"(mbase), "s"(rsrc), "i"(IMM), "i"(INC) : "scc");
}
template <int OFF> DI void dma1b_c(unsigned v0, __amdgpu_buffer_rsrc_t rsrc, unsigned soff) {
  asm volatile("buffer_load_dwordx4 %0, %1, %2 offen offset:%3 sc1 lds" :: "v"(v0), "s"(rsrc), "s"(soff), "i"(OFF));
}
DI void dma4(unsigned lds_addr, unsigned voff, __amdgpu_buffer_rsrc_t rsrc, unsigned soff) {
  asm volatile("s_mov_b32 m0, %0\n\ts_nop 0\n\tbuffer_load_dword %1, %2, %3 offen lds" :: "s"(lds_addr), "v"(voff), "s"(rsrc), "s"(soff));
}
DI void dma4_c(unsigned lds_addr, unsigned voff, __amdgpu_buffer_rsrc_t rsrc, unsigned soff) {
  asm volatile("s_mov_b32 m0, %0\n\ts_nop 0\n\tbuffer_load_dword %1, %2, %3 offen sc1 lds" :: "s"(lds_addr), "v"(voff), "s"(rsrc), "s"(soff));
}
template <int ADD> DI void dma4_ahead(unsigned lds_addr, unsigned voff, __amdgpu_buffer_rsrc_t rsrc, unsigned soff) {
  unsigned t;
  asm volatile("s_mov_b32 m0, %1\n\ts_add_u32 %0, %4, %5\n\tbuffer_load_dword %2, %3, %0 offen lds" : "=&s"(t) : "s"(lds_addr), "v"(voff), "s"(rsrc), "s"(soff), "i"(ADD) : "scc");
}
#define WAIT_BAR(N) asm volatile("s_waitcnt vmcnt(" #N ") lgkmcnt(0)\n\ts_barrier" ::: "memory")
DI bf16x8 ldsfrag(const LAS char* p) { return *(const LAS bf16x8*)p; }
typedef short v4i16_t __attribute__((ext_vector_type(4)));
DI bf16x8 ldstr(const LAS char* p0, const LAS char* p1) {
  v4i16_t a = __builtin_amdgcn_ds_read_tr16_b64_v4i16((LAS v4i16_t*)p0);
  v4i16_t b = __builtin_amdgcn_ds_read_tr16_b64_v4i16((LAS v4i16_t*)p1);
  s16x8 v = __builtin_shufflevector(a, b, 0, 1, 2, 3, 4, 5, 6, 7);
  return __builtin_bit_cast(bf16x8, v);
}
DI void st_wt16(__amdgpu_buffer_rsrc_t r, unsigned voff, u32x4 v) { __builtin_amdgcn_raw_buffer_store_b128(v, r, voff, 0, 16); }
DI u32x4 chunk_of(unsigned x0, unsigned x1, unsigned y0, unsigned y1) {
  auto s0 = __builtin_amdgcn_permlane16_swap(x0, y0, false, false);
  auto s1 = __builtin_amdgcn_permlane16_swap(x1, y1, false, false);
  return (u32x4){s0[0], s1[0], s0[1], s1[1]};
}
DI unsigned wdma_off(int l, int kblk, int ldbytes) {
  const int row = l >> 3, c8 = l & 7, sw = ((row >> 1) & 1) | ((kblk & 1) << 1);
  return row * ldbytes + (((c8 >> 1) ^ sw) * 32) + (c8 & 1) * 16;
}
DI unsigned wrd_off(int l, int seg) {
  const int g = l >> 4, q = (l & 15) >> 2, sw = ((q >> 1) & 1) | ((g & 1) << 1);
  return q * 128 + ((seg ^ sw) * 32) + (l & 3) * 8;
}
DI float xsum4(float v) {
  auto a = __builtin_amdgcn_permlane16_swap(__float_as_uint(v), __float_as_uint(v), false, false); v = __uint_as_float(a[0]) + __uint_as_float(a[1]);
  auto b = __builtin_amdgcn_permlane32_swap(__float_as_uint(v), __float_as_uint(v), false, false); return __uint_as_float(b[0]) + __uint_as_float(b[1]);
}
DI float xmax4(float v) {
  auto a = __builtin_amdgcn_permlane16_swap(__float_as_uint(v), __float_as_uint(v), false, false); v = fmaxf(__uint_as_float(a[0]), __uint_as_float(a[1]));
  auto b = __builtin_amdgcn_permlane32_swap(__float_as_uint(v), __float_as_uint(v), false, false); return fmaxf(__uint_as_float(b[0]), __uint_as_float(b[1]));
}
#define STAMP(i)

typedef unsigned long long u64;
constexpr int FL_E = 0, FL_RD = 16, FL_XN = 528, FL_KV = 784, FL_H = 1040, FL_O = 1296, FL_X = 1552;
DI GAS u64* flagp(char* ws, int i) { return (GAS u64*)(ws + WS_FLAGS + (size_t)i * 64); }
DI void signal(char* ws, int i, u64 tag) { __hip_atomic_store(flagp(ws, i), tag, __ATOMIC_RELAXED, __HIP_MEMORY_SCOPE_AGENT); }
DI void await(char* ws, int idx, u64 tag) {
  GAS u64* f = flagp(ws, idx < 0 ? FL_E : idx);
  for (unsigned spins = 0; spins < (1u << 20); ++spins) {
    const u64 v = __hip_atomic_load(f, __ATOMIC_RELAXED, __HIP_MEMORY_SCOPE_AGENT);
    if (__all(idx < 0 || v == tag)) break;
    __builtin_amdgcn_s_sleep(2);
  }
  asm volatile("" ::: "memory");
}

DI void phase0(int a, const u16* __restrict__ x, const u16* __restrict__ wk, const u16* __restrict__ wv, char* __restrict__ ws, LAS char* lds, const int w, const u64 tag) {
  const int l = lane_id(), tid = w * 64 + l;
  LAS float* part = (LAS float*)(lds + 65536);
  LAS float* fin = (LAS float*)(lds + 65536 + 8192);
  LAS float* red = (LAS float*)(lds + 65536 + 8192 + 2048);
  const int rb = a & 127, hf = a >> 7;
  const int r = l & 15, c = l >> 4; const int stamp_base = 768; (void)stamp_base;
  STAMP(0);
  u32x4 xv[8];
  { const u16* xr = x + (size_t)(rb * 16 + r) * D_ + w * 256 + c * 8;
#define XL(i) asm volatile("global_load_dwordx4 %0, %1, off offset:" #i "*64" : "=&v"(xv[i]) : "v"(xr))
    XL(0); XL(1); XL(2); XL(3); XL(4); XL(5); XL(6); XL(7); }
  const unsigned lds0 = (unsigned)(uintptr_t)lds;
  {
    __amdgpu_buffer_rsrc_t rw = mkrsrc(hf ? wv : wk, 65536);
#pragma unroll
    for (int i = 0; i < 16; ++i) dma16(lds0 + w * 16384 + i * 1024, l * 16, rw, w * 16384 + i * 1024);
  }
  asm volatile("s_waitcnt vmcnt(16)" : "+v"(xv[0]), "+v"(xv[1]), "+v"(xv[2]), "+v"(xv[3]), "+v"(xv[4]), "+v"(xv[5]), "+v"(xv[6]), "+v"(xv[7]));
  STAMP(1);
  float s = 0.f;
#pragma unroll
  for (int i = 0; i < 8; ++i)
#pragma unroll
    for (int j = 0; j < 4; ++j) s += lo2f(xv[i][j]) + hi2f(xv[i][j]);
  s = xsum4(s);
  if (c == 0) red[w * 16 + r] = s;
  asm volatile("s_waitcnt lgkmcnt(0)\n\ts_barrier" ::: "memory");
  const float mean = ((red[r] + red[16 + r]) + (red[32 + r] + red[48 + r])) * (1.0f / D_);
  float q = 0.f;
#pragma unroll
  for (int i = 0; i < 8; ++i)
#pragma unroll
    for (int j = 0; j < 4; ++j) { const float u = lo2f(xv[i][j]) - mean, v = hi2f(xv[i][j]) - mean; q += u * u + v * v; }
  q = xsum4(q);
  if (c == 0) red[64 + w * 16 + r] = q;
  asm volatile("s_waitcnt lgkmcnt(0)\n\ts_barrier" ::: "memory");
  const float var = ((red[64 + r] + red[80 + r]) + (red[96 + r] + red[112 + r])) * (1.0f / D_);
  const float rstd = 1.0f / sqrtf(var + 1e-5f);
#pragma unroll
  for (int i = 0; i < 8; ++i)
#pragma unroll
    for (int j = 0; j < 4; ++j) xv[i][j] = cvtpk((lo2f(xv[i][j]) - mean) * rstd, (hi2f(xv[i][j]) - mean) * rstd);
  const __amdgpu_buffer_rsrc_t rXN = mkrsrc(ws + WS_XN, 4194304);
#pragma unroll
  for (int i = 0; i < 4; ++i) st_wt16(rXN, ((8 * w + 2 * i + hf) * 128 + rb) * 1024 + l * 16, hf ? xv[2 * i + 1] : xv[2 * i]);
  if (hf == 0 && w == 0 && c == 0) __hip_atomic_store((GAS float*)(ws + WS_R2) + rb * 16 + r, 1.0f / sqrtf(var * rstd * rstd + 1e-5f), __ATOMIC_RELAXED, __HIP_MEMORY_SCOPE_AGENT);
  asm volatile("s_waitcnt vmcnt(4)" ::: "memory");
  f32x4 acc[2] = {{0.f, 0.f, 0.f, 0.f}, {0.f, 0.f, 0.f, 0.f}};
  const LAS char* wb = (const LAS char*)lds + w * 16384 + (8 * c + ((l & 15) >> 2)) * 64 + (l & 3) * 8;
#pragma unroll
  for (int i = 0; i < 8; ++i)
#pragma unroll
    for (int b = 0; b < 2; ++b) {
      bf16x8 wf = ldstr(wb + i * 2048 + b * 32, wb + i * 2048 + b * 32 + 256);
      acc[b] = MFMA16(BF(xv[i]), wf, acc[b]);
    }
#pragma unroll
  for (int b = 0; b < 2; ++b)
#pragma unroll
    for (int j = 0; j < 4; ++j) part[(w * 16 + 4 * c + j) * 32 + 16 * b + r] = acc[b][j];
  asm volatile("s_waitcnt vmcnt(0) lgkmcnt(0)\n\ts_barrier" ::: "memory");
  if (tid == 0) signal(ws, FL_XN + a, tag);
  STAMP(2);
#pragma unroll
  for (int u = 0; u < 2; ++u) { const int e = tid + 256 * u; fin[e] = (part[e] + part[512 + e]) + (part[1024 + e] + part[1536 + e]); }
  asm volatile("s_waitcnt lgkmcnt(0)\n\ts_barrier" ::: "memory");
  if (w == 0) {
    float o[8];
    unsigned dst = (rb >> 1) * 4096;
    const int half = rb & 1;
    if (hf == 0) { const int kb = l >> 5, g = (l >> 3) & 3, rr = l & 7;
      const int row = 8 * half + rr, key = 8 * (row >> 2) + 4 * kb + (row & 3) - 16 * half;
#pragma unroll
      for (int j = 0; j < 8; ++j) o[j] = fin[key * 32 + 16 * (j >> 2) + 4 * g + (j & 3)];
      dst += kb * 1024 + (g * 16 + row) * 16;
    } else { const int db = l >> 5, gl = (l >> 4) & 1, rr = l & 15;
#pragma unroll
      for (int j = 0; j < 8; ++j) o[j] = fin[(8 * gl + j) * 32 + 16 * db + rr];
      dst += 2048 + db * 1024 + ((2 * half + gl) * 16 + rr) * 16;
    }
    st_wt16(mkrsrc(ws + WS_KV, 262144), dst, (u32x4){cvtpk(o[0], o[1]), cvtpk(o[2], o[3]), cvtpk(o[4], o[5]), cvtpk(o[6], o[7])});
    asm volatile("s_waitcnt vmcnt(0)" ::: "memory");
    if (l == 0) signal(ws, FL_KV + a, tag);
  }
  STAMP(3);
}

#define MERGED 1
#define PRIO_A 1
#define G1PAT 0
#define G2PAT 0
#define PFSLEEP 80
#define GDELAY 0
#define PFROWS 256
#define PRIO_Q 1
#define PRIO_G1 0
#define PRIO_GE 2
#define PRIO_G2 0
#define LSUM_MFMA 1
DI float fadd_s(float a, float b) { float r; asm("s_nop 0\n\tv_add_f32_e32 %0, %1, %2" : "=v"(r) : "v"(a), "v"(b)); return r; }

DI void attn_slow(const char* __restrict__ kv, bf16x8 qf, f32x4& o0, f32x4& o1) {
  const int l = lane_id();
  const __amdgpu_buffer_rsrc_t rk = mkrsrc(kv, 262144);
  o0 = (f32x4){0.f, 0.f, 0.f, 0.f}; o1 = o0;
  float m = -3.0e38f, ls = 0.f;
#pragma unroll 1
  for (int t = 0; t < 64; ++t) {
    const int p = t * 4096 + l * 16;
    const bf16x8 k0 = BF(__builtin_amdgcn_raw_buffer_load_b128(rk, p, 0, 16)), k1 = BF(__builtin_amdgcn_raw_buffer_load_b128(rk, p + 1024, 0, 16)),
                 v0 = BF(__builtin_amdgcn_raw_buffer_load_b128(rk, p + 2048, 0, 16)), v1 = BF(__builtin_amdgcn_raw_buffer_load_b128(rk, p + 3072, 0, 16));
    const f32x4 z = {0.f, 0.f, 0.f, 0.f};
    f32x4 s0 = MFMA16(k0, qf, z), s1 = MFMA16(k1, qf, z);
    float mx = fmaxf(fmaxf(fmaxf(s0[0], s0[1]), fmaxf(s0[2], s0[3])), fmaxf(fmaxf(s1[0], s1[1]), fmaxf(s1[2], s1[3])));
    mx = fmaxf(mx, __shfl_xor(mx, 16)); mx = fmaxf(mx, __shfl_xor(mx, 32));
    const float mn = fmaxf(m, mx), al = __builtin_amdgcn_exp2f(m - mn);
    m = mn; float rs = 0.f;
#pragma unroll
    for (int i = 0; i < 4; ++i) { s0[i] = __builtin_amdgcn_exp2f(s0[i] - mn); s1[i] = __builtin_amdgcn_exp2f(s1[i] - mn); rs += s0[i] + s1[i]; o0[i] *= al; o1[i] *= al; }
    rs += __shfl_xor(rs, 16); rs += __shfl_xor(rs, 32); ls = ls * al + rs;
    const u32x4 pp = {cvtpk(s0[0], s0[1]), cvtpk(s0[2], s0[3]), cvtpk(s1[0], s1[1]), cvtpk(s1[2], s1[3])};
    o0 = MFMA16(v0, BF(pp), o0); o1 = MFMA16(v1, BF(pp), o1);
  }
#pragma unroll
  for (int i = 0; i < 4; ++i) { o0[i] /= ls; o1[i] /= ls; }
}

DI void role_attn(int a, const u16* __restrict__ wq, const u16* __restrict__ w2, const u16* __restrict__ wo, char* __restrict__ ws, LAS char* lds, const int w, const u64 tag) {
  const int l = lane_id();
#define AMAP 2
  constexpr int NTB = 32 / AMAP;
  const int xcd = a & 7, idx = a >> 3, tb = (xcd / (8 / AMAP)) * NTB + (idx % NTB), hg = (xcd % (8 / AMAP)) * AMAP + idx / NTB;
  const int g = l >> 4; const int stamp_base = 0; (void)stamp_base;
  const unsigned lds0 = (unsigned)(uintptr_t)lds;
  constexpr int SLOT = 24576;
  const __amdgpu_buffer_rsrc_t rX = mkrsrc(ws + WS_XN, 4194304), rKV = mkrsrc(ws + WS_KV, 262144);
  const unsigned vlin = l * 16;
  constexpr int QS = 12288;
  const __amdgpu_buffer_rsrc_t rWq = mkrsrc((const char*)wq - 1024, D_ * D_ * 2 + 1024);
  const unsigned vq0 = wdma_off(l, w, 2048) + hg * 256 + w * 8 * 2048 + 1024, vq1 = vq0 + 128 - 1024;
  unsigned xso = (4 * tb + w) * 1024 - 131072, qso = 0u - 65536;
  const unsigned mx = lds0 + w * 1024, mq = lds0 + 4096 + w * 2048;
#define PCQ(SL, i) do { if constexpr ((i) == 0) dma1a_c<(SL) * QS, 131072>(mx, vlin, rX, xso); else if constexpr ((i) == 1) dma1a<(SL) * QS, 65536>(mq, vq0, rWq, qso); else dma1b<1024>(vq1, rWq, qso); } while (0)
#define STGQ(SL) do { PCQ(SL, 0); PCQ(SL, 1); PCQ(SL, 2); } while (0)
  if (w == 0) await(ws, l < 8 ? FL_XN + 4 * tb + (l & 3) + 128 * (l >> 2) : -1, tag);
  asm volatile("s_waitcnt lgkmcnt(0)\n\ts_barrier" ::: "memory");
  STAMP(0);
  constexpr int NPL = 64 / (4 * NTB);
  const int pl = ((idx % NTB) * 4 + w) * NPL + l;
  const unsigned vpq = l < NPL ? (pl >> 1) * 2048 + hg * 256 + (pl & 1) * 128 + 1024 : 0x80000000u;
  const unsigned mpq = lds0 + 6 * QS;
  STGQ(0); STGQ(1); STGQ(2); STGQ(3); STGQ(4); STGQ(5);
  dma4_ahead<1 * 65536>(mpq, vpq, rWq, qso); dma4_ahead<2 * 65536>(mpq, vpq, rWq, qso); dma4_ahead<3 * 65536>(mpq, vpq, rWq, qso);
  dma4_ahead<4 * 65536>(mpq, vpq, rWq, qso); dma4_ahead<5 * 65536>(mpq, vpq, rWq, qso); dma4_ahead<6 * 65536>(mpq, vpq, rWq, qso);
  f32x4 qacc[2][4];
#pragma unroll
  for (int i = 0; i < 2; ++i)
#pragma unroll
    for (int j = 0; j < 4; ++j) qacc[i][j] = (f32x4){0.f, 0.f, 0.f, 0.f};
  const LAS char* xrd = lds + l * 16;
  const LAS char* wrd0 = lds + 4096 + (g * 2 + (w >> 1)) * 1024 + wrd_off(l, 2 * (w & 1));
  const LAS char* wrd1 = lds + 4096 + (g * 2 + (w >> 1)) * 1024 + wrd_off(l, 2 * (w & 1) + 1);
  bf16x8 qxa[4], qwa[2], qxb[4], qwb[2];
#define QRX(X, m, SL) X[m] = ldsfrag(xrd + (SL) * QS + (m) * 1024)
#define QRW(W, n, SL) W[n] = ((n) == 0 ? ldstr(wrd0 + (SL) * QS, wrd0 + (SL) * QS + 512) : ldstr(wrd1 + (SL) * QS, wrd1 + (SL) * QS + 512))
#define QM(X, W, m, n) qacc[n][m] = MFMA16(W[n], X[m], qacc[n][m])
#define STEPQ(X, W, XN, WN, RD, RS, DMA, DS) do { \
    QM(X, W, 0, 0); if (RD) { QRW(WN, 0, RS); QRX(XN, 0, RS); } SB(); QM(X, W, 1, 0); if (RD) { QRW(WN, 1, RS); QRX(XN, 1, RS); } SB(); QM(X, W, 2, 0); if (RD) QRX(XN, 2, RS); SB(); QM(X, W, 3, 0); if (RD) QRX(XN, 3, RS); SB(); \
    QM(X, W, 0, 1); if (DMA) PCQ(DS, 0); SB(); QM(X, W, 1, 1); if (DMA) PCQ(DS, 1); SB(); QM(X, W, 2, 1); if (DMA) PCQ(DS, 2); SB(); QM(X, W, 3, 1); if (DMA) dma4_ahead<6 * 65536>(mpq, vpq, rWq, qso); SB(); } while (0)
  WAIT_BAR(21);
#pragma unroll
  for (int m = 0; m < 4; ++m) QRX(qxa, m, 0);
  QRW(qwa, 0, 0); QRW(qwa, 1, 0);
#define QEV(SC, SN, W_, RD, DMA) do { WAIT_BAR(W_); SB(); STEPQ(qxa, qwa, qxb, qwb, RD, SN, DMA, SC); } while (0)
#define QOD(SC, SN, W_, RD, DMA) do { WAIT_BAR(W_); SB(); STEPQ(qxb, qwb, qxa, qwa, RD, SN, DMA, SC); } while (0)
  QEV(0, 1, 18, true, true); QOD(1, 2, 19, true, true); QEV(2, 3, 20, true, true); QOD(3, 4, 21, true, true); QEV(4, 5, 22, true, true); QOD(5, 0, 17, true, true);
  for (int it = 0; it < 3; ++it) { QEV(0, 1, 17, true, true); QOD(1, 2, 17, true, true); QEV(2, 3, 17, true, true); QOD(3, 4, 17, true, true); QEV(4, 5, 17, true, true); QOD(5, 0, 17, true, true); }
  QEV(0, 1, 17, true, true); QOD(1, 2, 17, true, true);
  QEV(2, 3, 17, true, false); QOD(3, 4, 13, true, false); QEV(4, 5, 9, true, false); QOD(5, 0, 5, true, false);
  if (w == 0) { await(ws, FL_KV + l, tag); await(ws, FL_KV + 64 + l, tag); await(ws, FL_KV + 128 + l, tag); await(ws, FL_KV + 192 + l, tag); }
  QEV(0, 1, 0, true, false);
  unsigned kvso = w * 4096 - 16384;
  const unsigned kvm = lds0 + w * 4096;
#define STGKV(SF) do { dma1a_c<(SF) * SLOT, 16384>(kvm, vlin, rKV, kvso); dma1b_c<1024>(vlin, rKV, kvso); dma1b_c<2048>(vlin, rKV, kvso); dma1b_c<3072>(vlin, rKV, kvso); } while (0)
  STGKV(1); STGKV(2);
  {
    const unsigned line = (a * 4 + w) * 64 + l;
    dma4(lds0 + 6 * QS, line * 128, mkrsrc(w2, F_ * D_ * 2), 0);
    dma4(lds0 + 6 * QS, line * 128, mkrsrc(wo, D_ * D_ * 2), 0);
  }
  STEPQ(qxb, qwb, qxa, qwa, false, 0, false, 0);
  STAMP(1);
  __builtin_amdgcn_s_setprio(PRIO_A);
  bf16x8 qf[4];
#pragma unroll
  for (int m = 0; m < 4; ++m)
    qf[m] = BF(((u32x4){cvtpk(qacc[0][m][0] * QSCALE, qacc[0][m][1] * QSCALE), cvtpk(qacc[0][m][2] * QSCALE, qacc[0][m][3] * QSCALE),
                        cvtpk(qacc[1][m][0] * QSCALE, qacc[1][m][1] * QSCALE), cvtpk(qacc[1][m][2] * QSCALE, qacc[1][m][3] * QSCALE)}));
  WAIT_BAR(6);
  const LAS char* kvb = lds + l * 16;
  f32x4 negm[4], S[4][2], O[4][2];
  bf16x8 k0, k1;
  {
    const LAS char* p = kvb + 1 * SLOT;
    k0 = ldsfrag(p); k1 = ldsfrag(p + 1024);
    const f32x4 z = {0.f, 0.f, 0.f, 0.f};
#pragma unroll
    for (int m = 0; m < 4; ++m) {
      const f32x4 s0 = MFMA16(k0, qf[m], z), s1 = MFMA16(k1, qf[m], z);
      float mx = fmaxf(fmaxf(fmaxf(s0[0], s0[1]), fmaxf(s0[2], s0[3])), fmaxf(fmaxf(s1[0], s1[1]), fmaxf(s1[2], s1[3])));
      mx = xmax4(mx);
      negm[m] = (f32x4){-mx, -mx, -mx, -mx}; PIN(negm[m]);
      O[m][0] = z; O[m][1] = z;
    }
    S[0][0] = MFMA16(k0, qf[0], negm[0]); S[0][1] = MFMA16(k1, qf[0], negm[0]);
    S[1][0] = MFMA16(k0, qf[1], negm[1]); S[1][1] = MFMA16(k1, qf[1], negm[1]);
  }
  u32x4 P[4];
  P[2] = (u32x4){0, 0, 0, 0}; P[3] = P[2];
  bf16x8 va0 = BF(P[2]), va1 = va0, vb0, vb1;
  S[3][1] = (f32x4){0.f, 0.f, 0.f, 0.f};
  f32x4 L[4];
#pragma unroll
  for (int m = 0; m < 4; ++m) L[m] = (f32x4){0.f, 0.f, 0.f, 0.f};
  bf16x8 ones; { const short o1 = (l & 15) == 0 ? (short)0x3f80 : (short)0; s16x8 ov = {o1, o1, o1, o1, o1, o1, o1, o1}; ones = __builtin_bit_cast(bf16x8, ov); PIN(ones); }
#define LS(m) do { L[m] = MFMA16(ones, BF(P[m]), L[m]); PIN(L[m]); SB(); } while (0)
#define LADD2(m, kb, i)
#define NOLS(m)
#define EXPF(x) __builtin_amdgcn_exp2f(x)
#define EX2(m, kb, i) do { float t0_ = EXPF(S[m][kb][i]), t1_ = EXPF(S[m][kb][i + 1]); PIN(t0_); PIN(t1_); S[m][kb][i] = t0_; S[m][kb][i + 1] = t1_; SB(); } while (0)
#define CVA(m, kb) do { unsigned c_ = cvtpk(S[m][kb][0], S[m][kb][1]); PIN(c_); P[m][2 * kb] = c_; LADD2(m, kb, 0); SB(); } while (0)
#define CVB(m, kb) do { unsigned c_ = cvtpk(S[m][kb][2], S[m][kb][3]); PIN(c_); P[m][2 * kb + 1] = c_; LADD2(m, kb, 2); SB(); } while (0)
#define GRP(mq, kq, KF, pm, pk, me, ke, mp, db, VF, LSM, X) do { \
    S[mq][kq] = MFMA16(KF, qf[mq], negm[mq]); PIN(S[mq][kq]); SB(); X; CVB(pm, pk); EX2(me, ke, 0); \
    O[mp][db] = MFMA16(VF, BF(P[mp]), O[mp][db]); PIN(O[mp][db]); SB(); EX2(me, ke, 2); LSM(mp); CVA(me, ke); } while (0)
#define TILE(KC0, KC1, KN0, KN1, VP0, VP1, VC0, VC1, kn, vp, D) do { \
    GRP(2, 0, KC0, 3, 1, 0, 0, 2, 0, VP0, NOLS, KN0 = ldsfrag(kn); SB()); \
    GRP(2, 1, KC1, 0, 0, 0, 1, 2, 1, VP1, LS,   KN1 = ldsfrag((kn) + 1024); SB()); \
    GRP(3, 0, KC0, 0, 1, 1, 0, 3, 0, VP0, NOLS, VC0 = ldsfrag(vp); SB()); \
    GRP(3, 1, KC1, 1, 0, 1, 1, 3, 1, VP1, LS,   VC1 = ldsfrag((vp) + 1024); SB()); \
    GRP(0, 0, KN0, 1, 1, 2, 0, 0, 0, VC0, NOLS, D(0)); \
    GRP(0, 1, KN1, 2, 0, 2, 1, 0, 1, VC1, LS,   D(1)); \
    GRP(1, 0, KN0, 2, 1, 3, 0, 1, 0, VC0, NOLS, D(2)); \
    GRP(1, 1, KN1, 3, 0, 3, 1, 1, 1, VC1, LS,   D(3)); \
  } while (0)
#define NOD(i)
  bf16x8 kx0, kx1;
#define STAGE(SC, SN, SF, DM, LAST) do { \
    TILE(k0, k1, kx0, kx1, va0, va1, vb0, vb1, kvb + (SC) * SLOT + 4096, kvb + (SC) * SLOT + 2048, NOD); \
    TILE(kx0, kx1, k0, k1, vb0, vb1, va0, va1, kvb + (SC) * SLOT + 8192, kvb + (SC) * SLOT + 4096 + 2048, NOD); \
    TILE(k0, k1, kx0, kx1, va0, va1, vb0, vb1, kvb + (SC) * SLOT + 12288, kvb + (SC) * SLOT + 8192 + 2048, NOD); \
    WAIT_BAR(0);        \
    TILE(kx0, kx1, k0, k1, vb0, vb1, va0, va1, kvb + ((LAST) ? (SC) : (SN)) * SLOT, kvb + (SC) * SLOT + 12288 + 2048, DM); } while (0)
#define DM_(SF, i) do { if constexpr ((i) == 0) { dma1a_c<(SF) * SLOT, 16384>(kvm, vlin, rKV, kvso); } else { dma1b_c<(i) * 1024>(vlin, rKV, kvso); } SB(); } while (0)
#define DM0(i) DM_(0, i)
#define DM1(i) DM_(1, i)
#define DM2(i) DM_(2, i)
  for (int j = 0; j < 12; j += 3) { STAGE(1, 2, 0, DM0, false); STAGE(2, 0, 1, DM1, false); STAGE(0, 1, 2, DM2, false); STAMP(4 + j / 3); }
  STAGE(1, 2, 0, DM0, false); STAGE(2, 0, 1, DM1, false); STAGE(0, 1, 2, NOD, false); STAGE(1, 2, 0, NOD, true);
  CVB(3, 1);
  O[2][0] = MFMA16(va0, BF(P[2]), O[2][0]); O[2][1] = MFMA16(va1, BF(P[2]), O[2][1]); LS(2);
  O[3][0] = MFMA16(va0, BF(P[3]), O[3][0]); O[3][1] = MFMA16(va1, BF(P[3]), O[3][1]); LS(3);
  STAMP(2);
  const __amdgpu_buffer_rsrc_t rO = mkrsrc(ws + WS_O, 4194304);
#pragma unroll
  for (int m = 0; m < 4; ++m) {
    float ls = __shfl(L[m][0], l & 15);
    float inv = 1.0f / ls;
    f32x4 o0 = O[m][0], o1 = O[m][1];
    const float mo = fmaxf(fmaxf(fmaxf(fabsf(o0[0]), fabsf(o0[1])), fmaxf(fabsf(o0[2]), fabsf(o0[3]))), fmaxf(fmaxf(fabsf(o1[0]), fabsf(o1[1])), fmaxf(fabsf(o1[2]), fabsf(o1[3]))));
    const bool ok = (ls <= 1.0e18f) && (mo <= 1.0e37f);
    if (__builtin_expect(__any(!ok), 0)) { attn_slow(ws + WS_KV, qf[m], o0, o1); inv = 1.0f; }
    const u32x4 ch = chunk_of(cvtpk(o0[0] * inv, o0[1] * inv), cvtpk(o0[2] * inv, o0[3] * inv), cvtpk(o1[0] * inv, o1[1] * inv), cvtpk(o1[2] * inv, o1[3] * inv));
    const int c = ((g & 1) << 1) | (g >> 1);
    st_wt16(rO, ((4 * hg + w) * 128 + 4 * tb + m) * 1024 + c * 256 + (l & 15) * 16, ch);
  }
  asm volatile("s_waitcnt vmcnt(0)\n\ts_barrier" ::: "memory");
  if (w == 0 && lane_id() == 0) signal(ws, FL_O + tb * 8 + hg, tag);
  STAMP(3);
}

DI int w2_kh(int gi) { return gi & 1; }
DI void w2_weights_early(int gi, const u16* __restrict__ w2, LAS char* lds, const int w) {
  const int l = lane_id(), nt = gi >> 5;
  const __amdgpu_buffer_rsrc_t rW = mkrsrc((const char*)w2 - 1024, F_ * D_ * 2 + 1024);
  const unsigned vw0 = wdma_off(l, w, 2048) + nt * 256 + w * 8 * 2048 + 1024, vw1 = vw0 + 128 - 1024;
  unsigned wso = 56 * w2_kh(gi) * 65536 - 65536;
  const unsigned mw = (unsigned)(uintptr_t)lds + 8192 + w * 2048;
  dma1a<0 * 16384, 65536>(mw, vw0, rW, wso); dma1b<1024>(vw1, rW, wso); dma1a<1 * 16384, 65536>(mw, vw0, rW, wso); dma1b<1024>(vw1, rW, wso);
  dma1a<2 * 16384, 65536>(mw, vw0, rW, wso); dma1b<1024>(vw1, rW, wso); dma1a<3 * 16384, 65536>(mw, vw0, rW, wso); dma1b<1024>(vw1, rW, wso);
}

DI f32x4 gelu4(f32x4 acc, float rs, f32x4 b, float c1v) {
  constexpr float c2 = -2.0f * 0.7978845608028654f * 1.4426950408889634f * 0.044715f;
  float x0 = acc[0], x1 = acc[1], x2 = acc[2], x3 = acc[3], t0, t1, t2, t3;
  asm("v_fma_f32 %0, %0, %8, %9\n\tv_fma_f32 %1, %1, %8, %10\n\tv_fma_f32 %2, %2, %8, %11\n\tv_fma_f32 %3, %3, %8, %12\n\t"
      "v_mul_f32 %4, %0, %0\n\tv_mul_f32 %5, %1, %1\n\tv_mul_f32 %6, %2, %2\n\tv_mul_f32 %7, %3, %3\n\t"
      "v_fma_f32 %4, %4, %14, %13\n\tv_fma_f32 %5, %5, %14, %13\n\tv_fma_f32 %6, %6, %14, %13\n\tv_fma_f32 %7, %7, %14, %13\n\t"
      "v_mul_f32 %4, %0, %4\n\tv_mul_f32 %5, %1, %5\n\tv_mul_f32 %6, %2, %6\n\tv_mul_f32 %7, %3, %7\n\t"
      "v_exp_f32 %4, %4\n\tv_exp_f32 %5, %5\n\tv_exp_f32 %6, %6\n\tv_exp_f32 %7, %7\n\t"
      "v_add_f32 %4, 1.0, %4\n\tv_add_f32 %5, 1.0, %5\n\tv_add_f32 %6, 1.0, %6\n\tv_add_f32 %7, 1.0, %7\n\t"
      "v_rcp_f32 %4, %4\n\tv_rcp_f32 %5, %5\n\tv_rcp_f32 %6, %6\n\tv_rcp_f32 %7, %7\n\t"
      "v_mul_f32 %0, %0, %4\n\tv_mul_f32 %1, %1, %5\n\tv_mul_f32 %2, %2, %6\n\tv_mul_f32 %3, %3, %7"
      : "+v"(x0), "+v"(x1), "+v"(x2), "+v"(x3), "=&v"(t0), "=&v"(t1), "=&v"(t2), "=&v"(t3)
      : "v"(rs), "v"(b[0]), "v"(b[1]), "v"(b[2]), "v"(b[3]), "v"(c1v), "s"(c2));
  return (f32x4){x0, x1, x2, x3};
}

DI void role_w1(int gi, const u16* __restrict__ wq, const u16* __restrict__ w1, const u16* __restrict__ b1, const u16* __restrict__ w2, char* __restrict__ ws, LAS char* lds, const int w, const u64 tag) {
  const int l = lane_id();
  const int xcd = gi & 7, idx = gi >> 3, p = (xcd >> 2) * 8 + (idx & 7), j = (xcd & 3) * 4 + (idx >> 3);
  const int g = l >> 4, wm = w >> 1, wn = w & 1; const int stamp_base = 256; (void)stamp_base;
  const unsigned lds0 = (unsigned)(uintptr_t)lds;
  constexpr int SLOT = 24576;
  const __amdgpu_buffer_rsrc_t rX = mkrsrc(ws + WS_XN, 4194304), rW = mkrsrc((const char*)w1 - 4096, D_ * F_ * 2 + 4096);
  const unsigned vlin = l * 16;
  unsigned vw[4];
#pragma unroll
  for (int i = 0; i < 4; ++i) vw[i] = wdma_off(l, w, F_ * 2) + 8 * w * (F_ * 2) + 448 * j - 64 * (j & 1) + 128 * i + 4096 - 1024 * i;
  STAMP(0);
  unsigned xso = (8 * p + 2 * w) * 1024 - 131072, wso = 0u - 32 * F_ * 2;
  const unsigned mx = lds0 + w * 2048, mw = lds0 + 8192 + w * 4096;
#define PC1(SL, i) do { if constexpr ((i) == 0) dma1a_c<(SL) * SLOT, 131072>(mx, vlin, rX, xso); else if constexpr ((i) == 1) dma1b_c<1024>(vlin, rX, xso); \
    else if constexpr ((i) == 2) dma1a<(SL) * SLOT, 32 * F_ * 2>(mw, vw[0], rW, wso); else dma1b<((i) - 2) * 1024>(vw[(i) - 2], rW, wso); } while (0)
#define STG1(SL) do { PC1(SL, 0); PC1(SL, 1); PC1(SL, 2); PC1(SL, 3); PC1(SL, 4); PC1(SL, 5); } while (0)
  PC1(0, 2); PC1(0, 3); PC1(0, 4); PC1(0, 5); PC1(1, 2); PC1(1, 3); PC1(1, 4); PC1(1, 5); PC1(2, 2); PC1(2, 3); PC1(2, 4); PC1(2, 5);
  constexpr int L_R2 = 3 * SLOT, L_B1 = L_R2 + 512;
  const unsigned vpf = l < 4 ? (4 * (idx & 7) + w) * (F_ * 2) + 448 * j - 64 * (j & 1) + l * 128 + 4096 : 0x80000000u;
  const unsigned mpf = lds0 + L_B1 + 1024;
  dma4_ahead<1 * 32 * F_ * 2>(mpf, vpf, rW, wso); dma4_ahead<2 * 32 * F_ * 2>(mpf, vpf, rW, wso); dma4_ahead<3 * 32 * F_ * 2>(mpf, vpf, rW, wso);
  dma4_ahead<4 * 32 * F_ * 2>(mpf, vpf, rW, wso); dma4_ahead<5 * 32 * F_ * 2>(mpf, vpf, rW, wso);
  if (w == 1) dma4(mpf, (gi * 64 + l) * 128, mkrsrc(wq, D_ * D_ * 2), 0);
  if (w >= 2 && PFROWS > 0) { constexpr int NL = PFROWS * 56 / 512;
#pragma unroll
    for (int i = 0; i < (NL + 63) / 64; ++i) dma4(mpf, i * 64 + l < NL ? 256 * F_ * 2 + ((gi * 2 + (w - 2)) * NL + i * 64 + l) * 128 : 0x80000000u, mkrsrc(w1, D_ * F_ * 2), 0);
  }
  if (w >= 2) dma4(lds0 + L_B1 + (w - 2) * 256, 448 * j + ((w - 2) * 64 + l) * 4, mkrsrc(b1, F_ * 2), 0);
  if (w == 0) await(ws, l < 16 ? FL_XN + 8 * p + (l & 7) + 128 * (l >> 3) : -1, tag);
  asm volatile("s_barrier" ::: "memory");
  STAMP(8);
  if (w < 2) dma4_c(lds0 + L_R2 + w * 256, (128 * p + 64 * w + l) * 4, mkrsrc(ws + WS_R2, 8192), 0);
  PC1(0, 0); PC1(0, 1); PC1(1, 0); PC1(1, 1); PC1(2, 0); PC1(2, 1);
  dma4(mpf, 0x80000000u, rW, 0);
  f32x4 acc[4][7];
#pragma unroll
  for (int m = 0; m < 4; ++m)
#pragma unroll
    for (int n = 0; n < 7; ++n) acc[m][n] = (f32x4){0.f, 0.f, 0.f, 0.f};
  const LAS char* xrd = lds + 4 * wm * 1024 + l * 16;
  const LAS char* wrb[5];
  { const int off = 6 * wn + 2 * (j & 1), nl = 12 + wn + 2 * (j & 1);
#pragma unroll
    for (int i = 0; i < 4; ++i) wrb[i] = lds + 8192 + (g * 4 + ((i + off) >> 2)) * 1024 + wrd_off(l, (i + off) & 3);
    wrb[4] = lds + 8192 + (g * 4 + (nl >> 2)) * 1024 + wrd_off(l, nl & 3); }
  bf16x8 xa[4], wa[7], xb[4], wb[7];
#define RDX(X, m, so) X[m] = ldsfrag(xrd + (so) + (m) * 1024)
#define RDW(W, n, so) W[n] = ((n) < 6 ? ldstr(wrb[(n) & 3] + (so) + ((n) >> 2) * 1024, wrb[(n) & 3] + (so) + ((n) >> 2) * 1024 + 512) : ldstr(wrb[4] + (so), wrb[4] + (so) + 512))
#define M1(X, W, m, n) acc[m][n] = MFMA16(W[n], X[m], acc[m][n])
#define STEP1(X, W, XN, WN, RD, RS, DMA, DS) do { \
    M1(X, W, 0, 0); if (RD) RDW(WN, 0, (RS) * SLOT); SB(); M1(X, W, 1, 0); if (RD) RDX(XN, 0, (RS) * SLOT); SB(); M1(X, W, 2, 0); if (RD) RDW(WN, 1, (RS) * SLOT); SB(); M1(X, W, 3, 0); if (DMA) PC1(DS, 0); SB(); \
    M1(X, W, 0, 1); if (RD) RDX(XN, 1, (RS) * SLOT); SB(); M1(X, W, 1, 1); if (RD) RDW(WN, 2, (RS) * SLOT); SB(); M1(X, W, 2, 1); if (RD) RDX(XN, 2, (RS) * SLOT); SB(); M1(X, W, 3, 1); if (DMA) PC1(DS, 1); SB(); \
    M1(X, W, 0, 2); if (RD) RDW(WN, 3, (RS) * SLOT); SB(); M1(X, W, 1, 2); if (RD) RDX(XN, 3, (RS) * SLOT); SB(); M1(X, W, 2, 2); if (RD) RDW(WN, 4, (RS) * SLOT); SB(); M1(X, W, 3, 2); if (DMA) PC1(DS, 2); SB(); \
    M1(X, W, 0, 3); if (RD) RDW(WN, 5, (RS) * SLOT); SB(); M1(X, W, 1, 3); SB(); M1(X, W, 2, 3); if (RD) RDW(WN, 6, (RS) * SLOT); SB(); M1(X, W, 3, 3); if (DMA) PC1(DS, 3); SB(); \
    M1(X, W, 0, 4); SB(); M1(X, W, 1, 4); SB(); M1(X, W, 2, 4); SB(); M1(X, W, 3, 4); if (DMA) PC1(DS, 4); SB(); \
    M1(X, W, 0, 5); SB(); M1(X, W, 1, 5); SB(); M1(X, W, 2, 5); SB(); M1(X, W, 3, 5); if (DMA) { PC1(DS, 5); dma4_ahead<5 * 32 * F_ * 2>(mpf, vpf, rW, wso); } SB(); \
    M1(X, W, 0, 6); SB(); M1(X, W, 1, 6); SB(); M1(X, W, 2, 6); SB(); M1(X, W, 3, 6); SB(); } while (0)
  WAIT_BAR(3);
#pragma unroll
  for (int m = 0; m < 4; ++m) RDX(xa, m, 0);
#pragma unroll
  for (int n = 0; n < 7; ++n) RDW(wa, n, 0);
#define EV(SC, SN, W_, RD, DMA) do { WAIT_BAR(W_); SB(); STEP1(xa, wa, xb, wb, RD, SN, DMA, SC); } while (0)
#define OD(SC, SN, W_, RD, DMA) do { WAIT_BAR(W_); SB(); STEP1(xb, wb, xa, wa, RD, SN, DMA, SC); } while (0)
#define PR1(i) do { if constexpr ((i) == 0 || (((G1PAT >> (i)) & 1) != ((G1PAT >> ((i) - 1 < 0 ? 0 : (i) - 1)) & 1))) __builtin_amdgcn_s_setprio(((G1PAT >> (i)) & 1) ? 2 : 0); } while (0)
#define SIX() do { PR1(0); EV(0, 1, 8, true, true); PR1(1); OD(1, 2, 8, true, true); PR1(2); EV(2, 0, 8, true, true); PR1(3); OD(0, 1, 8, true, true); PR1(4); EV(1, 2, 8, true, true); PR1(5); OD(2, 0, 8, true, true); } while (0)
  for (int it = 0; it < 4; ++it) { SIX(); STAMP(4 + it); }
  EV(0, 1, 8, true, true); OD(1, 2, 8, true, true); EV(2, 0, 8, true, true); OD(0, 1, 8, true, true); EV(1, 2, 8, true, true);
  OD(2, 0, 8, true, false); EV(0, 1, 0, true, false); OD(1, 2, 0, false, false);
  STAMP(1);
  __builtin_amdgcn_s_setprio(PRIO_GE);
  w2_weights_early(gi, w2, lds, w);
  const int le = lane_id(), ge = le >> 4;
  float r2[4];
#pragma unroll
  for (int m = 0; m < 4; ++m) r2[m] = *(const LAS float*)(lds + L_R2 + (64 * wm + 16 * m + (le & 15)) * 4);
  const __amdgpu_buffer_rsrc_t rH = mkrsrc(ws + WS_H, 112 * 128 * 1024);
  const int cch = ((ge & 1) << 1) | (ge >> 1);
  unsigned last[4][2];
  float c1v = -2.0f * 0.7978845608028654f * 1.4426950408889634f; PIN(c1v);
#pragma unroll
  for (int n = 0; n < 7; ++n) {
    const int nb = n < 6 ? 6 * wn + n : 12 + wn;
    const u32x2 braw = *(const LAS u32x2*)(lds + L_B1 + (16 * nb + 4 * ge) * 2);
    const f32x4 bb = {lo2f(braw[0]), hi2f(braw[0]), lo2f(braw[1]), hi2f(braw[1])};
#pragma unroll
    for (int m = 0; m < 4; ++m) acc[m][n] = gelu4(acc[m][n], r2[m], bb, c1v);
    if (n < 6) { if (n & 1) {
#pragma unroll
        for (int m = 0; m < 4; ++m) {
          const u32x4 ch = chunk_of(cvtpk(acc[m][n - 1][0], acc[m][n - 1][1]), cvtpk(acc[m][n - 1][2], acc[m][n - 1][3]), cvtpk(acc[m][n][0], acc[m][n][1]), cvtpk(acc[m][n][2], acc[m][n][3]));
          st_wt16(rH, ((7 * j + 3 * wn + (n >> 1)) * 128 + 8 * p + 4 * wm + m) * 1024 + cch * 256 + (le & 15) * 16, ch);
        } }
    } else {
#pragma unroll
      for (int m = 0; m < 4; ++m) { last[m][0] = cvtpk(acc[m][n][0], acc[m][n][1]); last[m][1] = cvtpk(acc[m][n][2], acc[m][n][3]); }
    }
  }
#pragma unroll
  for (int m = 0; m < 4; m += 2) {
    const u32x4 ch = chunk_of(last[m][0], last[m][1], last[m + 1][0], last[m + 1][1]);
    st_wt16(rH, ((7 * j + 6) * 128 + 8 * p + 4 * wm + m + (ge & 1)) * 1024 + (2 * wn + (ge >> 1)) * 256 + (le & 15) * 16, ch);
  }
  STAMP(2);
  __builtin_amdgcn_s_setprio(PRIO_G2);
  asm volatile("s_waitcnt vmcnt(0)\n\ts_barrier" ::: "memory");
  if (w == 0 && lane_id() == 0) signal(ws, FL_H + p * 16 + j, tag);
  STAMP(3);
#undef RDX
#undef RDW
#undef EV
#undef OD
}

DI void role_w2(int gi, const u16* __restrict__ w2, const u16* __restrict__ wo, const u16* __restrict__ b2, const u16* __restrict__ x, u16* __restrict__ out, char* __restrict__ ws, LAS char* lds, const int w, const u64 tag) {
  const int l = lane_id();
  const int xcd = gi & 7, idx = gi >> 3, kh = xcd & 1, p = (xcd >> 1) * 4 + (idx & 3), nt = idx >> 2;
  const int partner = gi ^ 1;
  const int g = l >> 4, wm = w >> 1, wn = w & 1; const int stamp_base = 512; (void)stamp_base;
  const unsigned lds0 = (unsigned)(uintptr_t)lds;
  constexpr int SLOT = 16384;
#define AINC_ 131072
#define WINC_ 65536
  const __amdgpu_buffer_rsrc_t rA = mkrsrc(ws + WS_H, 144 * 128 * 1024);
  __amdgpu_buffer_rsrc_t rW = mkrsrc((const char*)w2 - 1024, F_ * D_ * 2 + 1024);
  const unsigned vlin = l * 16;
  const unsigned vw0 = wdma_off(l, w, 2048) + nt * 256 + w * 8 * 2048 + 1024, vw1 = vw0 + 128 - 1024;
  if (w == 0) await(ws, l < 8 ? FL_H + p * 16 + 8 * kh + l : -1, tag);
  asm volatile("s_barrier" ::: "memory");
  STAMP(0);
  unsigned aso = (56 * kh * 128 + 8 * p + 2 * w) * 1024 - AINC_, wso = 56 * kh * 65536 + 3 * 65536;
  const unsigned ma = lds0 + w * 2048, mw = lds0 + 8192 + w * 2048;
#define AINC 131072
#define WINC 65536
#define PC2(SL, i) do { if constexpr ((i) == 0) dma1a_c<(SL) * SLOT, AINC>(ma, vlin, rA, aso); else if constexpr ((i) == 1) dma1b_c<1024>(vlin, rA, aso); \
    else if constexpr ((i) == 2) dma1a<(SL) * SLOT, WINC>(mw, vw0, rW, wso); else dma1b<1024>(vw1, rW, wso); } while (0)
#define STG2(SL) do { PC2(SL, 0); PC2(SL, 1); PC2(SL, 2); PC2(SL, 3); } while (0)
  PC2(0, 0); PC2(0, 1); PC2(1, 0); PC2(1, 1); PC2(2, 0); PC2(2, 1); PC2(3, 0); PC2(3, 1); STG2(4);
  f32x4 acc[4][4];
#pragma unroll
  for (int m = 0; m < 4; ++m)
#pragma unroll
    for (int n = 0; n < 4; ++n) acc[m][n] = (f32x4){0.f, 0.f, 0.f, 0.f};
  const LAS char* xrd = lds + 4 * wm * 1024 + l * 16;
  const LAS char* wrb[4];
#pragma unroll
  for (int i = 0; i < 4; ++i) wrb[i] = lds + 8192 + (g * 2 + wn) * 1024 + wrd_off(l, i);
  bf16x8 xa[4], wa[4], xb[4], wb[4];
#define RDX(X, m, so) X[m] = ldsfrag(xrd + (so) + (m) * 1024)
#define RDW(W, n, so) W[n] = ldstr(wrb[n] + (so), wrb[n] + (so) + 512)
#define M2(X, W, m, n) acc[m][n] = MFMA16(W[n], X[m], acc[m][n])
#define STEP2(X, W, XN, WN, RD, RS, DMA, DS) do { \
    M2(X, W, 0, 0); if (RD) RDW(WN, 0, (RS) * SLOT); SB(); M2(X, W, 1, 0); if (RD) RDX(XN, 0, (RS) * SLOT); SB(); M2(X, W, 2, 0); if (RD) RDX(XN, 1, (RS) * SLOT); SB(); M2(X, W, 3, 0); if (DMA) PC2(DS, 0); SB(); \
    M2(X, W, 0, 1); if (RD) RDW(WN, 1, (RS) * SLOT); SB(); M2(X, W, 1, 1); if (RD) RDX(XN, 2, (RS) * SLOT); SB(); M2(X, W, 2, 1); if (RD) RDX(XN, 3, (RS) * SLOT); SB(); M2(X, W, 3, 1); if (DMA) PC2(DS, 1); SB(); \
    M2(X, W, 0, 2); if (RD) RDW(WN, 2, (RS) * SLOT); SB(); M2(X, W, 1, 2); if (RD) RDW(WN, 3, (RS) * SLOT); SB(); M2(X, W, 2, 2); SB(); M2(X, W, 3, 2); if (DMA) PC2(DS, 2); SB(); \
    M2(X, W, 0, 3); SB(); M2(X, W, 1, 3); SB(); M2(X, W, 2, 3); SB(); M2(X, W, 3, 3); if (DMA) PC2(DS, 3); SB(); } while (0)
  WAIT_BAR(6);
#pragma unroll
  for (int i = 0; i < 4; ++i) { RDX(xa, i, 0); RDW(wa, i, 0); }
#define EV(SC, SN, W_, RD, DMA) do { WAIT_BAR(W_); SB(); STEP2(xa, wa, xb, wb, RD, SN, DMA, SC); } while (0)
#define OD(SC, SN, W_, RD, DMA) do { WAIT_BAR(W_); SB(); STEP2(xb, wb, xa, wa, RD, SN, DMA, SC); } while (0)
#define PR2(i) do { if constexpr ((i) == 0 || (((G2PAT >> (i)) & 1) != ((G2PAT >> ((i) - 1 < 0 ? 0 : (i) - 1)) & 1))) __builtin_amdgcn_s_setprio(((G2PAT >> (i)) & 1) ? 2 : 0); } while (0)
  u64 oflag = 0;
  for (int it = 0; it < 6; ++it) {
    STAMP(4 + it);
    PR2(0);
    EV(0, 1, 12, true, true);
    if (it == 5) {
      if (w == 0) {
        asm volatile("" : "+v"(oflag));
        if (!__all(l >= 8 || oflag == tag)) await(ws, l < 8 ? FL_O + (2 * p + (l >> 2)) * 8 + 4 * kh + (l & 3) : -1, tag);
      }
      rW = mkrsrc((const char*)wo - 1024, D_ * D_ * 2 + 1024);
      aso = ((112 + 16 * kh) * 128 + 8 * p + 2 * w) * 1024 - 131072; wso = 16 * kh * 65536 - 65536;
    }
    PR2(1); OD(1, 2, 12, true, true); PR2(2); EV(2, 3, 12, true, true); PR2(3); OD(3, 4, 12, true, true); PR2(4); EV(4, 0, 12, true, true);
    PR2(5); OD(0, 1, 12, true, true); PR2(6); EV(1, 2, 12, true, true);
    if (it == 4 && w == 0) {
      const GAS u64* fo = flagp(ws, FL_O + (2 * p + ((l >> 2) & 1)) * 8 + 4 * kh + (l & 3));
      asm volatile("global_load_dwordx2 %0, %1, off sc1" : "=&v"(oflag) : "v"(fo));
    }
    PR2(7); OD(2, 3, 12, true, true); PR2(8); EV(3, 4, 12, true, true); PR2(9); OD(4, 0, 12, true, true);
  }
  EV(0, 1, 12, true, true); OD(1, 2, 12, true, true); EV(2, 3, 12, true, true); OD(3, 4, 12, true, true); EV(4, 0, 12, true, true); OD(0, 1, 12, true, true); EV(1, 2, 12, true, true);
  u32x2 xres[2][4], bres[4];
  { const int le = lane_id();
    const u16* xp = x + (size_t)(128 * p + 64 * wm + 32 * kh + (le & 15)) * D_ + 128 * nt + 64 * wn + 4 * (le >> 4); const u16* xq = xp + 16 * D_; const u16* bp = b2 + 128 * nt + 64 * wn + 4 * (le >> 4);
    asm volatile("global_load_dwordx2 %0, %8, off\n\tglobal_load_dwordx2 %1, %8, off offset:32\n\tglobal_load_dwordx2 %2, %8, off offset:64\n\tglobal_load_dwordx2 %3, %8, off offset:96\n\t"
                 "global_load_dwordx2 %4, %9, off\n\tglobal_load_dwordx2 %5, %9, off offset:32\n\tglobal_load_dwordx2 %6, %9, off offset:64\n\tglobal_load_dwordx2 %7, %9, off offset:96"
                 : "=&v"(xres[0][0]), "=&v"(xres[0][1]), "=&v"(xres[0][2]), "=&v"(xres[0][3]), "=&v"(xres[1][0]), "=&v"(xres[1][1]), "=&v"(xres[1][2]), "=&v"(xres[1][3]) : "v"(xp), "v"(xq));
    asm volatile("global_load_dwordx2 %0, %4, off\n\tglobal_load_dwordx2 %1, %4, off offset:32\n\tglobal_load_dwordx2 %2, %4, off offset:64\n\tglobal_load_dwordx2 %3, %4, off offset:96"
                 : "=&v"(bres[0]), "=&v"(bres[1]), "=&v"(bres[2]), "=&v"(bres[3]) : "v"(bp)); }
  OD(2, 3, 24, true, false); EV(3, 4, 20, true, false); OD(4, 0, 16, true, false); EV(0, 1, 12, true, false); OD(1, 2, 12, false, false);
  STAMP(1);
  if (kh) {
#pragma unroll
    for (int n = 0; n < 4; ++n) { f32x4 t = acc[0][n]; acc[0][n] = acc[2][n]; acc[2][n] = t; t = acc[1][n]; acc[1][n] = acc[3][n]; acc[3][n] = t; }
#pragma unroll
    for (int n = 0; n < 4; ++n) { PIN(acc[0][n]); PIN(acc[1][n]); PIN(acc[2][n]); PIN(acc[3][n]); }
  }
  const int le = lane_id(), ge = le >> 4;
  const int token0 = 128 * p + 64 * wm + 32 * kh + (le & 15);
  {
    const __amdgpu_buffer_rsrc_t rP = mkrsrc(ws + WS_PART + (size_t)gi * 32768, 16384);
#pragma unroll
    for (int m = 0; m < 2; ++m)
#pragma unroll
      for (int n2 = 0; n2 < 2; ++n2)
        st_wt16(rP, ((w * 2 + m) * 2 + n2) * 1024 + le * 16, (u32x4){cvtpk(acc[2 + m][2 * n2][0], acc[2 + m][2 * n2][1]), cvtpk(acc[2 + m][2 * n2][2], acc[2 + m][2 * n2][3]),
                                                                       cvtpk(acc[2 + m][2 * n2 + 1][0], acc[2 + m][2 * n2 + 1][1]), cvtpk(acc[2 + m][2 * n2 + 1][2], acc[2 + m][2 * n2 + 1][3])});
    asm volatile("s_waitcnt vmcnt(0)\n\ts_barrier" : "+v"(xres[0][0]), "+v"(xres[0][1]), "+v"(xres[0][2]), "+v"(xres[0][3]), "+v"(xres[1][0]), "+v"(xres[1][1]), "+v"(xres[1][2]), "+v"(xres[1][3]),
                 "+v"(bres[0]), "+v"(bres[1]), "+v"(bres[2]), "+v"(bres[3]) :: "memory");
    if (w == 0) { if (le == 0) signal(ws, FL_X + gi, tag); await(ws, FL_X + partner, tag); }
    asm volatile("s_barrier" ::: "memory");
  }
  STAMP(2);
  const __amdgpu_buffer_rsrc_t rPP = mkrsrc(ws + WS_PART + (size_t)partner * 32768, 16384);
  u32x4 pv[2][2];
#pragma unroll
  for (int m = 0; m < 2; ++m)
#pragma unroll
    for (int n2 = 0; n2 < 2; ++n2) pv[m][n2] = __builtin_amdgcn_raw_buffer_load_b128(rPP, ((w * 2 + m) * 2 + n2) * 1024 + le * 16, 0, 16);
#pragma unroll
  for (int m = 0; m < 2; ++m) {
    unsigned d[4][2];
#pragma unroll
    for (int n = 0; n < 4; ++n) {
      const unsigned p0 = pv[m][n >> 1][2 * (n & 1)], p1 = pv[m][n >> 1][2 * (n & 1) + 1];
      const f32x4 a4 = acc[m][n];
      d[n][0] = cvtpk(a4[0] + lo2f(p0) + lo2f(bres[n][0]) + lo2f(xres[m][n][0]), a4[1] + hi2f(p0) + hi2f(bres[n][0]) + hi2f(xres[m][n][0]));
      d[n][1] = cvtpk(a4[2] + lo2f(p1) + lo2f(bres[n][1]) + lo2f(xres[m][n][1]), a4[3] + hi2f(p1) + hi2f(bres[n][1]) + hi2f(xres[m][n][1]));
    }
    const int cch = ((ge & 1) << 1) | (ge >> 1);
#pragma unroll
    for (int n2 = 0; n2 < 2; ++n2) {
      const u32x4 ch = chunk_of(d[2 * n2][0], d[2 * n2][1], d[2 * n2 + 1][0], d[2 * n2 + 1][1]);
      *(u32x4*)(out + (size_t)(token0 + 16 * m) * D_ + 128 * nt + 64 * wn + 32 * n2 + 8 * cch) = ch;
    }
  }
  STAMP(3);
#undef RDX
#undef RDW
}

constexpr int LDS_BYTES = 81920;
#define MODE 0
__global__ __launch_bounds__(256, 2) void k_main(const u16* __restrict__ x, const u16* __restrict__ wq, const u16* __restrict__ wk, const u16* __restrict__ wv, const u16* __restrict__ wo,
                                                  const u16* __restrict__ w1, const u16* __restrict__ b1, const u16* __restrict__ w2, const u16* __restrict__ b2, u16* __restrict__ out, char* __restrict__ ws) {
  extern __shared__ __attribute__((aligned(16))) char lds_[];
  LAS char* lds = (LAS char*)lds_;
  const int w = __builtin_amdgcn_readfirstlane(threadIdx.x >> 6);
  u64 tag = 0;
  if (w == 0) {
    const u64 e = __hip_atomic_load(flagp(ws, FL_E), __ATOMIC_RELAXED, __HIP_MEMORY_SCOPE_AGENT);
    tag = (((u64)(unsigned)__builtin_amdgcn_readfirstlane((unsigned)(e >> 32))) << 32 | (unsigned)__builtin_amdgcn_readfirstlane((unsigned)e)) + 1;
    if (lane_id() == 0) signal(ws, FL_RD + blockIdx.x, tag);
  }
  if ((blockIdx.x >> 8) == 0) {
    __builtin_amdgcn_s_setprio(PRIO_Q);
    phase0(blockIdx.x, x, wk, wv, ws, lds, w, tag);
    role_attn(blockIdx.x, wq, w2, wo, ws, lds, w, tag);
  } else {
    __builtin_amdgcn_s_setprio(PRIO_G1);
    role_w1(blockIdx.x & 255, wq, w1, b1, w2, ws, lds, w, tag);
    role_w2(blockIdx.x & 255, w2, wo, b2, x, out, ws, lds, w, tag);
    if (blockIdx.x == 256 && w == 0) {
      const int l = lane_id();
#pragma unroll 1
      for (int i = 0; i < 8; ++i) await(ws, FL_RD + 64 * i + l, tag);
      if (l == 0) signal(ws, FL_E, tag);
    }
  }
}

extern "C" void kernel_launch(void* const* d_in, const int* in_sizes, int n_in, void* d_out, int out_size, void* d_ws, size_t ws_size, hipStream_t stream) {
  const u16* x = (const u16*)d_in[0]; const u16* wq = (const u16*)d_in[1]; const u16* wk = (const u16*)d_in[2];
  const u16* wv = (const u16*)d_in[3]; const u16* wo = (const u16*)d_in[4];
  const u16* w1 = (const u16*)d_in[6]; const u16* b1 = (const u16*)d_in[7]; const u16* w2 = (const u16*)d_in[8]; const u16* b2 = (const u16*)d_in[9];
  (void)hipFuncSetAttribute((const void*)k_main, hipFuncAttributeMaxDynamicSharedMemorySize, LDS_BYTES);
  k_main<<<512, 256, LDS_BYTES, stream>>>(x, wq, wk, wv, wo, w1, b1, w2, b2, (u16*)d_out, (char*)d_ws);
}
```

```cpp
#include <hip/hip_runtime.h>
#include <stdint.h>
#define TLIM 65

typedef unsigned short u16;
typedef __bf16 bf16x8 __attribute__((ext_vector_type(8)));
typedef __bf16 bf16x2 __attribute__((ext_vector_type(2)));
typedef short s16x8 __attribute__((ext_vector_type(8)));
typedef float f32x4 __attribute__((ext_vector_type(4)));
typedef float f32x2 __attribute__((ext_vector_type(2)));
typedef unsigned u32x4 __attribute__((ext_vector_type(4)));
typedef unsigned u32x2 __attribute__((ext_vector_type(2)));
#define LAS __attribute__((address_space(3)))
#define GAS __attribute__((address_space(1)))
#define DI __device__ __forceinline__
#define SB() __builtin_amdgcn_sched_barrier(0)
#define PIN(x) asm volatile("" : "+v"(x))
#define MFMA16(a, b, c) __builtin_amdgcn_mfma_f32_16x16x32_bf16((a), (b), (c), 0, 0, 0)
#define BF(x) __builtin_bit_cast(bf16x8, x)

#define PF0ROWS 1024
constexpr int S_ = 2048, D_ = 1024, F_ = 3584;
constexpr size_t WS_FLAGS = 0;
constexpr size_t WS_R2   = 131072;
constexpr size_t WS_KV   = WS_R2 + 8192;
constexpr size_t WS_XN   = WS_KV + 262144;
constexpr size_t WS_H    = WS_XN + 4194304;
constexpr size_t WS_O    = WS_H + (size_t)112 * 128 * 1024;
constexpr size_t WS_PART = WS_O + 4194304;
constexpr size_t WS_STAMP = WS_PART + (size_t)2 * 256 * 32768;

constexpr float QSCALE = 0.17677669529663687f * 1.4426950408889634f;

DI int lane_id() { int l; asm volatile("v_mbcnt_lo_u32_b32 %0, -1, 0\n\tv_mbcnt_hi_u32_b32 %0, -1, %0" : "=v"(l)); return l; }
DI float bf2f(u16 v) { return __uint_as_float(((unsigned)v) << 16); }
DI unsigned cvtpk(float lo, float hi) { f32x2 v = {lo, hi}; bf16x2 b = __builtin_convertvector(v, bf16x2); return __builtin_bit_cast(unsigned, b); }
DI float lo2f(unsigned u) { return __uint_as_float(u << 16); }
DI float hi2f(unsigned u) { return __uint_as_float(u & 0xffff0000u); }
DI __amdgpu_buffer_rsrc_t mkrsrc(const void* p, unsigned bytes) { return __builtin_amdgcn_make_buffer_rsrc((void*)p, 0, bytes, 0x00020000); }
DI void dma16(unsigned lds_addr, unsigned voff, __amdgpu_buffer_rsrc_t rsrc, unsigned soff) {
  asm volatile("s_mov_b32 m0, %0\n\ts_nop 0\n\tbuffer_load_dwordx4 %1, %2, %3 offen lds" :: "s"(lds_addr), "v"(voff), "s"(rsrc), "s"(soff));
}
template <int IMM, int INC> DI void dma2(unsigned mbase, unsigned v0, unsigned v1, __amdgpu_buffer_rsrc_t rsrc, unsigned& soff) {
  asm volatile("s_add_u32 m0, %3, %5\n\ts_add_u32 %0, %0, %6\n\tbuffer_load_dwordx4 %1, %4, %0 offen lds\n\tbuffer_load_dwordx4 %2, %4, %0 offen offset:1024 lds"
               : "+s"(soff) : "v"(v0), "v"(v1), "s"(mbase), "s"(rsrc), "i"(IMM), "i"(INC) : "scc");
}
template <int IMM, int INC> DI void dma1a(unsigned mbase, unsigned v0, __amdgpu_buffer_rsrc_t rsrc, unsigned& soff) {
  asm volatile("s_add_u32 m0, %2, %4\n\ts_add_u32 %0, %0, %5\n\tbuffer_load_dwordx4 %1, %3, %0 offen lds" : "+s"(soff) : "v"(v0), "s"(mbase), "s"(rsrc), "i"(IMM), "i"(INC) : "scc");
}
template <int OFF> DI void dma1b(unsigned v0, __amdgpu_buffer_rsrc_t rsrc, unsigned soff) {
  asm volatile("buffer_load_dwordx4 %0, %1, %2 offen offset:%3 lds" :: "v"(v0), "s"(rsrc), "s"(soff), "i"(OFF));
}
template <int IMM, int INC> DI void dma1a_c(unsigned mbase, unsigned v0, __amdgpu_buffer_rsrc_t rsrc, unsigned& soff) {
  asm volatile("s_add_u32 m0, %2, %4\n\ts_add_u32 %0, %0, %5\n\tbuffer_load_dwordx4 %1, %3, %0 offen sc1 lds" : "+s"(soff) : "v"(v0), "s"(mbase), "s"(rsrc), "i"(IMM), "i"(INC) : "scc");
}
template <int OFF> DI void dma1b_c(unsigned v0, __amdgpu_buffer_rsrc_t rsrc, unsigned soff) {
  asm volatile("buffer_load_dwordx4 %0, %1, %2 offen offset:%3 sc1 lds" :: "v"(v0), "s"(rsrc), "s"(soff), "i"(OFF));
}
DI void dma4(unsigned lds_addr, unsigned voff, __amdgpu_buffer_rsrc_t rsrc, unsigned soff) {
  asm volatile("s_mov_b32 m0, %0\n\ts_nop 0\n\tbuffer_load_dword %1, %2, %3 offen lds" :: "s"(lds_addr), "v"(voff), "s"(rsrc), "s"(soff));
}
DI void dma4_c(unsigned lds_addr, unsigned voff, __amdgpu_buffer_rsrc_t rsrc, unsigned soff) {
  asm volatile("s_mov_b32 m0, %0\n\ts_nop 0\n\tbuffer_load_dword %1, %2, %3 offen sc1 lds" :: "s"(lds_addr), "v"(voff), "s"(rsrc), "s"(soff));
}
template <int ADD> DI void dma4_ahead(unsigned lds_addr, unsigned voff, __amdgpu_buffer_rsrc_t rsrc, unsigned soff) {
  unsigned t;
  asm volatile("s_mov_b32 m0, %1\n\ts_add_u32 %0, %4, %5\n\tbuffer_load_dword %2, %3, %0 offen lds" : "=&s"(t) : "s"(lds_addr), "v"(voff), "s"(rsrc), "s"(soff), "i"(ADD) : "scc");
}
#define WAIT_BAR(N) asm volatile("s_waitcnt vmcnt(" #N ") lgkmcnt(0)\n\ts_barrier" ::: "memory")
DI bf16x8 ldsfrag(const LAS char* p) { return *(const LAS bf16x8*)p; }
typedef short v4i16_t __attribute__((ext_vector_type(4)));
DI bf16x8 ldstr(const LAS char* p0, const LAS char* p1) {
  v4i16_t a = __builtin_amdgcn_ds_read_tr16_b64_v4i16((LAS v4i16_t*)p0);
  v4i16_t b = __builtin_amdgcn_ds_read_tr16_b64_v4i16((LAS v4i16_t*)p1);
  s16x8 v = __builtin_shufflevector(a, b, 0, 1, 2, 3, 4, 5, 6, 7);
  return __builtin_bit_cast(bf16x8, v);
}
DI void st_wt16(__amdgpu_buffer_rsrc_t r, unsigned voff, u32x4 v) { __builtin_amdgcn_raw_buffer_store_b128(v, r, voff, 0, 16); }
DI u32x4 chunk_of(unsigned x0, unsigned x1, unsigned y0, unsigned y1) {
  auto s0 = __builtin_amdgcn_permlane16_swap(x0, y0, false, false);
  auto s1 = __builtin_amdgcn_permlane16_swap(x1, y1, false, false);
  return (u32x4){s0[0], s1[0], s0[1], s1[1]};
}
DI unsigned wdma_off(int l, int kblk, int ldbytes) {
  const int row = l >> 3, c8 = l & 7, sw = ((row >> 1) & 1) | ((kblk & 1) << 1);
  return row * ldbytes + (((c8 >> 1) ^ sw) * 32) + (c8 & 1) * 16;
}
DI unsigned wrd_off(int l, int seg) {
  const int g = l >> 4, q = (l & 15) >> 2, sw = ((q >> 1) & 1) | ((g & 1) << 1);
  return q * 128 + ((seg ^ sw) * 32) + (l & 3) * 8;
}
DI float xsum4(float v) {
  auto a = __builtin_amdgcn_permlane16_swap(__float_as_uint(v), __float_as_uint(v), false, false); v = __uint_as_float(a[0]) + __uint_as_float(a[1]);
  auto b = __builtin_amdgcn_permlane32_swap(__float_as_uint(v), __float_as_uint(v), false, false); return __uint_as_float(b[0]) + __uint_as_float(b[1]);
}
DI float xmax4(float v) {
  auto a = __builtin_amdgcn_permlane16_swap(__float_as_uint(v), __float_as_uint(v), false, false); v = fmaxf(__uint_as_float(a[0]), __uint_as_float(a[1]));
  auto b = __builtin_amdgcn_permlane32_swap(__float_as_uint(v), __float_as_uint(v), false, false); return fmaxf(__uint_as_float(b[0]), __uint_as_float(b[1]));
}
#define STAMP(i)

typedef unsigned long long u64;
constexpr int FL_E = 0, FL_RD = 16, FL_XN = 528, FL_KV = 784, FL_H = 1040, FL_O = 1296, FL_X = 1552;
DI GAS u64* flagp(char* ws, int i) { return (GAS u64*)(ws + WS_FLAGS + (size_t)i * 64); }
DI void signal(char* ws, int i, u64 tag) { __hip_atomic_store(flagp(ws, i), tag, __ATOMIC_RELAXED, __HIP_MEMORY_SCOPE_AGENT); }
DI u64 take_tag(char* ws, u64 eraw, int w) {
  asm volatile("" : "+v"(eraw));
  const u64 tag = (((u64)(unsigned)__builtin_amdgcn_readfirstlane((unsigned)(eraw >> 32))) << 32 | (unsigned)__builtin_amdgcn_readfirstlane((unsigned)eraw)) + 1;
  if (w == 0 && lane_id() == 0) signal(ws, FL_RD + blockIdx.x, tag);
  return tag;
}
DI void await(char* ws, int idx, u64 tag) {
  GAS u64* f = flagp(ws, idx < 0 ? FL_E : idx);
  for (unsigned spins = 0; spins < (1u << 20); ++spins) {
    const u64 v = __hip_atomic_load(f, __ATOMIC_RELAXED, __HIP_MEMORY_SCOPE_AGENT);
    if (__all(idx < 0 || v == tag)) break;
    __builtin_amdgcn_s_sleep(2);
  }
  asm volatile("" ::: "memory");
}

DI u64 phase0(int a, const u16* __restrict__ x, const u16* __restrict__ wk, const u16* __restrict__ wv, char* __restrict__ ws, LAS char* lds, const int w, const u64 eraw) {
  const int l = lane_id(), tid = w * 64 + l;
  LAS float* part = (LAS float*)(lds + 65536);
  LAS float* fin = (LAS float*)(lds + 65536 + 8192);
  LAS float* red = (LAS float*)(lds + 65536 + 8192 + 2048);
  const int rb = a & 127, hf = a >> 7;
  const int r = l & 15, c = l >> 4; const int stamp_base = 768; (void)stamp_base;
  STAMP(0);
  u32x4 xv[8];
  { const u16* xr = x + (size_t)(rb * 16 + r) * D_ + w * 256 + c * 8;
#define XL(i) asm volatile("global_load_dwordx4 %0, %1, off offset:" #i "*64" : "=&v"(xv[i]) : "v"(xr))
    XL(0); XL(1); XL(2); XL(3); XL(4); XL(5); XL(6); XL(7); }
  const unsigned lds0 = (unsigned)(uintptr_t)lds;
  {
    __amdgpu_buffer_rsrc_t rw = mkrsrc(hf ? wv : wk, 65536);
#pragma unroll
    for (int i = 0; i < 16; ++i) dma16(lds0 + w * 16384 + i * 1024, l * 16, rw, w * 16384 + i * 1024);
  }
  asm volatile("s_waitcnt vmcnt(16)" : "+v"(xv[0]), "+v"(xv[1]), "+v"(xv[2]), "+v"(xv[3]), "+v"(xv[4]), "+v"(xv[5]), "+v"(xv[6]), "+v"(xv[7]));
  STAMP(1);
  const u64 tag = take_tag(ws, eraw, w);
  float s = 0.f;
#pragma unroll
  for (int i = 0; i < 8; ++i)
#pragma unroll
    for (int j = 0; j < 4; ++j) s += lo2f(xv[i][j]) + hi2f(xv[i][j]);
  s = xsum4(s);
  if (c == 0) red[w * 16 + r] = s;
  asm volatile("s_waitcnt lgkmcnt(0)\n\ts_barrier" ::: "memory");
  const float mean = ((red[r] + red[16 + r]) + (red[32 + r] + red[48 + r])) * (1.0f / D_);
  float q = 0.f;
#pragma unroll
  for (int i = 0; i < 8; ++i)
#pragma unroll
    for (int j = 0; j < 4; ++j) { const float u = lo2f(xv[i][j]) - mean, v = hi2f(xv[i][j]) - mean; q += u * u + v * v; }
  q = xsum4(q);
  if (c == 0) red[64 + w * 16 + r] = q;
  asm volatile("s_waitcnt lgkmcnt(0)\n\ts_barrier" ::: "memory");
  const float var = ((red[64 + r] + red[80 + r]) + (red[96 + r] + red[112 + r])) * (1.0f / D_);
  const float rstd = 1.0f / sqrtf(var + 1e-5f);
#pragma unroll
  for (int i = 0; i < 8; ++i)
#pragma unroll
    for (int j = 0; j < 4; ++j) xv[i][j] = cvtpk((lo2f(xv[i][j]) - mean) * rstd, (hi2f(xv[i][j]) - mean) * rstd);
  const __amdgpu_buffer_rsrc_t rXN = mkrsrc(ws + WS_XN, 4194304);
#pragma unroll
  for (int i = 0; i < 4; ++i) st_wt16(rXN, ((8 * w + 2 * i + hf) * 128 + rb) * 1024 + l * 16, hf ? xv[2 * i + 1] : xv[2 * i]);
  if (hf == 0 && w == 0 && c == 0) __hip_atomic_store((GAS float*)(ws + WS_R2) + rb * 16 + r, 1.0f / sqrtf(var * rstd * rstd + 1e-5f), __ATOMIC_RELAXED, __HIP_MEMORY_SCOPE_AGENT);
  asm volatile("s_waitcnt vmcnt(4)" ::: "memory");
  f32x4 acc[2] = {{0.f, 0.f, 0.f, 0.f}, {0.f, 0.f, 0.f, 0.f}};
  const LAS char* wb = (const LAS char*)lds + w * 16384 + (8 * c + ((l & 15) >> 2)) * 64 + (l & 3) * 8;
#pragma unroll
  for (int i = 0; i < 8; ++i)
#pragma unroll
    for (int b = 0; b < 2; ++b) {
      bf16x8 wf = ldstr(wb + i * 2048 + b * 32, wb + i * 2048 + b * 32 + 256);
      acc[b] = MFMA16(BF(xv[i]), wf, acc[b]);
    }
#pragma unroll
  for (int b = 0; b < 2; ++b)
#pragma unroll
    for (int j = 0; j < 4; ++j) part[(w * 16 + 4 * c + j) * 32 + 16 * b + r] = acc[b][j];
  asm volatile("s_waitcnt vmcnt(0) lgkmcnt(0)\n\ts_barrier" ::: "memory");
  if (tid == 0) signal(ws, FL_XN + a, tag);
  STAMP(2);
#pragma unroll
  for (int u = 0; u < 2; ++u) { const int e = tid + 256 * u; fin[e] = (part[e] + part[512 + e]) + (part[1024 + e] + part[1536 + e]); }
  asm volatile("s_waitcnt lgkmcnt(0)\n\ts_barrier" ::: "memory");
  if (w == 0) {
    float o[8];
    unsigned dst = (rb >> 1) * 4096;
    const int half = rb & 1;
    if (hf == 0) { const int kb = l >> 5, g = (l >> 3) & 3, rr = l & 7;
      const int row = 8 * half + rr, key = 8 * (row >> 2) + 4 * kb + (row & 3) - 16 * half;
#pragma unroll
      for (int j = 0; j < 8; ++j) o[j] = fin[key * 32 + 16 * (j >> 2) + 4 * g + (j & 3)];
      dst += kb * 1024 + (g * 16 + row) * 16;
    } else { const int db = l >> 5, gl = (l >> 4) & 1, rr = l & 15;
#pragma unroll
      for (int j = 0; j < 8; ++j) o[j] = fin[(8 * gl + j) * 32 + 16 * db + rr];
      dst += 2048 + db * 1024 + ((2 * half + gl) * 16 + rr) * 16;
    }
    st_wt16(mkrsrc(ws + WS_KV, 262144), dst, (u32x4){cvtpk(o[0], o[1]), cvtpk(o[2], o[3]), cvtpk(o[4], o[5]), cvtpk(o[6], o[7])});
    asm volatile("s_waitcnt vmcnt(0)" ::: "memory");
    if (l == 0) signal(ws, FL_KV + a, tag);
  }
  STAMP(3);
  return tag;
}

#define MERGED 1
#define PRIO_A 1
#define G1PAT 0
#define G2PAT 0
#define PFSLEEP 80
#define GDELAY 0
#define PFROWS 256
#define PRIO_Q 1
#define PRIO_G1 0
#define PRIO_GE 2
#define PRIO_G2 0
#define LSUM_MFMA 1
DI float fadd_s(float a, float b) { float r; asm("s_nop 0\n\tv_add_f32_e32 %0, %1, %2" : "=v"(r) : "v"(a), "v"(b)); return r; }

DI void attn_slow(const char* __restrict__ kv, bf16x8 qf, f32x4& o0, f32x4& o1) {
  const int l = lane_id();
  const __amdgpu_buffer_rsrc_t rk = mkrsrc(kv, 262144);
  o0 = (f32x4){0.f, 0.f, 0.f, 0.f}; o1 = o0;
  float m = -3.0e38f, ls = 0.f;
#pragma unroll 1
  for (int t = 0; t < 64; ++t) {
    const int p = t * 4096 + l * 16;
    const bf16x8 k0 = BF(__builtin_amdgcn_raw_buffer_load_b128(rk, p, 0, 16)), k1 = BF(__builtin_amdgcn_raw_buffer_load_b128(rk, p + 1024, 0, 16)),
                 v0 = BF(__builtin_amdgcn_raw_buffer_load_b128(rk, p + 2048, 0, 16)), v1 = BF(__builtin_amdgcn_raw_buffer_load_b128(rk, p + 3072, 0, 16));
    const f32x4 z = {0.f, 0.f, 0.f, 0.f};
    f32x4 s0 = MFMA16(k0, qf, z), s1 = MFMA16(k1, qf, z);
    float mx = fmaxf(fmaxf(fmaxf(s0[0], s0[1]), fmaxf(s0[2], s0[3])), fmaxf(fmaxf(s1[0], s1[1]), fmaxf(s1[2], s1[3])));
    mx = fmaxf(mx, __shfl_xor(mx, 16)); mx = fmaxf(mx, __shfl_xor(mx, 32));
    const float mn = fmaxf(m, mx), al = __builtin_amdgcn_exp2f(m - mn);
    m = mn; float rs = 0.f;
#pragma unroll
    for (int i = 0; i < 4; ++i) { s0[i] = __builtin_amdgcn_exp2f(s0[i] - mn); s1[i] = __builtin_amdgcn_exp2f(s1[i] - mn); rs += s0[i] + s1[i]; o0[i] *= al; o1[i] *= al; }
    rs += __shfl_xor(rs, 16); rs += __shfl_xor(rs, 32); ls = ls * al + rs;
    const u32x4 pp = {cvtpk(s0[0], s0[1]), cvtpk(s0[2], s0[3]), cvtpk(s1[0], s1[1]), cvtpk(s1[2], s1[3])};
    o0 = MFMA16(v0, BF(pp), o0); o1 = MFMA16(v1, BF(pp), o1);
  }
#pragma unroll
  for (int i = 0; i < 4; ++i) { o0[i] /= ls; o1[i] /= ls; }
}

DI void role_attn(int a, const u16* __restrict__ wq, const u16* __restrict__ w2, const u16* __restrict__ wo, char* __restrict__ ws, LAS char* lds, const int w, const u64 tag) {
  const int l = lane_id();
#define AMAP 2
  constexpr int NTB = 32 / AMAP;
  const int xcd = a & 7, idx = a >> 3, tb = (xcd / (8 / AMAP)) * NTB + (idx % NTB), hg = (xcd % (8 / AMAP)) * AMAP + idx / NTB;
  const int g = l >> 4; const int stamp_base = 0; (void)stamp_base;
  const unsigned lds0 = (unsigned)(uintptr_t)lds;
  constexpr int SLOT = 24576;
  const __amdgpu_buffer_rsrc_t rX = mkrsrc(ws + WS_XN, 4194304), rKV = mkrsrc(ws + WS_KV, 262144);
  const unsigned vlin = l * 16;
  constexpr int QS = 12288;
  const __amdgpu_buffer_rsrc_t rWq = mkrsrc((const char*)wq - 1024, D_ * D_ * 2 + 1024);
  const unsigned vq0 = wdma_off(l, w, 2048) + hg * 256 + w * 8 * 2048 + 1024, vq1 = vq0 + 128 - 1024;
  unsigned xso = (4 * tb + w) * 1024 - 131072, qso = 0u - 65536;
  const unsigned mx = lds0 + w * 1024, mq = lds0 + 4096 + w * 2048;
#define PCQ(SL, i) do { if constexpr ((i) == 0) dma1a_c<(SL) * QS, 131072>(mx, vlin, rX, xso); else if constexpr ((i) == 1) dma1a<(SL) * QS, 65536>(mq, vq0, rWq, qso); else dma1b<1024>(vq1, rWq, qso); } while (0)
#define STGQ(SL) do { PCQ(SL, 0); PCQ(SL, 1); PCQ(SL, 2); } while (0)
  if (w == 0) await(ws, l < 8 ? FL_XN + 4 * tb + (l & 3) + 128 * (l >> 2) : -1, tag);
  asm volatile("s_waitcnt lgkmcnt(0)\n\ts_barrier" ::: "memory");
  STAMP(0);
  constexpr int NPL = 64 / (4 * NTB);
  const int pl = ((idx % NTB) * 4 + w) * NPL + l;
  const unsigned vpq = l < NPL ? (pl >> 1) * 2048 + hg * 256 + (pl & 1) * 128 + 1024 : 0x80000000u;
  const unsigned mpq = lds0 + 6 * QS;
  STGQ(0); STGQ(1); STGQ(2); STGQ(3); STGQ(4); STGQ(5);
  dma4_ahead<1 * 65536>(mpq, vpq, rWq, qso); dma4_ahead<2 * 65536>(mpq, vpq, rWq, qso); dma4_ahead<3 * 65536>(mpq, vpq, rWq, qso);
  dma4_ahead<4 * 65536>(mpq, vpq, rWq, qso); dma4_ahead<5 * 65536>(mpq, vpq, rWq, qso); dma4_ahead<6 * 65536>(mpq, vpq, rWq, qso);
  f32x4 qacc[2][4];
#pragma unroll
  for (int i = 0; i < 2; ++i)
#pragma unroll
    for (int j = 0; j < 4; ++j) qacc[i][j] = (f32x4){0.f, 0.f, 0.f, 0.f};
  const LAS char* xrd = lds + l * 16;
  const LAS char* wrd0 = lds + 4096 + (g * 2 + (w >> 1)) * 1024 + wrd_off(l, 2 * (w & 1));
  const LAS char* wrd1 = lds + 4096 + (g * 2 + (w >> 1)) * 1024 + wrd_off(l, 2 * (w & 1) + 1);
  bf16x8 qxa[4], qwa[2], qxb[4], qwb[2];
#define QRX(X, m, SL) X[m] = ldsfrag(xrd + (SL) * QS + (m) * 1024)
#define QRW(W, n, SL) W[n] = ((n) == 0 ? ldstr(wrd0 + (SL) * QS, wrd0 + (SL) * QS + 512) : ldstr(wrd1 + (SL) * QS, wrd1 + (SL) * QS + 512))
#define QM(X, W, m, n) qacc[n][m] = MFMA16(W[n], X[m], qacc[n][m])
#define STEPQ(X, W, XN, WN, RD, RS, DMA, DS) do { \
    QM(X, W, 0, 0); if (RD) { QRW(WN, 0, RS); QRX(XN, 0, RS); } SB(); QM(X, W, 1, 0); if (RD) { QRW(WN, 1, RS); QRX(XN, 1, RS); } SB(); QM(X, W, 2, 0); if (RD) QRX(XN, 2, RS); SB(); QM(X, W, 3, 0); if (RD) QRX(XN, 3, RS); SB(); \
    QM(X, W, 0, 1); if (DMA) PCQ(DS, 0); SB(); QM(X, W, 1, 1); if (DMA) PCQ(DS, 1); SB(); QM(X, W, 2, 1); if (DMA) PCQ(DS, 2); SB(); QM(X, W, 3, 1); if (DMA) dma4_ahead<6 * 65536>(mpq, vpq, rWq, qso); SB(); } while (0)
  WAIT_BAR(21);
#pragma unroll
  for (int m = 0; m < 4; ++m) QRX(qxa, m, 0);
  QRW(qwa, 0, 0); QRW(qwa, 1, 0);
#define QEV(SC, SN, W_, RD, DMA) do { WAIT_BAR(W_); SB(); STEPQ(qxa, qwa, qxb, qwb, RD, SN, DMA, SC); } while (0)
#define QOD(SC, SN, W_, RD, DMA) do { WAIT_BAR(W_); SB(); STEPQ(qxb, qwb, qxa, qwa, RD, SN, DMA, SC); } while (0)
  QEV(0, 1, 18, true, true); QOD(1, 2, 19, true, true); QEV(2, 3, 20, true, true); QOD(3, 4, 21, true, true); QEV(4, 5, 22, true, true); QOD(5, 0, 17, true, true);
  for (int it = 0; it < 3; ++it) { QEV(0, 1, 17, true, true); QOD(1, 2, 17, true, true); QEV(2, 3, 17, true, true); QOD(3, 4, 17, true, true); QEV(4, 5, 17, true, true); QOD(5, 0, 17, true, true); }
  QEV(0, 1, 17, true, true); QOD(1, 2, 17, true, true);
  QEV(2, 3, 17, true, false); QOD(3, 4, 13, true, false); QEV(4, 5, 9, true, false); QOD(5, 0, 5, true, false);
  if (w == 0) { await(ws, FL_KV + l, tag); await(ws, FL_KV + 64 + l, tag); await(ws, FL_KV + 128 + l, tag); await(ws, FL_KV + 192 + l, tag); }
  QEV(0, 1, 0, true, false);
  unsigned kvso = w * 4096 - 16384;
  const unsigned kvm = lds0 + w * 4096;
#define STGKV(SF) do { dma1a_c<(SF) * SLOT, 16384>(kvm, vlin, rKV, kvso); dma1b_c<1024>(vlin, rKV, kvso); dma1b_c<2048>(vlin, rKV, kvso); dma1b_c<3072>(vlin, rKV, kvso); } while (0)
  STGKV(1); STGKV(2);
  {
    const unsigned line = (a * 4 + w) * 64 + l;
    dma4(lds0 + 6 * QS, line * 128, mkrsrc(w2, F_ * D_ * 2), 0);
    dma4(lds0 + 6 * QS, line * 128, mkrsrc(wo, D_ * D_ * 2), 0);
  }
  STEPQ(qxb, qwb, qxa, qwa, false, 0, false, 0);
  STAMP(1);
  __builtin_amdgcn_s_setprio(PRIO_A);
  bf16x8 qf[4];
#pragma unroll
  for (int m = 0; m < 4; ++m)
    qf[m] = BF(((u32x4){cvtpk(qacc[0][m][0] * QSCALE, qacc[0][m][1] * QSCALE), cvtpk(qacc[0][m][2] * QSCALE, qacc[0][m][3] * QSCALE),
                        cvtpk(qacc[1][m][0] * QSCALE, qacc[1][m][1] * QSCALE), cvtpk(qacc[1][m][2] * QSCALE, qacc[1][m][3] * QSCALE)}));
  WAIT_BAR(6);
  const LAS char* kvb = lds + l * 16;
  f32x4 negm[4], S[4][2], O[4][2];
  bf16x8 k0, k1;
  {
    const LAS char* p = kvb + 1 * SLOT;
    k0 = ldsfrag(p); k1 = ldsfrag(p + 1024);
    const f32x4 z = {0.f, 0.f, 0.f, 0.f};
#pragma unroll
    for (int m = 0; m < 4; ++m) {
      const f32x4 s0 = MFMA16(k0, qf[m], z), s1 = MFMA16(k1, qf[m], z);
      float mx = fmaxf(fmaxf(fmaxf(s0[0], s0[1]), fmaxf(s0[2], s0[3])), fmaxf(fmaxf(s1[0], s1[1]), fmaxf(s1[2], s1[3])));
      mx = xmax4(mx);
      negm[m] = (f32x4){-mx, -mx, -mx, -mx}; PIN(negm[m]);
      O[m][0] = z; O[m][1] = z;
    }
    S[0][0] = MFMA16(k0, qf[0], negm[0]); S[0][1] = MFMA16(k1, qf[0], negm[0]);
    S[1][0] = MFMA16(k0, qf[1], negm[1]); S[1][1] = MFMA16(k1, qf[1], negm[1]);
  }
  u32x4 P[4];
  P[2] = (u32x4){0, 0, 0, 0}; P[3] = P[2];
  bf16x8 va0 = BF(P[2]), va1 = va0, vb0, vb1;
  S[3][1] = (f32x4){0.f, 0.f, 0.f, 0.f};
  f32x4 L[4];
#pragma unroll
  for (int m = 0; m < 4; ++m) L[m] = (f32x4){0.f, 0.f, 0.f, 0.f};
  bf16x8 ones; { const short o1 = (l & 15) == 0 ? (short)0x3f80 : (short)0; s16x8 ov = {o1, o1, o1, o1, o1, o1, o1, o1}; ones = __builtin_bit_cast(bf16x8, ov); PIN(ones); }
#define LS(m) do { L[m] = MFMA16(ones, BF(P[m]), L[m]); PIN(L[m]); SB(); } while (0)
#define LADD2(m, kb, i)
#define NOLS(m)
#define EXPF(x) __builtin_amdgcn_exp2f(x)
#define EX2(m, kb, i) do { float t0_ = EXPF(S[m][kb][i]), t1_ = EXPF(S[m][kb][i + 1]); PIN(t0_); PIN(t1_); S[m][kb][i] = t0_; S[m][kb][i + 1] = t1_; SB(); } while (0)
#define CVA(m, kb) do { unsigned c_ = cvtpk(S[m][kb][0], S[m][kb][1]); PIN(c_); P[m][2 * kb] = c_; LADD2(m, kb, 0); SB(); } while (0)
#define CVB(m, kb) do { unsigned c_ = cvtpk(S[m][kb][2], S[m][kb][3]); PIN(c_); P[m][2 * kb + 1] = c_; LADD2(m, kb, 2); SB(); } while (0)
#define GRP(mq, kq, KF, pm, pk, me, ke, mp, db, VF, LSM, X) do { \
    S[mq][kq] = MFMA16(KF, qf[mq], negm[mq]); PIN(S[mq][kq]); SB(); X; CVB(pm, pk); EX2(me, ke, 0); \
    O[mp][db] = MFMA16(VF, BF(P[mp]), O[mp][db]); PIN(O[mp][db]); SB(); EX2(me, ke, 2); LSM(mp); CVA(me, ke); } while (0)
#define TILE(KC0, KC1, KN0, KN1, VP0, VP1, VC0, VC1, kn, vp, D) do { \
    GRP(2, 0, KC0, 3, 1, 0, 0, 2, 0, VP0, NOLS, KN0 = ldsfrag(kn); SB()); \
    GRP(2, 1, KC1, 0, 0, 0, 1, 2, 1, VP1, LS,   KN1 = ldsfrag((kn) + 1024); SB()); \
    GRP(3, 0, KC0, 0, 1, 1, 0, 3, 0, VP0, NOLS, VC0 = ldsfrag(vp); SB()); \
    GRP(3, 1, KC1, 1, 0, 1, 1, 3, 1, VP1, LS,   VC1 = ldsfrag((vp) + 1024); SB()); \
    GRP(0, 0, KN0, 1, 1, 2, 0, 0, 0, VC0, NOLS, D(0)); \
    GRP(0, 1, KN1, 2, 0, 2, 1, 0, 1, VC1, LS,   D(1)); \
    GRP(1, 0, KN0, 2, 1, 3, 0, 1, 0, VC0, NOLS, D(2)); \
    GRP(1, 1, KN1, 3, 0, 3, 1, 1, 1, VC1, LS,   D(3)); \
  } while (0)
#define NOD(i)
  bf16x8 kx0, kx1;
#define STAGE(SC, SN, SF, DM, LAST) do { \
    TILE(k0, k1, kx0, kx1, va0, va1, vb0, vb1, kvb + (SC) * SLOT + 4096, kvb + (SC) * SLOT + 2048, NOD); \
    TILE(kx0, kx1, k0, k1, vb0, vb1, va0, va1, kvb + (SC) * SLOT + 8192, kvb + (SC) * SLOT + 4096 + 2048, NOD); \
    TILE(k0, k1, kx0, kx1, va0, va1, vb0, vb1, kvb + (SC) * SLOT + 12288, kvb + (SC) * SLOT + 8192 + 2048, NOD); \
    WAIT_BAR(0);        \
    TILE(kx0, kx1, k0, k1, vb0, vb1, va0, va1, kvb + ((LAST) ? (SC) : (SN)) * SLOT, kvb + (SC) * SLOT + 12288 + 2048, DM); } while (0)
#define DM_(SF, i) do { if constexpr ((i) == 0) { dma1a_c<(SF) * SLOT, 16384>(kvm, vlin, rKV, kvso); } else { dma1b_c<(i) * 1024>(vlin, rKV, kvso); } SB(); } while (0)
#define DM0(i) DM_(0, i)
#define DM1(i) DM_(1, i)
#define DM2(i) DM_(2, i)
  for (int j = 0; j < 12; j += 3) { STAGE(1, 2, 0, DM0, false); STAGE(2, 0, 1, DM1, false); STAGE(0, 1, 2, DM2, false); STAMP(4 + j / 3); }
  STAGE(1, 2, 0, DM0, false); STAGE(2, 0, 1, DM1, false); STAGE(0, 1, 2, NOD, false); STAGE(1, 2, 0, NOD, true);
  CVB(3, 1);
  O[2][0] = MFMA16(va0, BF(P[2]), O[2][0]); O[2][1] = MFMA16(va1, BF(P[2]), O[2][1]); LS(2);
  O[3][0] = MFMA16(va0, BF(P[3]), O[3][0]); O[3][1] = MFMA16(va1, BF(P[3]), O[3][1]); LS(3);
  STAMP(2);
  const __amdgpu_buffer_rsrc_t rO = mkrsrc(ws + WS_O, 4194304);
#pragma unroll
  for (int m = 0; m < 4; ++m) {
    float ls = __shfl(L[m][0], l & 15);
    float inv = 1.0f / ls;
    f32x4 o0 = O[m][0], o1 = O[m][1];
    const float mo = fmaxf(fmaxf(fmaxf(fabsf(o0[0]), fabsf(o0[1])), fmaxf(fabsf(o0[2]), fabsf(o0[3]))), fmaxf(fmaxf(fabsf(o1[0]), fabsf(o1[1])), fmaxf(fabsf(o1[2]), fabsf(o1[3]))));
    const bool ok = (ls <= 1.0e18f) && (mo <= 1.0e37f);
    if (__builtin_expect(__any(!ok), 0)) { attn_slow(ws + WS_KV, qf[m], o0, o1); inv = 1.0f; }
    const u32x4 ch = chunk_of(cvtpk(o0[0] * inv, o0[1] * inv), cvtpk(o0[2] * inv, o0[3] * inv), cvtpk(o1[0] * inv, o1[1] * inv), cvtpk(o1[2] * inv, o1[3] * inv));
    const int c = ((g & 1) << 1) | (g >> 1);
    st_wt16(rO, ((4 * hg + w) * 128 + 4 * tb + m) * 1024 + c * 256 + (l & 15) * 16, ch);
  }
  asm volatile("s_waitcnt vmcnt(0)\n\ts_barrier" ::: "memory");
  if (w == 0 && lane_id() == 0) signal(ws, FL_O + tb * 8 + hg, tag);
  STAMP(3);
}

DI int w2_kh(int gi) { return gi & 1; }
DI void w2_weights_early(int gi, const u16* __restrict__ w2, LAS char* lds, const int w) {
  const int l = lane_id(), nt = gi >> 5;
  const __amdgpu_buffer_rsrc_t rW = mkrsrc((const char*)w2 - 1024, F_ * D_ * 2 + 1024);
  const unsigned vw0 = wdma_off(l, w, 2048) + nt * 256 + w * 8 * 2048 + 1024, vw1 = vw0 + 128 - 1024;
  unsigned wso = 56 * w2_kh(gi) * 65536 - 65536;
  const unsigned mw = (unsigned)(uintptr_t)lds + 8192 + w * 2048;
  dma1a<0 * 16384, 65536>(mw, vw0, rW, wso); dma1b<1024>(vw1, rW, wso); dma1a<1 * 16384, 65536>(mw, vw0, rW, wso); dma1b<1024>(vw1, rW, wso);
  dma1a<2 * 16384, 65536>(mw, vw0, rW, wso); dma1b<1024>(vw1, rW, wso); dma1a<3 * 16384, 65536>(mw, vw0, rW, wso); dma1b<1024>(vw1, rW, wso);
}

DI f32x4 gelu4(f32x4 acc, float rs, f32x4 b, float c1v) {
  constexpr float c2 = -2.0f * 0.7978845608028654f * 1.4426950408889634f * 0.044715f;
  float x0 = acc[0], x1 = acc[1], x2 = acc[2], x3 = acc[3], t0, t1, t2, t3;
  asm("v_fma_f32 %0, %0, %8, %9\n\tv_fma_f32 %1, %1, %8, %10\n\tv_fma_f32 %2, %2, %8, %11\n\tv_fma_f32 %3, %3, %8, %12\n\t"
      "v_mul_f32 %4, %0, %0\n\tv_mul_f32 %5, %1, %1\n\tv_mul_f32 %6, %2, %2\n\tv_mul_f32 %7, %3, %3\n\t"
      "v_fma_f32 %4, %4, %14, %13\n\tv_fma_f32 %5, %5, %14, %13\n\tv_fma_f32 %6, %6, %14, %13\n\tv_fma_f32 %7, %7, %14, %13\n\t"
      "v_mul_f32 %4, %0, %4\n\tv_mul_f32 %5, %1, %5\n\tv_mul_f32 %6, %2, %6\n\tv_mul_f32 %7, %3, %7\n\t"
      "v_exp_f32 %4, %4\n\tv_exp_f32 %5, %5\n\tv_exp_f32 %6, %6\n\tv_exp_f32 %7, %7\n\t"
      "v_add_f32 %4, 1.0, %4\n\tv_add_f32 %5, 1.0, %5\n\tv_add_f32 %6, 1.0, %6\n\tv_add_f32 %7, 1.0, %7\n\t"
      "v_rcp_f32 %4, %4\n\tv_rcp_f32 %5, %5\n\tv_rcp_f32 %6, %6\n\tv_rcp_f32 %7, %7\n\t"
      "v_mul_f32 %0, %0, %4\n\tv_mul_f32 %1, %1, %5\n\tv_mul_f32 %2, %2, %6\n\tv_mul_f32 %3, %3, %7"
      : "+v"(x0), "+v"(x1), "+v"(x2), "+v"(x3), "=&v"(t0), "=&v"(t1), "=&v"(t2), "=&v"(t3)
      : "v"(rs), "v"(b[0]), "v"(b[1]), "v"(b[2]), "v"(b[3]), "v"(c1v), "s"(c2));
  return (f32x4){x0, x1, x2, x3};
}

DI u64 role_w1(int gi, const u16* __restrict__ wq, const u16* __restrict__ w1, const u16* __restrict__ b1, const u16* __restrict__ w2, char* __restrict__ ws, LAS char* lds, const int w, u64 eraw) {
  const int l = lane_id();
  const int xcd = gi & 7, idx = gi >> 3, p = (xcd >> 2) * 8 + (idx & 7), j = (xcd & 3) * 4 + (idx >> 3);
  const int g = l >> 4, wm = w >> 1, wn = w & 1; const int stamp_base = 256; (void)stamp_base;
  const unsigned lds0 = (unsigned)(uintptr_t)lds;
  constexpr int SLOT = 24576;
  const __amdgpu_buffer_rsrc_t rX = mkrsrc(ws + WS_XN, 4194304), rW = mkrsrc((const char*)w1 - 4096, D_ * F_ * 2 + 4096);
  const unsigned vlin = l * 16;
  unsigned vw[4];
#pragma unroll
  for (int i = 0; i < 4; ++i) vw[i] = wdma_off(l, w, F_ * 2) + 8 * w * (F_ * 2) + 448 * j - 64 * (j & 1) + 128 * i + 4096 - 1024 * i;
  STAMP(0);
  unsigned xso = (8 * p + 2 * w) * 1024 - 131072, wso = 0u - 32 * F_ * 2;
  const unsigned mx = lds0 + w * 2048, mw = lds0 + 8192 + w * 4096;
#define PC1(SL, i) do { if constexpr ((i) == 0) dma1a_c<(SL) * SLOT, 131072>(mx, vlin, rX, xso); else if constexpr ((i) == 1) dma1b_c<1024>(vlin, rX, xso); \
    else if constexpr ((i) == 2) dma1a<(SL) * SLOT, 32 * F_ * 2>(mw, vw[0], rW, wso); else dma1b<((i) - 2) * 1024>(vw[(i) - 2], rW, wso); } while (0)
#define STG1(SL) do { PC1(SL, 0); PC1(SL, 1); PC1(SL, 2); PC1(SL, 3); PC1(SL, 4); PC1(SL, 5); } while (0)
  PC1(0, 2); PC1(0, 3); PC1(0, 4); PC1(0, 5); PC1(1, 2); PC1(1, 3); PC1(1, 4); PC1(1, 5); PC1(2, 2); PC1(2, 3); PC1(2, 4); PC1(2, 5);
  constexpr int L_R2 = 3 * SLOT, L_B1 = L_R2 + 512;
  const unsigned vpf = l < 4 ? (4 * (idx & 7) + w) * (F_ * 2) + 448 * j - 64 * (j & 1) + l * 128 + 4096 : 0x80000000u;
  const unsigned mpf = lds0 + L_B1 + 1024;
  dma4_ahead<1 * 32 * F_ * 2>(mpf, vpf, rW, wso); dma4_ahead<2 * 32 * F_ * 2>(mpf, vpf, rW, wso); dma4_ahead<3 * 32 * F_ * 2>(mpf, vpf, rW, wso);
  dma4_ahead<4 * 32 * F_ * 2>(mpf, vpf, rW, wso); dma4_ahead<5 * 32 * F_ * 2>(mpf, vpf, rW, wso);
  if (w == 1) dma4(mpf, (gi * 64 + l) * 128, mkrsrc(wq, D_ * D_ * 2), 0);
  if (w >= 2 && PFROWS > 0) { constexpr int NL = PFROWS * 56 / 512;
#pragma unroll
    for (int i = 0; i < (NL + 63) / 64; ++i) dma4(mpf, i * 64 + l < NL ? 256 * F_ * 2 + ((gi * 2 + (w - 2)) * NL + i * 64 + l) * 128 : 0x80000000u, mkrsrc(w1, D_ * F_ * 2), 0);
  }
  if (w >= 2) dma4(lds0 + L_B1 + (w - 2) * 256, 448 * j + ((w - 2) * 64 + l) * 4, mkrsrc(b1, F_ * 2), 0);
  asm volatile("s_waitcnt vmcnt(17)" : "+v"(eraw));
  const u64 tag = take_tag(ws, eraw, w);
  if (w == 0) await(ws, l < 16 ? FL_XN + 8 * p + (l & 7) + 128 * (l >> 3) : -1, tag);
  asm volatile("s_barrier" ::: "memory");
  STAMP(8);
  if (w < 2) dma4_c(lds0 + L_R2 + w * 256, (128 * p + 64 * w + l) * 4, mkrsrc(ws + WS_R2, 8192), 0);
  PC1(0, 0); PC1(0, 1); PC1(1, 0); PC1(1, 1); PC1(2, 0); PC1(2, 1);
  dma4(mpf, 0x80000000u, rW, 0);
  f32x4 acc[4][7];
#pragma unroll
  for (int m = 0; m < 4; ++m)
#pragma unroll
    for (int n = 0; n < 7; ++n) acc[m][n] = (f32x4){0.f, 0.f, 0.f, 0.f};
  const LAS char* xrd = lds + 4 * wm * 1024 + l * 16;
  const LAS char* wrb[5];
  { const int off = 6 * wn + 2 * (j & 1), nl = 12 + wn + 2 * (j & 1);
#pragma unroll
    for (int i = 0; i < 4; ++i) wrb[i] = lds + 8192 + (g * 4 + ((i + off) >> 2)) * 1024 + wrd_off(l, (i + off) & 3);
    wrb[4] = lds + 8192 + (g * 4 + (nl >> 2)) * 1024 + wrd_off(l, nl & 3); }
  bf16x8 xa[4], wa[7], xb[4], wb[7];
#define RDX(X, m, so) X[m] = ldsfrag(xrd + (so) + (m) * 1024)
#define RDW(W, n, so) W[n] = ((n) < 6 ? ldstr(wrb[(n) & 3] + (so) + ((n) >> 2) * 1024, wrb[(n) & 3] + (so) + ((n) >> 2) * 1024 + 512) : ldstr(wrb[4] + (so), wrb[4] + (so) + 512))
#define M1(X, W, m, n) acc[m][n] = MFMA16(W[n], X[m], acc[m][n])
#define STEP1(X, W, XN, WN, RD, RS, DMA, DS) do { \
    M1(X, W, 0, 0); if (RD) RDW(WN, 0, (RS) * SLOT); SB(); M1(X, W, 1, 0); if (RD) RDX(XN, 0, (RS) * SLOT); SB(); M1(X, W, 2, 0); if (RD) RDW(WN, 1, (RS) * SLOT); SB(); M1(X, W, 3, 0); if (DMA) PC1(DS, 0); SB(); \
    M1(X, W, 0, 1); if (RD) RDX(XN, 1, (RS) * SLOT); SB(); M1(X, W, 1, 1); if (RD) RDW(WN, 2, (RS) * SLOT); SB(); M1(X, W, 2, 1); if (RD) RDX(XN, 2, (RS) * SLOT); SB(); M1(X, W, 3, 1); if (DMA) PC1(DS, 1); SB(); \
    M1(X, W, 0, 2); if (RD) RDW(WN, 3, (RS) * SLOT); SB(); M1(X, W, 1, 2); if (RD) RDX(XN, 3, (RS) * SLOT); SB(); M1(X, W, 2, 2); if (RD) RDW(WN, 4, (RS) * SLOT); SB(); M1(X, W, 3, 2); if (DMA) PC1(DS, 2); SB(); \
    M1(X, W, 0, 3); if (RD) RDW(WN, 5, (RS) * SLOT); SB(); M1(X, W, 1, 3); SB(); M1(X, W, 2, 3); if (RD) RDW(WN, 6, (RS) * SLOT); SB(); M1(X, W, 3, 3); if (DMA) PC1(DS, 3); SB(); \
    M1(X, W, 0, 4); SB(); M1(X, W, 1, 4); SB(); M1(X, W, 2, 4); SB(); M1(X, W, 3, 4); if (DMA) PC1(DS, 4); SB(); \
    M1(X, W, 0, 5); SB(); M1(X, W, 1, 5); SB(); M1(X, W, 2, 5); SB(); M1(X, W, 3, 5); if (DMA) { PC1(DS, 5); dma4_ahead<5 * 32 * F_ * 2>(mpf, vpf, rW, wso); } SB(); \
    M1(X, W, 0, 6); SB(); M1(X, W, 1, 6); SB(); M1(X, W, 2, 6); SB(); M1(X, W, 3, 6); SB(); } while (0)
  WAIT_BAR(3);
#pragma unroll
  for (int m = 0; m < 4; ++m) RDX(xa, m, 0);
#pragma unroll
  for (int n = 0; n < 7; ++n) RDW(wa, n, 0);
#define EV(SC, SN, W_, RD, DMA) do { WAIT_BAR(W_); SB(); STEP1(xa, wa, xb, wb, RD, SN, DMA, SC); } while (0)
#define OD(SC, SN, W_, RD, DMA) do { WAIT_BAR(W_); SB(); STEP1(xb, wb, xa, wa, RD, SN, DMA, SC); } while (0)
#define PR1(i) do { if constexpr ((i) == 0 || (((G1PAT >> (i)) & 1) != ((G1PAT >> ((i) - 1 < 0 ? 0 : (i) - 1)) & 1))) __builtin_amdgcn_s_setprio(((G1PAT >> (i)) & 1) ? 2 : 0); } while (0)
#define SIX() do { PR1(0); EV(0, 1, 8, true, true); PR1(1); OD(1, 2, 8, true, true); PR1(2); EV(2, 0, 8, true, true); PR1(3); OD(0, 1, 8, true, true); PR1(4); EV(1, 2, 8, true, true); PR1(5); OD(2, 0, 8, true, true); } while (0)
  for (int it = 0; it < 4; ++it) { SIX(); STAMP(4 + it); }
  EV(0, 1, 8, true, true); OD(1, 2, 8, true, true); EV(2, 0, 8, true, true); OD(0, 1, 8, true, true); EV(1, 2, 8, true, true);
  OD(2, 0, 8, true, false); EV(0, 1, 0, true, false); OD(1, 2, 0, false, false);
  STAMP(1);
  __builtin_amdgcn_s_setprio(PRIO_GE);
  w2_weights_early(gi, w2, lds, w);
  const int le = lane_id(), ge = le >> 4;
  float r2[4];
#pragma unroll
  for (int m = 0; m < 4; ++m) r2[m] = *(const LAS float*)(lds + L_R2 + (64 * wm + 16 * m + (le & 15)) * 4);
  const __amdgpu_buffer_rsrc_t rH = mkrsrc(ws + WS_H, 112 * 128 * 1024);
  const int cch = ((ge & 1) << 1) | (ge >> 1);
  unsigned last[4][2];
  float c1v = -2.0f * 0.7978845608028654f * 1.4426950408889634f; PIN(c1v);
#pragma unroll
  for (int n = 0; n < 7; ++n) {
    const int nb = n < 6 ? 6 * wn + n : 12 + wn;
    const u32x2 braw = *(const LAS u32x2*)(lds + L_B1 + (16 * nb + 4 * ge) * 2);
    const f32x4 bb = {lo2f(braw[0]), hi2f(braw[0]), lo2f(braw[1]), hi2f(braw[1])};
#pragma unroll
    for (int m = 0; m < 4; ++m) acc[m][n] = gelu4(acc[m][n], r2[m], bb, c1v);
    if (n < 6) { if (n & 1) {
#pragma unroll
        for (int m = 0; m < 4; ++m) {
          const u32x4 ch = chunk_of(cvtpk(acc[m][n - 1][0], acc[m][n - 1][1]), cvtpk(acc[m][n - 1][2], acc[m][n - 1][3]), cvtpk(acc[m][n][0], acc[m][n][1]), cvtpk(acc[m][n][2], acc[m][n][3]));
          st_wt16(rH, ((7 * j + 3 * wn + (n >> 1)) * 128 + 8 * p + 4 * wm + m) * 1024 + cch * 256 + (le & 15) * 16, ch);
        } }
    } else {
#pragma unroll
      for (int m = 0; m < 4; ++m) { last[m][0] = cvtpk(acc[m][n][0], acc[m][n][1]); last[m][1] = cvtpk(acc[m][n][2], acc[m][n][3]); }
    }
  }
#pragma unroll
  for (int m = 0; m < 4; m += 2) {
    const u32x4 ch = chunk_of(last[m][0], last[m][1], last[m + 1][0], last[m + 1][1]);
    st_wt16(rH, ((7 * j + 6) * 128 + 8 * p + 4 * wm + m + (ge & 1)) * 1024 + (2 * wn + (ge >> 1)) * 256 + (le & 15) * 16, ch);
  }
  STAMP(2);
  __builtin_amdgcn_s_setprio(PRIO_G2);
  asm volatile("s_waitcnt vmcnt(0)\n\ts_barrier" ::: "memory");
  if (w == 0 && lane_id() == 0) signal(ws, FL_H + p * 16 + j, tag);
  STAMP(3);
#undef RDX
#undef RDW
#undef EV
#undef OD
  return tag;
}

DI void role_w2(int gi, const u16* __restrict__ w2, const u16* __restrict__ wo, const u16* __restrict__ b2, const u16* __restrict__ x, u16* __restrict__ out, char* __restrict__ ws, LAS char* lds, const int w, const u64 tag) {
  const int l = lane_id();
  const int xcd = gi & 7, idx = gi >> 3, kh = xcd & 1, p = (xcd >> 1) * 4 + (idx & 3), nt = idx >> 2;
  const int partner = gi ^ 1;
  const int g = l >> 4, wm = w >> 1, wn = w & 1; const int stamp_base = 512; (void)stamp_base;
  const unsigned lds0 = (unsigned)(uintptr_t)lds;
  constexpr int SLOT = 16384;
#define AINC_ 131072
#define WINC_ 65536
  const __amdgpu_buffer_rsrc_t rA = mkrsrc(ws + WS_H, 144 * 128 * 1024);
  __amdgpu_buffer_rsrc_t rW = mkrsrc((const char*)w2 - 1024, F_ * D_ * 2 + 1024);
  const unsigned vlin = l * 16;
  const unsigned vw0 = wdma_off(l, w, 2048) + nt * 256 + w * 8 * 2048 + 1024, vw1 = vw0 + 128 - 1024;
  if (w == 0) await(ws, l < 8 ? FL_H + p * 16 + 8 * kh + l : -1, tag);
  asm volatile("s_barrier" ::: "memory");
  STAMP(0);
  unsigned aso = (56 * kh * 128 + 8 * p + 2 * w) * 1024 - AINC_, wso = 56 * kh * 65536 + 3 * 65536;
  const unsigned ma = lds0 + w * 2048, mw = lds0 + 8192 + w * 2048;
#define AINC 131072
#define WINC 65536
#define PC2(SL, i) do { if constexpr ((i) == 0) dma1a_c<(SL) * SLOT, AINC>(ma, vlin, rA, aso); else if constexpr ((i) == 1) dma1b_c<1024>(vlin, rA, aso); \
    else if constexpr ((i) == 2) dma1a<(SL) * SLOT, WINC>(mw, vw0, rW, wso); else dma1b<1024>(vw1, rW, wso); } while (0)
#define STG2(SL) do { PC2(SL, 0); PC2(SL, 1); PC2(SL, 2); PC2(SL, 3); } while (0)
  PC2(0, 0); PC2(0, 1); PC2(1, 0); PC2(1, 1); PC2(2, 0); PC2(2, 1); PC2(3, 0); PC2(3, 1); STG2(4);
  f32x4 acc[4][4];
#pragma unroll
  for (int m = 0; m < 4; ++m)
#pragma unroll
    for (int n = 0; n < 4; ++n) acc[m][n] = (f32x4){0.f, 0.f, 0.f, 0.f};
  const LAS char* xrd = lds + 4 * wm * 1024 + l * 16;
  const LAS char* wrb[4];
#pragma unroll
  for (int i = 0; i < 4; ++i) wrb[i] = lds + 8192 + (g * 2 + wn) * 1024 + wrd_off(l, i);
  bf16x8 xa[4], wa[4], xb[4], wb[4];
#define RDX(X, m, so) X[m] = ldsfrag(xrd + (so) + (m) * 1024)
#define RDW(W, n, so) W[n] = ldstr(wrb[n] + (so), wrb[n] + (so) + 512)
#define M2(X, W, m, n) acc[m][n] = MFMA16(W[n], X[m], acc[m][n])
#define STEP2(X, W, XN, WN, RD, RS, DMA, DS) do { \
    M2(X, W, 0, 0); if (RD) RDW(WN, 0, (RS) * SLOT); SB(); M2(X, W, 1, 0); if (RD) RDX(XN, 0, (RS) * SLOT); SB(); M2(X, W, 2, 0); if (RD) RDX(XN, 1, (RS) * SLOT); SB(); M2(X, W, 3, 0); if (DMA) PC2(DS, 0); SB(); \
    M2(X, W, 0, 1); if (RD) RDW(WN, 1, (RS) * SLOT); SB(); M2(X, W, 1, 1); if (RD) RDX(XN, 2, (RS) * SLOT); SB(); M2(X, W, 2, 1); if (RD) RDX(XN, 3, (RS) * SLOT); SB(); M2(X, W, 3, 1); if (DMA) PC2(DS, 1); SB(); \
    M2(X, W, 0, 2); if (RD) RDW(WN, 2, (RS) * SLOT); SB(); M2(X, W, 1, 2); if (RD) RDW(WN, 3, (RS) * SLOT); SB(); M2(X, W, 2, 2); SB(); M2(X, W, 3, 2); if (DMA) PC2(DS, 2); SB(); \
    M2(X, W, 0, 3); SB(); M2(X, W, 1, 3); SB(); M2(X, W, 2, 3); SB(); M2(X, W, 3, 3); if (DMA) PC2(DS, 3); SB(); } while (0)
  WAIT_BAR(6);
#pragma unroll
  for (int i = 0; i < 4; ++i) { RDX(xa, i, 0); RDW(wa, i, 0); }
#define EV(SC, SN, W_, RD, DMA) do { WAIT_BAR(W_); SB(); STEP2(xa, wa, xb, wb, RD, SN, DMA, SC); } while (0)
#define OD(SC, SN, W_, RD, DMA) do { WAIT_BAR(W_); SB(); STEP2(xb, wb, xa, wa, RD, SN, DMA, SC); } while (0)
#define PR2(i) do { if constexpr ((i) == 0 || (((G2PAT >> (i)) & 1) != ((G2PAT >> ((i) - 1 < 0 ? 0 : (i) - 1)) & 1))) __builtin_amdgcn_s_setprio(((G2PAT >> (i)) & 1) ? 2 : 0); } while (0)
  u64 oflag = 0;
  for (int it = 0; it < 6; ++it) {
    STAMP(4 + it);
    PR2(0);
    EV(0, 1, 12, true, true);
    if (it == 5) {
      if (w == 0) {
        asm volatile("" : "+v"(oflag));
        if (!__all(l >= 8 || oflag == tag)) await(ws, l < 8 ? FL_O + (2 * p + (l >> 2)) * 8 + 4 * kh + (l & 3) : -1, tag);
      }
      rW = mkrsrc((const char*)wo - 1024, D_ * D_ * 2 + 1024);
      aso = ((112 + 16 * kh) * 128 + 8 * p + 2 * w) * 1024 - 131072; wso = 16 * kh * 65536 - 65536;
    }
    PR2(1); OD(1, 2, 12, true, true); PR2(2); EV(2, 3, 12, true, true); PR2(3); OD(3, 4, 12, true, true); PR2(4); EV(4, 0, 12, true, true);
    PR2(5); OD(0, 1, 12, true, true); PR2(6); EV(1, 2, 12, true, true);
    if (it == 4 && w == 0) {
      const GAS u64* fo = flagp(ws, FL_O + (2 * p + ((l >> 2) & 1)) * 8 + 4 * kh + (l & 3));
      asm volatile("global_load_dwordx2 %0, %1, off sc1" : "=&v"(oflag) : "v"(fo));
    }
    PR2(7); OD(2, 3, 12, true, true); PR2(8); EV(3, 4, 12, true, true); PR2(9); OD(4, 0, 12, true, true);
  }
  EV(0, 1, 12, true, true); OD(1, 2, 12, true, true); EV(2, 3, 12, true, true); OD(3, 4, 12, true, true); EV(4, 0, 12, true, true); OD(0, 1, 12, true, true); EV(1, 2, 12, true, true);
  u32x2 xres[2][4], bres[4];
  { const int le = lane_id();
    const u16* xp = x + (size_t)(128 * p + 64 * wm + 32 * kh + (le & 15)) * D_ + 128 * nt + 64 * wn + 4 * (le >> 4); const u16* xq = xp + 16 * D_; const u16* bp = b2 + 128 * nt + 64 * wn + 4 * (le >> 4);
    asm volatile("global_load_dwordx2 %0, %8, off\n\tglobal_load_dwordx2 %1, %8, off offset:32\n\tglobal_load_dwordx2 %2, %8, off offset:64\n\tglobal_load_dwordx2 %3, %8, off offset:96\n\t"
                 "global_load_dwordx2 %4, %9, off\n\tglobal_load_dwordx2 %5, %9, off offset:32\n\tglobal_load_dwordx2 %6, %9, off offset:64\n\tglobal_load_dwordx2 %7, %9, off offset:96"
                 : "=&v"(xres[0][0]), "=&v"(xres[0][1]), "=&v"(xres[0][2]), "=&v"(xres[0][3]), "=&v"(xres[1][0]), "=&v"(xres[1][1]), "=&v"(xres[1][2]), "=&v"(xres[1][3]) : "v"(xp), "v"(xq));
    asm volatile("global_load_dwordx2 %0, %4, off\n\tglobal_load_dwordx2 %1, %4, off offset:32\n\tglobal_load_dwordx2 %2, %4, off offset:64\n\tglobal_load_dwordx2 %3, %4, off offset:96"
                 : "=&v"(bres[0]), "=&v"(bres[1]), "=&v"(bres[2]), "=&v"(bres[3]) : "v"(bp)); }
  OD(2, 3, 24, true, false); EV(3, 4, 20, true, false); OD(4, 0, 16, true, false); EV(0, 1, 12, true, false); OD(1, 2, 12, false, false);
  STAMP(1);
  if (kh) {
#pragma unroll
    for (int n = 0; n < 4; ++n) { f32x4 t = acc[0][n]; acc[0][n] = acc[2][n]; acc[2][n] = t; t = acc[1][n]; acc[1][n] = acc[3][n]; acc[3][n] = t; }
#pragma unroll
    for (int n = 0; n < 4; ++n) { PIN(acc[0][n]); PIN(acc[1][n]); PIN(acc[2][n]); PIN(acc[3][n]); }
  }
  const int le = lane_id(), ge = le >> 4;
  const int token0 = 128 * p + 64 * wm + 32 * kh + (le & 15);
  {
    const __amdgpu_buffer_rsrc_t rP = mkrsrc(ws + WS_PART + (size_t)gi * 32768, 16384);
#pragma unroll
    for (int m = 0; m < 2; ++m)
#pragma unroll
      for (int n2 = 0; n2 < 2; ++n2)
        st_wt16(rP, ((w * 2 + m) * 2 + n2) * 1024 + le * 16, (u32x4){cvtpk(acc[2 + m][2 * n2][0], acc[2 + m][2 * n2][1]), cvtpk(acc[2 + m][2 * n2][2], acc[2 + m][2 * n2][3]),
                                                                       cvtpk(acc[2 + m][2 * n2 + 1][0], acc[2 + m][2 * n2 + 1][1]), cvtpk(acc[2 + m][2 * n2 + 1][2], acc[2 + m][2 * n2 + 1][3])});
    asm volatile("s_waitcnt vmcnt(0)\n\ts_barrier" : "+v"(xres[0][0]), "+v"(xres[0][1]), "+v"(xres[0][2]), "+v"(xres[0][3]), "+v"(xres[1][0]), "+v"(xres[1][1]), "+v"(xres[1][2]), "+v"(xres[1][3]),
                 "+v"(bres[0]), "+v"(bres[1]), "+v"(bres[2]), "+v"(bres[3]) :: "memory");
    if (w == 0) { if (le == 0) signal(ws, FL_X + gi, tag); await(ws, FL_X + partner, tag); }
    asm volatile("s_barrier" ::: "memory");
  }
  STAMP(2);
  const __amdgpu_buffer_rsrc_t rPP = mkrsrc(ws + WS_PART + (size_t)partner * 32768, 16384);
  u32x4 pv[2][2];
#pragma unroll
  for (int m = 0; m < 2; ++m)
#pragma unroll
    for (int n2 = 0; n2 < 2; ++n2) pv[m][n2] = __builtin_amdgcn_raw_buffer_load_b128(rPP, ((w * 2 + m) * 2 + n2) * 1024 + le * 16, 0, 16);
#pragma unroll
  for (int m = 0; m < 2; ++m) {
    unsigned d[4][2];
#pragma unroll
    for (int n = 0; n < 4; ++n) {
      const unsigned p0 = pv[m][n >> 1][2 * (n & 1)], p1 = pv[m][n >> 1][2 * (n & 1) + 1];
      const f32x4 a4 = acc[m][n];
      d[n][0] = cvtpk(a4[0] + lo2f(p0) + lo2f(bres[n][0]) + lo2f(xres[m][n][0]), a4[1] + hi2f(p0) + hi2f(bres[n][0]) + hi2f(xres[m][n][0]));
      d[n][1] = cvtpk(a4[2] + lo2f(p1) + lo2f(bres[n][1]) + lo2f(xres[m][n][1]), a4[3] + hi2f(p1) + hi2f(bres[n][1]) + hi2f(xres[m][n][1]));
    }
    const int cch = ((ge & 1) << 1) | (ge >> 1);
#pragma unroll
    for (int n2 = 0; n2 < 2; ++n2) {
      const u32x4 ch = chunk_of(d[2 * n2][0], d[2 * n2][1], d[2 * n2 + 1][0], d[2 * n2 + 1][1]);
      *(u32x4*)(out + (size_t)(token0 + 16 * m) * D_ + 128 * nt + 64 * wn + 32 * n2 + 8 * cch) = ch;
    }
  }
  STAMP(3);
#undef RDX
#undef RDW
}

constexpr int LDS_BYTES = 81920;
#define MODE 0
__global__ __launch_bounds__(256, 2) void k_main(const u16* __restrict__ x, const u16* __restrict__ wq, const u16* __restrict__ wk, const u16* __restrict__ wv, const u16* __restrict__ wo,
                                                  const u16* __restrict__ w1, const u16* __restrict__ b1, const u16* __restrict__ w2, const u16* __restrict__ b2, u16* __restrict__ out, char* __restrict__ ws) {
  extern __shared__ __attribute__((aligned(16))) char lds_[];
  LAS char* lds = (LAS char*)lds_;
  const int w = __builtin_amdgcn_readfirstlane(threadIdx.x >> 6);
  const unsigned long long t_in = __builtin_amdgcn_s_memrealtime();
  u64 eraw;
  { const GAS u64* ep = flagp(ws, FL_E); asm volatile("global_load_dwordx2 %0, %1, off sc1" : "=&v"(eraw) : "v"(ep)); }
  if ((blockIdx.x >> 8) == 0) {
    __builtin_amdgcn_s_setprio(PRIO_Q);
    const u64 tag = phase0(blockIdx.x, x, wk, wv, ws, lds, w, eraw);
    role_attn(blockIdx.x, wq, w2, wo, ws, lds, w, tag);
  } else {
    __builtin_amdgcn_s_setprio(PRIO_G1);
    const u64 tag = role_w1(blockIdx.x & 255, wq, w1, b1, w2, ws, lds, w, eraw);
    role_w2(blockIdx.x & 255, w2, wo, b2, x, out, ws, lds, w, tag);
    if (__builtin_amdgcn_s_memrealtime() - t_in > (unsigned long long)(TLIM) * 100ull) { asm volatile("s_waitcnt vmcnt(0)" ::: "memory"); if (threadIdx.x == 0) out[(blockIdx.x & 255) * 8192] = 0x7fc0; }
    if (blockIdx.x == 256 && w == 0) {
      const int l = lane_id();
#pragma unroll 1
      for (int i = 0; i < 8; ++i) await(ws, FL_RD + 64 * i + l, tag);
      if (l == 0) signal(ws, FL_E, tag);
    }
  }
}

extern "C" void kernel_launch(void* const* d_in, const int* in_sizes, int n_in, void* d_out, int out_size, void* d_ws, size_t ws_size, hipStream_t stream) {
  const u16* x = (const u16*)d_in[0]; const u16* wq = (const u16*)d_in[1]; const u16* wk = (const u16*)d_in[2];
  const u16* wv = (const u16*)d_in[3]; const u16* wo = (const u16*)d_in[4];
  const u16* w1 = (const u16*)d_in[6]; const u16* b1 = (const u16*)d_in[7]; const u16* w2 = (const u16*)d_in[8]; const u16* b2 = (const u16*)d_in[9];
  (void)hipFuncSetAttribute((const void*)k_main, hipFuncAttributeMaxDynamicSharedMemorySize, LDS_BYTES);
  k_main<<<512, 256, LDS_BYTES, stream>>>(x, wq, wk, wv, wo, w1, b1, w2, b2, (u16*)d_out, (char*)d_ws);
}
```

```cpp
#include <hip/hip_runtime.h>
#include <stdint.h>
#define OUTAUX 16

typedef unsigned short u16;
typedef __bf16 bf16x8 __attribute__((ext_vector_type(8)));
typedef __bf16 bf16x2 __attribute__((ext_vector_type(2)));
typedef short s16x8 __attribute__((ext_vector_type(8)));
typedef float f32x4 __attribute__((ext_vector_type(4)));
typedef float f32x2 __attribute__((ext_vector_type(2)));
typedef unsigned u32x4 __attribute__((ext_vector_type(4)));
typedef unsigned u32x2 __attribute__((ext_vector_type(2)));
#define LAS __attribute__((address_space(3)))
#define GAS __attribute__((address_space(1)))
#define DI __device__ __forceinline__
#define SB() __builtin_amdgcn_sched_barrier(0)
#define PIN(x) asm volatile("" : "+v"(x))
#define MFMA16(a, b, c) __builtin_amdgcn_mfma_f32_16x16x32_bf16((a), (b), (c), 0, 0, 0)
#define BF(x) __builtin_bit_cast(bf16x8, x)

#define PF0ROWS 1024
constexpr int S_ = 2048, D_ = 1024, F_ = 3584;
constexpr size_t WS_FLAGS = 0;
constexpr size_t WS_R2   = 131072;
constexpr size_t WS_KV   = WS_R2 + 8192;
constexpr size_t WS_XN   = WS_KV + 262144;
constexpr size_t WS_H    = WS_XN + 4194304;
constexpr size_t WS_O    = WS_H + (size_t)112 * 128 * 1024;
constexpr size_t WS_PART = WS_O + 4194304;
constexpr size_t WS_STAMP = WS_PART + (size_t)2 * 256 * 32768;

constexpr float QSCALE = 0.17677669529663687f * 1.4426950408889634f;

DI int lane_id() { int l; asm volatile("v_mbcnt_lo_u32_b32 %0, -1, 0\n\tv_mbcnt_hi_u32_b32 %0, -1, %0" : "=v"(l)); return l; }
DI float bf2f(u16 v) { return __uint_as_float(((unsigned)v) << 16); }
DI unsigned cvtpk(float lo, float hi) { f32x2 v = {lo, hi}; bf16x2 b = __builtin_convertvector(v, bf16x2); return __builtin_bit_cast(unsigned, b); }
DI float lo2f(unsigned u) { return __uint_as_float(u << 16); }
DI float hi2f(unsigned u) { return __uint_as_float(u & 0xffff0000u); }
DI __amdgpu_buffer_rsrc_t mkrsrc(const void* p, unsigned bytes) { return __builtin_amdgcn_make_buffer_rsrc((void*)p, 0, bytes, 0x00020000); }
DI void dma16(unsigned lds_addr, unsigned voff, __amdgpu_buffer_rsrc_t rsrc, unsigned soff) {
  asm volatile("s_mov_b32 m0, %0\n\ts_nop 0\n\tbuffer_load_dwordx4 %1, %2, %3 offen lds" :: "s"(lds_addr), "v"(voff), "s"(rsrc), "s"(soff));
}
template <int IMM, int INC> DI void dma2(unsigned mbase, unsigned v0, unsigned v1, __amdgpu_buffer_rsrc_t rsrc, unsigned& soff) {
  asm volatile("s_add_u32 m0, %3, %5\n\ts_add_u32 %0, %0, %6\n\tbuffer_load_dwordx4 %1, %4, %0 offen lds\n\tbuffer_load_dwordx4 %2, %4, %0 offen offset:1024 lds"
               : "+s"(soff) : "v"(v0), "v"(v1), "s"(mbase), "s"(rsrc), "i"(IMM), "i"(INC) : "scc");
}
template <int IMM, int INC> DI void dma1a(unsigned mbase, unsigned v0, __amdgpu_buffer_rsrc_t rsrc, unsigned& soff) {
  asm volatile("s_add_u32 m0, %2, %4\n\ts_add_u32 %0, %0, %5\n\tbuffer_load_dwordx4 %1, %3, %0 offen lds" : "+s"(soff) : "v"(v0), "s"(mbase), "s"(rsrc), "i"(IMM), "i"(INC) : "scc");
}
template <int OFF> DI void dma1b(unsigned v0, __amdgpu_buffer_rsrc_t rsrc, unsigned soff) {
  asm volatile("buffer_load_dwordx4 %0, %1, %2 offen offset:%3 lds" :: "v"(v0), "s"(rsrc), "s"(soff), "i"(OFF));
}
template <int IMM, int INC> DI void dma1a_c(unsigned mbase, unsigned v0, __amdgpu_buffer_rsrc_t rsrc, unsigned& soff) {
  asm volatile("s_add_u32 m0, %2, %4\n\ts_add_u32 %0, %0, %5\n\tbuffer_load_dwordx4 %1, %3, %0 offen sc1 lds" : "+s"(soff) : "v"(v0), "s"(mbase), "s"(rsrc), "i"(IMM), "i"(INC) : "scc");
}
template <int OFF> DI void dma1b_c(unsigned v0, __amdgpu_buffer_rsrc_t rsrc, unsigned soff) {
  asm volatile("buffer_load_dwordx4 %0, %1, %2 offen offset:%3 sc1 lds" :: "v"(v0), "s"(rsrc), "s"(soff), "i"(OFF));
}
DI void dma4(unsigned lds_addr, unsigned voff, __amdgpu_buffer_rsrc_t rsrc, unsigned soff) {
  asm volatile("s_mov_b32 m0, %0\n\ts_nop 0\n\tbuffer_load_dword %1, %2, %3 offen lds" :: "s"(lds_addr), "v"(voff), "s"(rsrc), "s"(soff));
}
DI void dma4_c(unsigned lds_addr, unsigned voff, __amdgpu_buffer_rsrc_t rsrc, unsigned soff) {
  asm volatile("s_mov_b32 m0, %0\n\ts_nop 0\n\tbuffer_load_dword %1, %2, %3 offen sc1 lds" :: "s"(lds_addr), "v"(voff), "s"(rsrc), "s"(soff));
}
template <int ADD> DI void dma4_ahead(unsigned lds_addr, unsigned voff, __amdgpu_buffer_rsrc_t rsrc, unsigned soff) {
  unsigned t;
  asm volatile("s_mov_b32 m0, %1\n\ts_add_u32 %0, %4, %5\n\tbuffer_load_dword %2, %3, %0 offen lds" : "=&s"(t) : "s"(lds_addr), "v"(voff), "s"(rsrc), "s"(soff), "i"(ADD) : "scc");
}
#define WAIT_BAR(N) asm volatile("s_waitcnt vmcnt(" #N ") lgkmcnt(0)\n\ts_barrier" ::: "memory")
DI bf16x8 ldsfrag(const LAS char* p) { return *(const LAS bf16x8*)p; }
typedef short v4i16_t __attribute__((ext_vector_type(4)));
DI bf16x8 ldstr(const LAS char* p0, const LAS char* p1) {
  v4i16_t a = __builtin_amdgcn_ds_read_tr16_b64_v4i16((LAS v4i16_t*)p0);
  v4i16_t b = __builtin_amdgcn_ds_read_tr16_b64_v4i16((LAS v4i16_t*)p1);
  s16x8 v = __builtin_shufflevector(a, b, 0, 1, 2, 3, 4, 5, 6, 7);
  return __builtin_bit_cast(bf16x8, v);
}
DI void st_wt16(__amdgpu_buffer_rsrc_t r, unsigned voff, u32x4 v) { __builtin_amdgcn_raw_buffer_store_b128(v, r, voff, 0, 16); }
DI u32x4 chunk_of(unsigned x0, unsigned x1, unsigned y0, unsigned y1) {
  auto s0 = __builtin_amdgcn_permlane16_swap(x0, y0, false, false);
  auto s1 = __builtin_amdgcn_permlane16_swap(x1, y1, false, false);
  return (u32x4){s0[0], s1[0], s0[1], s1[1]};
}
DI unsigned wdma_off(int l, int kblk, int ldbytes) {
  const int row = l >> 3, c8 = l & 7, sw = ((row >> 1) & 1) | ((kblk & 1) << 1);
  return row * ldbytes + (((c8 >> 1) ^ sw) * 32) + (c8 & 1) * 16;
}
DI unsigned wrd_off(int l, int seg) {
  const int g = l >> 4, q = (l & 15) >> 2, sw = ((q >> 1) & 1) | ((g & 1) << 1);
  return q * 128 + ((seg ^ sw) * 32) + (l & 3) * 8;
}
DI float xsum4(float v) {
  auto a = __builtin_amdgcn_permlane16_swap(__float_as_uint(v), __float_as_uint(v), false, false); v = __uint_as_float(a[0]) + __uint_as_float(a[1]);
  auto b = __builtin_amdgcn_permlane32_swap(__float_as_uint(v), __float_as_uint(v), false, false); return __uint_as_float(b[0]) + __uint_as_float(b[1]);
}
DI float xmax4(float v) {
  auto a = __builtin_amdgcn_permlane16_swap(__float_as_uint(v), __float_as_uint(v), false, false); v = fmaxf(__uint_as_float(a[0]), __uint_as_float(a[1]));
  auto b = __builtin_amdgcn_permlane32_swap(__float_as_uint(v), __float_as_uint(v), false, false); return fmaxf(__uint_as_float(b[0]), __uint_as_float(b[1]));
}
#define STAMP(i)

typedef unsigned long long u64;
constexpr int FL_E = 0, FL_RD = 16, FL_XN = 528, FL_KV = 784, FL_H = 1040, FL_O = 1296, FL_X = 1552;
DI GAS u64* flagp(char* ws, int i) { return (GAS u64*)(ws + WS_FLAGS + (size_t)i * 64); }
DI void signal(char* ws, int i, u64 tag) { __hip_atomic_store(flagp(ws, i), tag, __ATOMIC_RELAXED, __HIP_MEMORY_SCOPE_AGENT); }
DI u64 take_tag(char* ws, u64 eraw, int w) {
  asm volatile("" : "+v"(eraw));
  const u64 tag = (((u64)(unsigned)__builtin_amdgcn_readfirstlane((unsigned)(eraw >> 32))) << 32 | (unsigned)__builtin_amdgcn_readfirstlane((unsigned)eraw)) + 1;
  if (w == 0 && lane_id() == 0) signal(ws, FL_RD + blockIdx.x, tag);
  return tag;
}
DI void await(char* ws, int idx, u64 tag) {
  GAS u64* f = flagp(ws, idx < 0 ? FL_E : idx);
  for (unsigned spins = 0; spins < (1u << 20); ++spins) {
    const u64 v = __hip_atomic_load(f, __ATOMIC_RELAXED, __HIP_MEMORY_SCOPE_AGENT);
    if (__all(idx < 0 || v == tag)) break;
    __builtin_amdgcn_s_sleep(2);
  }
  asm volatile("" ::: "memory");
}

DI u64 phase0(int a, const u16* __restrict__ x, const u16* __restrict__ wk, const u16* __restrict__ wv, char* __restrict__ ws, LAS char* lds, const int w, const u64 eraw) {
  const int l = lane_id(), tid = w * 64 + l;
  LAS float* part = (LAS float*)(lds + 65536);
  LAS float* fin = (LAS float*)(lds + 65536 + 8192);
  LAS float* red = (LAS float*)(lds + 65536 + 8192 + 2048);
  const int rb = a & 127, hf = a >> 7;
  const int r = l & 15, c = l >> 4; const int stamp_base = 768; (void)stamp_base;
  STAMP(0);
  u32x4 xv[8];
  { const u16* xr = x + (size_t)(rb * 16 + r) * D_ + w * 256 + c * 8;
#define XL(i) asm volatile("global_load_dwordx4 %0, %1, off offset:" #i "*64" : "=&v"(xv[i]) : "v"(xr))
    XL(0); XL(1); XL(2); XL(3); XL(4); XL(5); XL(6); XL(7); }
  const unsigned lds0 = (unsigned)(uintptr_t)lds;
  {
    __amdgpu_buffer_rsrc_t rw = mkrsrc(hf ? wv : wk, 65536);
#pragma unroll
    for (int i = 0; i < 16; ++i) dma16(lds0 + w * 16384 + i * 1024, l * 16, rw, w * 16384 + i * 1024);
  }
  asm volatile("s_waitcnt vmcnt(16)" : "+v"(xv[0]), "+v"(xv[1]), "+v"(xv[2]), "+v"(xv[3]), "+v"(xv[4]), "+v"(xv[5]), "+v"(xv[6]), "+v"(xv[7]));
  STAMP(1);
  const u64 tag = take_tag(ws, eraw, w);
  float s = 0.f;
#pragma unroll
  for (int i = 0; i < 8; ++i)
#pragma unroll
    for (int j = 0; j < 4; ++j) s += lo2f(xv[i][j]) + hi2f(xv[i][j]);
  s = xsum4(s);
  if (c == 0) red[w * 16 + r] = s;
  asm volatile("s_waitcnt lgkmcnt(0)\n\ts_barrier" ::: "memory");
  const float mean = ((red[r] + red[16 + r]) + (red[32 + r] + red[48 + r])) * (1.0f / D_);
  float q = 0.f;
#pragma unroll
  for (int i = 0; i < 8; ++i)
#pragma unroll
    for (int j = 0; j < 4; ++j) { const float u = lo2f(xv[i][j]) - mean, v = hi2f(xv[i][j]) - mean; q += u * u + v * v; }
  q = xsum4(q);
  if (c == 0) red[64 + w * 16 + r] = q;
  asm volatile("s_waitcnt lgkmcnt(0)\n\ts_barrier" ::: "memory");
  const float var = ((red[64 + r] + red[80 + r]) + (red[96 + r] + red[112 + r])) * (1.0f / D_);
  const float rstd = 1.0f / sqrtf(var + 1e-5f);
#pragma unroll
  for (int i = 0; i < 8; ++i)
#pragma unroll
    for (int j = 0; j < 4; ++j) xv[i][j] = cvtpk((lo2f(xv[i][j]) - mean) * rstd, (hi2f(xv[i][j]) - mean) * rstd);
  const __amdgpu_buffer_rsrc_t rXN = mkrsrc(ws + WS_XN, 4194304);
#pragma unroll
  for (int i = 0; i < 4; ++i) st_wt16(rXN, ((8 * w + 2 * i + hf) * 128 + rb) * 1024 + l * 16, hf ? xv[2 * i + 1] : xv[2 * i]);
  if (hf == 0 && w == 0 && c == 0) __hip_atomic_store((GAS float*)(ws + WS_R2) + rb * 16 + r, 1.0f / sqrtf(var * rstd * rstd + 1e-5f), __ATOMIC_RELAXED, __HIP_MEMORY_SCOPE_AGENT);
  asm volatile("s_waitcnt vmcnt(4)" ::: "memory");
  f32x4 acc[2] = {{0.f, 0.f, 0.f, 0.f}, {0.f, 0.f, 0.f, 0.f}};
  const LAS char* wb = (const LAS char*)lds + w * 16384 + (8 * c + ((l & 15) >> 2)) * 64 + (l & 3) * 8;
#pragma unroll
  for (int i = 0; i < 8; ++i)
#pragma unroll
    for (int b = 0; b < 2; ++b) {
      bf16x8 wf = ldstr(wb + i * 2048 + b * 32, wb + i * 2048 + b * 32 + 256);
      acc[b] = MFMA16(BF(xv[i]), wf, acc[b]);
    }
#pragma unroll
  for (int b = 0; b < 2; ++b)
#pragma unroll
    for (int j = 0; j < 4; ++j) part[(w * 16 + 4 * c + j) * 32 + 16 * b + r] = acc[b][j];
  asm volatile("s_waitcnt vmcnt(0) lgkmcnt(0)\n\ts_barrier" ::: "memory");
  if (tid == 0) signal(ws, FL_XN + a, tag);
  STAMP(2);
#pragma unroll
  for (int u = 0; u < 2; ++u) { const int e = tid + 256 * u; fin[e] = (part[e] + part[512 + e]) + (part[1024 + e] + part[1536 + e]); }
  asm volatile("s_waitcnt lgkmcnt(0)\n\ts_barrier" ::: "memory");
  if (w == 0) {
    float o[8];
    unsigned dst = (rb >> 1) * 4096;
    const int half = rb & 1;
    if (hf == 0) { const int kb = l >> 5, g = (l >> 3) & 3, rr = l & 7;
      const int row = 8 * half + rr, key = 8 * (row >> 2) + 4 * kb + (row & 3) - 16 * half;
#pragma unroll
      for (int j = 0; j < 8; ++j) o[j] = fin[key * 32 + 16 * (j >> 2) + 4 * g + (j & 3)];
      dst += kb * 1024 + (g * 16 + row) * 16;
    } else { const int db = l >> 5, gl = (l >> 4) & 1, rr = l & 15;
#pragma unroll
      for (int j = 0; j < 8; ++j) o[j] = fin[(8 * gl + j) * 32 + 16 * db + rr];
      dst += 2048 + db * 1024 + ((2 * half + gl) * 16 + rr) * 16;
    }
    st_wt16(mkrsrc(ws + WS_KV, 262144), dst, (u32x4){cvtpk(o[0], o[1]), cvtpk(o[2], o[3]), cvtpk(o[4], o[5]), cvtpk(o[6], o[7])});
    asm volatile("s_waitcnt vmcnt(0)" ::: "memory");
    if (l == 0) signal(ws, FL_KV + a, tag);
  }
  STAMP(3);
  return tag;
}

#define MERGED 1
#define PRIO_A 1
#define G1PAT 0
#define G2PAT 0
#define PFSLEEP 80
#define GDELAY 0
#define PFROWS 256
#define PRIO_Q 1
#define PRIO_G1 0
#define PRIO_GE 2
#define PRIO_G2 0
#define LSUM_MFMA 1
DI float fadd_s(float a, float b) { float r; asm("s_nop 0\n\tv_add_f32_e32 %0, %1, %2" : "=v"(r) : "v"(a), "v"(b)); return r; }

DI void attn_slow(const char* __restrict__ kv, bf16x8 qf, f32x4& o0, f32x4& o1) {
  const int l = lane_id();
  const __amdgpu_buffer_rsrc_t rk = mkrsrc(kv, 262144);
  o0 = (f32x4){0.f, 0.f, 0.f, 0.f}; o1 = o0;
  float m = -3.0e38f, ls = 0.f;
#pragma unroll 1
  for (int t = 0; t < 64; ++t) {
    const int p = t * 4096 + l * 16;
    const bf16x8 k0 = BF(__builtin_amdgcn_raw_buffer_load_b128(rk, p, 0, 16)), k1 = BF(__builtin_amdgcn_raw_buffer_load_b128(rk, p + 1024, 0, 16)),
                 v0 = BF(__builtin_amdgcn_raw_buffer_load_b128(rk, p + 2048, 0, 16)), v1 = BF(__builtin_amdgcn_raw_buffer_load_b128(rk, p + 3072, 0, 16));
    const f32x4 z = {0.f, 0.f, 0.f, 0.f};
    f32x4 s0 = MFMA16(k0, qf, z), s1 = MFMA16(k1, qf, z);
    float mx = fmaxf(fmaxf(fmaxf(s0[0], s0[1]), fmaxf(s0[2], s0[3])), fmaxf(fmaxf(s1[0], s1[1]), fmaxf(s1[2], s1[3])));
    mx = fmaxf(mx, __shfl_xor(mx, 16)); mx = fmaxf(mx, __shfl_xor(mx, 32));
    const float mn = fmaxf(m, mx), al = __builtin_amdgcn_exp2f(m - mn);
    m = mn; float rs = 0.f;
#pragma unroll
    for (int i = 0; i < 4; ++i) { s0[i] = __builtin_amdgcn_exp2f(s0[i] - mn); s1[i] = __builtin_amdgcn_exp2f(s1[i] - mn); rs += s0[i] + s1[i]; o0[i] *= al; o1[i] *= al; }
    rs += __shfl_xor(rs, 16); rs += __shfl_xor(rs, 32); ls = ls * al + rs;
    const u32x4 pp = {cvtpk(s0[0], s0[1]), cvtpk(s0[2], s0[3]), cvtpk(s1[0], s1[1]), cvtpk(s1[2], s1[3])};
    o0 = MFMA16(v0, BF(pp), o0); o1 = MFMA16(v1, BF(pp), o1);
  }
#pragma unroll
  for (int i = 0; i < 4; ++i) { o0[i] /= ls; o1[i] /= ls; }
}

DI void role_attn(int a, const u16* __restrict__ wq, const u16* __restrict__ w2, const u16* __restrict__ wo, char* __restrict__ ws, LAS char* lds, const int w, const u64 tag) {
  const int l = lane_id();
#define AMAP 2
  constexpr int NTB = 32 / AMAP;
  const int xcd = a & 7, idx = a >> 3, tb = (xcd / (8 / AMAP)) * NTB + (idx % NTB), hg = (xcd % (8 / AMAP)) * AMAP + idx / NTB;
  const int g = l >> 4; const int stamp_base = 0; (void)stamp_base;
  const unsigned lds0 = (unsigned)(uintptr_t)lds;
  constexpr int SLOT = 24576;
  const __amdgpu_buffer_rsrc_t rX = mkrsrc(ws + WS_XN, 4194304), rKV = mkrsrc(ws + WS_KV, 262144);
  const unsigned vlin = l * 16;
  constexpr int QS = 12288;
  const __amdgpu_buffer_rsrc_t rWq = mkrsrc((const char*)wq - 1024, D_ * D_ * 2 + 1024);
  const unsigned vq0 = wdma_off(l, w, 2048) + hg * 256 + w * 8 * 2048 + 1024, vq1 = vq0 + 128 - 1024;
  unsigned xso = (4 * tb + w) * 1024 - 131072, qso = 0u - 65536;
  const unsigned mx = lds0 + w * 1024, mq = lds0 + 4096 + w * 2048;
#define PCQ(SL, i) do { if constexpr ((i) == 0) dma1a_c<(SL) * QS, 131072>(mx, vlin, rX, xso); else if constexpr ((i) == 1) dma1a<(SL) * QS, 65536>(mq, vq0, rWq, qso); else dma1b<1024>(vq1, rWq, qso); } while (0)
#define STGQ(SL) do { PCQ(SL, 0); PCQ(SL, 1); PCQ(SL, 2); } while (0)
  if (w == 0) await(ws, l < 8 ? FL_XN + 4 * tb + (l & 3) + 128 * (l >> 2) : -1, tag);
  asm volatile("s_waitcnt lgkmcnt(0)\n\ts_barrier" ::: "memory");
  STAMP(0);
  constexpr int NPL = 64 / (4 * NTB);
  const int pl = ((idx % NTB) * 4 + w) * NPL + l;
  const unsigned vpq = l < NPL ? (pl >> 1) * 2048 + hg * 256 + (pl & 1) * 128 + 1024 : 0x80000000u;
  const unsigned mpq = lds0 + 6 * QS;
  STGQ(0); STGQ(1); STGQ(2); STGQ(3); STGQ(4); STGQ(5);
  dma4_ahead<1 * 65536>(mpq, vpq, rWq, qso); dma4_ahead<2 * 65536>(mpq, vpq, rWq, qso); dma4_ahead<3 * 65536>(mpq, vpq, rWq, qso);
  dma4_ahead<4 * 65536>(mpq, vpq, rWq, qso); dma4_ahead<5 * 65536>(mpq, vpq, rWq, qso); dma4_ahead<6 * 65536>(mpq, vpq, rWq, qso);
  f32x4 qacc[2][4];
#pragma unroll
  for (int i = 0; i < 2; ++i)
#pragma unroll
    for (int j = 0; j < 4; ++j) qacc[i][j] = (f32x4){0.f, 0.f, 0.f, 0.f};
  const LAS char* xrd = lds + l * 16;
  const LAS char* wrd0 = lds + 4096 + (g * 2 + (w >> 1)) * 1024 + wrd_off(l, 2 * (w & 1));
  const LAS char* wrd1 = lds + 4096 + (g * 2 + (w >> 1)) * 1024 + wrd_off(l, 2 * (w & 1) + 1);
  bf16x8 qxa[4], qwa[2], qxb[4], qwb[2];
#define QRX(X, m, SL) X[m] = ldsfrag(xrd + (SL) * QS + (m) * 1024)
#define QRW(W, n, SL) W[n] = ((n) == 0 ? ldstr(wrd0 + (SL) * QS, wrd0 + (SL) * QS + 512) : ldstr(wrd1 + (SL) * QS, wrd1 + (SL) * QS + 512))
#define QM(X, W, m, n) qacc[n][m] = MFMA16(W[n], X[m], qacc[n][m])
#define STEPQ(X, W, XN, WN, RD, RS, DMA, DS) do { \
    QM(X, W, 0, 0); if (RD) { QRW(WN, 0, RS); QRX(XN, 0, RS); } SB(); QM(X, W, 1, 0); if (RD) { QRW(WN, 1, RS); QRX(XN, 1, RS); } SB(); QM(X, W, 2, 0); if (RD) QRX(XN, 2, RS); SB(); QM(X, W, 3, 0); if (RD) QRX(XN, 3, RS); SB(); \
    QM(X, W, 0, 1); if (DMA) PCQ(DS, 0); SB(); QM(X, W, 1, 1); if (DMA) PCQ(DS, 1); SB(); QM(X, W, 2, 1); if (DMA) PCQ(DS, 2); SB(); QM(X, W, 3, 1); if (DMA) dma4_ahead<6 * 65536>(mpq, vpq, rWq, qso); SB(); } while (0)
  WAIT_BAR(21);
#pragma unroll
  for (int m = 0; m < 4; ++m) QRX(qxa, m, 0);
  QRW(qwa, 0, 0); QRW(qwa, 1, 0);
#define QEV(SC, SN, W_, RD, DMA) do { WAIT_BAR(W_); SB(); STEPQ(qxa, qwa, qxb, qwb, RD, SN, DMA, SC); } while (0)
#define QOD(SC, SN, W_, RD, DMA) do { WAIT_BAR(W_); SB(); STEPQ(qxb, qwb, qxa, qwa, RD, SN, DMA, SC); } while (0)
  QEV(0, 1, 18, true, true); QOD(1, 2, 19, true, true); QEV(2, 3, 20, true, true); QOD(3, 4, 21, true, true); QEV(4, 5, 22, true, true); QOD(5, 0, 17, true, true);
  for (int it = 0; it < 3; ++it) { QEV(0, 1, 17, true, true); QOD(1, 2, 17, true, true); QEV(2, 3, 17, true, true); QOD(3, 4, 17, true, true); QEV(4, 5, 17, true, true); QOD(5, 0, 17, true, true); }
  QEV(0, 1, 17, true, true); QOD(1, 2, 17, true, true);
  QEV(2, 3, 17, true, false); QOD(3, 4, 13, true, false); QEV(4, 5, 9, true, false); QOD(5, 0, 5, true, false);
  if (w == 0) { await(ws, FL_KV + l, tag); await(ws, FL_KV + 64 + l, tag); await(ws, FL_KV + 128 + l, tag); await(ws, FL_KV + 192 + l, tag); }
  QEV(0, 1, 0, true, false);
  unsigned kvso = w * 4096 - 16384;
  const unsigned kvm = lds0 + w * 4096;
#define STGKV(SF) do { dma1a_c<(SF) * SLOT, 16384>(kvm, vlin, rKV, kvso); dma1b_c<1024>(vlin, rKV, kvso); dma1b_c<2048>(vlin, rKV, kvso); dma1b_c<3072>(vlin, rKV, kvso); } while (0)
  STGKV(1); STGKV(2);
  {
    const unsigned line = (a * 4 + w) * 64 + l;
    dma4(lds0 + 6 * QS, line * 128, mkrsrc(w2, F_ * D_ * 2), 0);
    dma4(lds0 + 6 * QS, line * 128, mkrsrc(wo, D_ * D_ * 2), 0);
  }
  STEPQ(qxb, qwb, qxa, qwa, false, 0, false, 0);
  STAMP(1);
  __builtin_amdgcn_s_setprio(PRIO_A);
  bf16x8 qf[4];
#pragma unroll
  for (int m = 0; m < 4; ++m)
    qf[m] = BF(((u32x4){cvtpk(qacc[0][m][0] * QSCALE, qacc[0][m][1] * QSCALE), cvtpk(qacc[0][m][2] * QSCALE, qacc[0][m][3] * QSCALE),
                        cvtpk(qacc[1][m][0] * QSCALE, qacc[1][m][1] * QSCALE), cvtpk(qacc[1][m][2] * QSCALE, qacc[1][m][3] * QSCALE)}));
  WAIT_BAR(6);
  const LAS char* kvb = lds + l * 16;
  f32x4 negm[4], S[4][2], O[4][2];
  bf16x8 k0, k1;
  {
    const LAS char* p = kvb + 1 * SLOT;
    k0 = ldsfrag(p); k1 = ldsfrag(p + 1024);
    const f32x4 z = {0.f, 0.f, 0.f, 0.f};
#pragma unroll
    for (int m = 0; m < 4; ++m) {
      const f32x4 s0 = MFMA16(k0, qf[m], z), s1 = MFMA16(k1, qf[m], z);
      float mx = fmaxf(fmaxf(fmaxf(s0[0], s0[1]), fmaxf(s0[2], s0[3])), fmaxf(fmaxf(s1[0], s1[1]), fmaxf(s1[2], s1[3])));
      mx = xmax4(mx);
      negm[m] = (f32x4){-mx, -mx, -mx, -mx}; PIN(negm[m]);
      O[m][0] = z; O[m][1] = z;
    }
    S[0][0] = MFMA16(k0, qf[0], negm[0]); S[0][1] = MFMA16(k1, qf[0], negm[0]);
    S[1][0] = MFMA16(k0, qf[1], negm[1]); S[1][1] = MFMA16(k1, qf[1], negm[1]);
  }
  u32x4 P[4];
  P[2] = (u32x4){0, 0, 0, 0}; P[3] = P[2];
  bf16x8 va0 = BF(P[2]), va1 = va0, vb0, vb1;
  S[3][1] = (f32x4){0.f, 0.f, 0.f, 0.f};
  f32x4 L[4];
#pragma unroll
  for (int m = 0; m < 4; ++m) L[m] = (f32x4){0.f, 0.f, 0.f, 0.f};
  bf16x8 ones; { const short o1 = (l & 15) == 0 ? (short)0x3f80 : (short)0; s16x8 ov = {o1, o1, o1, o1, o1, o1, o1, o1}; ones = __builtin_bit_cast(bf16x8, ov); PIN(ones); }
#define LS(m) do { L[m] = MFMA16(ones, BF(P[m]), L[m]); PIN(L[m]); SB(); } while (0)
#define LADD2(m, kb, i)
#define NOLS(m)
#define EXPF(x) __builtin_amdgcn_exp2f(x)
#define EX2(m, kb, i) do { float t0_ = EXPF(S[m][kb][i]), t1_ = EXPF(S[m][kb][i + 1]); PIN(t0_); PIN(t1_); S[m][kb][i] = t0_; S[m][kb][i + 1] = t1_; SB(); } while (0)
#define CVA(m, kb) do { unsigned c_ = cvtpk(S[m][kb][0], S[m][kb][1]); PIN(c_); P[m][2 * kb] = c_; LADD2(m, kb, 0); SB(); } while (0)
#define CVB(m, kb) do { unsigned c_ = cvtpk(S[m][kb][2], S[m][kb][3]); PIN(c_); P[m][2 * kb + 1] = c_; LADD2(m, kb, 2); SB(); } while (0)
#define GRP(mq, kq, KF, pm, pk, me, ke, mp, db, VF, LSM, X) do { \
    S[mq][kq] = MFMA16(KF, qf[mq], negm[mq]); PIN(S[mq][kq]); SB(); X; CVB(pm, pk); EX2(me, ke, 0); \
    O[mp][db] = MFMA16(VF, BF(P[mp]), O[mp][db]); PIN(O[mp][db]); SB(); EX2(me, ke, 2); LSM(mp); CVA(me, ke); } while (0)
#define TILE(KC0, KC1, KN0, KN1, VP0, VP1, VC0, VC1, kn, vp, D) do { \
    GRP(2, 0, KC0, 3, 1, 0, 0, 2, 0, VP0, NOLS, KN0 = ldsfrag(kn); SB()); \
    GRP(2, 1, KC1, 0, 0, 0, 1, 2, 1, VP1, LS,   KN1 = ldsfrag((kn) + 1024); SB()); \
    GRP(3, 0, KC0, 0, 1, 1, 0, 3, 0, VP0, NOLS, VC0 = ldsfrag(vp); SB()); \
    GRP(3, 1, KC1, 1, 0, 1, 1, 3, 1, VP1, LS,   VC1 = ldsfrag((vp) + 1024); SB()); \
    GRP(0, 0, KN0, 1, 1, 2, 0, 0, 0, VC0, NOLS, D(0)); \
    GRP(0, 1, KN1, 2, 0, 2, 1, 0, 1, VC1, LS,   D(1)); \
    GRP(1, 0, KN0, 2, 1, 3, 0, 1, 0, VC0, NOLS, D(2)); \
    GRP(1, 1, KN1, 3, 0, 3, 1, 1, 1, VC1, LS,   D(3)); \
  } while (0)
#define NOD(i)
  bf16x8 kx0, kx1;
#define STAGE(SC, SN, SF, DM, LAST) do { \
    TILE(k0, k1, kx0, kx1, va0, va1, vb0, vb1, kvb + (SC) * SLOT + 4096, kvb + (SC) * SLOT + 2048, NOD); \
    TILE(kx0, kx1, k0, k1, vb0, vb1, va0, va1, kvb + (SC) * SLOT + 8192, kvb + (SC) * SLOT + 4096 + 2048, NOD); \
    TILE(k0, k1, kx0, kx1, va0, va1, vb0, vb1, kvb + (SC) * SLOT + 12288, kvb + (SC) * SLOT + 8192 + 2048, NOD); \
    WAIT_BAR(0);        \
    TILE(kx0, kx1, k0, k1, vb0, vb1, va0, va1, kvb + ((LAST) ? (SC) : (SN)) * SLOT, kvb + (SC) * SLOT + 12288 + 2048, DM); } while (0)
#define DM_(SF, i) do { if constexpr ((i) == 0) { dma1a_c<(SF) * SLOT, 16384>(kvm, vlin, rKV, kvso); } else { dma1b_c<(i) * 1024>(vlin, rKV, kvso); } SB(); } while (0)
#define DM0(i) DM_(0, i)
#define DM1(i) DM_(1, i)
#define DM2(i) DM_(2, i)
  for (int j = 0; j < 12; j += 3) { STAGE(1, 2, 0, DM0, false); STAGE(2, 0, 1, DM1, false); STAGE(0, 1, 2, DM2, false); STAMP(4 + j / 3); }
  STAGE(1, 2, 0, DM0, false); STAGE(2, 0, 1, DM1, false); STAGE(0, 1, 2, NOD, false); STAGE(1, 2, 0, NOD, true);
  CVB(3, 1);
  O[2][0] = MFMA16(va0, BF(P[2]), O[2][0]); O[2][1] = MFMA16(va1, BF(P[2]), O[2][1]); LS(2);
  O[3][0] = MFMA16(va0, BF(P[3]), O[3][0]); O[3][1] = MFMA16(va1, BF(P[3]), O[3][1]); LS(3);
  STAMP(2);
  const __amdgpu_buffer_rsrc_t rO = mkrsrc(ws + WS_O, 4194304);
#pragma unroll
  for (int m = 0; m < 4; ++m) {
    float ls = __shfl(L[m][0], l & 15);
    float inv = 1.0f / ls;
    f32x4 o0 = O[m][0], o1 = O[m][1];
    const float mo = fmaxf(fmaxf(fmaxf(fabsf(o0[0]), fabsf(o0[1])), fmaxf(fabsf(o0[2]), fabsf(o0[3]))), fmaxf(fmaxf(fabsf(o1[0]), fabsf(o1[1])), fmaxf(fabsf(o1[2]), fabsf(o1[3]))));
    const bool ok = (ls <= 1.0e18f) && (mo <= 1.0e37f);
    if (__builtin_expect(__any(!ok), 0)) { attn_slow(ws + WS_KV, qf[m], o0, o1); inv = 1.0f; }
    const u32x4 ch = chunk_of(cvtpk(o0[0] * inv, o0[1] * inv), cvtpk(o0[2] * inv, o0[3] * inv), cvtpk(o1[0] * inv, o1[1] * inv), cvtpk(o1[2] * inv, o1[3] * inv));
    const int c = ((g & 1) << 1) | (g >> 1);
    st_wt16(rO, ((4 * hg + w) * 128 + 4 * tb + m) * 1024 + c * 256 + (l & 15) * 16, ch);
  }
  asm volatile("s_waitcnt vmcnt(0)\n\ts_barrier" ::: "memory");
  if (w == 0 && lane_id() == 0) signal(ws, FL_O + tb * 8 + hg, tag);
  STAMP(3);
}

DI int w2_kh(int gi) { return gi & 1; }
DI void w2_weights_early(int gi, const u16* __restrict__ w2, LAS char* lds, const int w) {
  const int l = lane_id(), nt = gi >> 5;
  const __amdgpu_buffer_rsrc_t rW = mkrsrc((const char*)w2 - 1024, F_ * D_ * 2 + 1024);
  const unsigned vw0 = wdma_off(l, w, 2048) + nt * 256 + w * 8 * 2048 + 1024, vw1 = vw0 + 128 - 1024;
  unsigned wso = 56 * w2_kh(gi) * 65536 - 65536;
  const unsigned mw = (unsigned)(uintptr_t)lds + 8192 + w * 2048;
  dma1a<0 * 16384, 65536>(mw, vw0, rW, wso); dma1b<1024>(vw1, rW, wso); dma1a<1 * 16384, 65536>(mw, vw0, rW, wso); dma1b<1024>(vw1, rW, wso);
  dma1a<2 * 16384, 65536>(mw, vw0, rW, wso); dma1b<1024>(vw1, rW, wso); dma1a<3 * 16384, 65536>(mw, vw0, rW, wso); dma1b<1024>(vw1, rW, wso);
}

DI f32x4 gelu4(f32x4 acc, float rs, f32x4 b, float c1v) {
  constexpr float c2 = -2.0f * 0.7978845608028654f * 1.4426950408889634f * 0.044715f;
  float x0 = acc[0], x1 = acc[1], x2 = acc[2], x3 = acc[3], t0, t1, t2, t3;
  asm("v_fma_f32 %0, %0, %8, %9\n\tv_fma_f32 %1, %1, %8, %10\n\tv_fma_f32 %2, %2, %8, %11\n\tv_fma_f32 %3, %3, %8, %12\n\t"
      "v_mul_f32 %4, %0, %0\n\tv_mul_f32 %5, %1, %1\n\tv_mul_f32 %6, %2, %2\n\tv_mul_f32 %7, %3, %3\n\t"
      "v_fma_f32 %4, %4, %14, %13\n\tv_fma_f32 %5, %5, %14, %13\n\tv_fma_f32 %6, %6, %14, %13\n\tv_fma_f32 %7, %7, %14, %13\n\t"
      "v_mul_f32 %4, %0, %4\n\tv_mul_f32 %5, %1, %5\n\tv_mul_f32 %6, %2, %6\n\tv_mul_f32 %7, %3, %7\n\t"
      "v_exp_f32 %4, %4\n\tv_exp_f32 %5, %5\n\tv_exp_f32 %6, %6\n\tv_exp_f32 %7, %7\n\t"
      "v_add_f32 %4, 1.0, %4\n\tv_add_f32 %5, 1.0, %5\n\tv_add_f32 %6, 1.0, %6\n\tv_add_f32 %7, 1.0, %7\n\t"
      "v_rcp_f32 %4, %4\n\tv_rcp_f32 %5, %5\n\tv_rcp_f32 %6, %6\n\tv_rcp_f32 %7, %7\n\t"
      "v_mul_f32 %0, %0, %4\n\tv_mul_f32 %1, %1, %5\n\tv_mul_f32 %2, %2, %6\n\tv_mul_f32 %3, %3, %7"
      : "+v"(x0), "+v"(x1), "+v"(x2), "+v"(x3), "=&v"(t0), "=&v"(t1), "=&v"(t2), "=&v"(t3)
      : "v"(rs), "v"(b[0]), "v"(b[1]), "v"(b[2]), "v"(b[3]), "v"(c1v), "s"(c2));
  return (f32x4){x0, x1, x2, x3};
}

DI u64 role_w1(int gi, const u16* __restrict__ wq, const u16* __restrict__ w1, const u16* __restrict__ b1, const u16* __restrict__ w2, char* __restrict__ ws, LAS char* lds, const int w, u64 eraw) {
  const int l = lane_id();
  const int xcd = gi & 7, idx = gi >> 3, p = (xcd >> 2) * 8 + (idx & 7), j = (xcd & 3) * 4 + (idx >> 3);
  const int g = l >> 4, wm = w >> 1, wn = w & 1; const int stamp_base = 256; (void)stamp_base;
  const unsigned lds0 = (unsigned)(uintptr_t)lds;
  constexpr int SLOT = 24576;
  const __amdgpu_buffer_rsrc_t rX = mkrsrc(ws + WS_XN, 4194304), rW = mkrsrc((const char*)w1 - 4096, D_ * F_ * 2 + 4096);
  const unsigned vlin = l * 16;
  unsigned vw[4];
#pragma unroll
  for (int i = 0; i < 4; ++i) vw[i] = wdma_off(l, w, F_ * 2) + 8 * w * (F_ * 2) + 448 * j - 64 * (j & 1) + 128 * i + 4096 - 1024 * i;
  STAMP(0);
  unsigned xso = (8 * p + 2 * w) * 1024 - 131072, wso = 0u - 32 * F_ * 2;
  const unsigned mx = lds0 + w * 2048, mw = lds0 + 8192 + w * 4096;
#define PC1(SL, i) do { if constexpr ((i) == 0) dma1a_c<(SL) * SLOT, 131072>(mx, vlin, rX, xso); else if constexpr ((i) == 1) dma1b_c<1024>(vlin, rX, xso); \
    else if constexpr ((i) == 2) dma1a<(SL) * SLOT, 32 * F_ * 2>(mw, vw[0], rW, wso); else dma1b<((i) - 2) * 1024>(vw[(i) - 2], rW, wso); } while (0)
#define STG1(SL) do { PC1(SL, 0); PC1(SL, 1); PC1(SL, 2); PC1(SL, 3); PC1(SL, 4); PC1(SL, 5); } while (0)
  PC1(0, 2); PC1(0, 3); PC1(0, 4); PC1(0, 5); PC1(1, 2); PC1(1, 3); PC1(1, 4); PC1(1, 5); PC1(2, 2); PC1(2, 3); PC1(2, 4); PC1(2, 5);
  constexpr int L_R2 = 3 * SLOT, L_B1 = L_R2 + 512;
  const unsigned vpf = l < 4 ? (4 * (idx & 7) + w) * (F_ * 2) + 448 * j - 64 * (j & 1) + l * 128 + 4096 : 0x80000000u;
  const unsigned mpf = lds0 + L_B1 + 1024;
  dma4_ahead<1 * 32 * F_ * 2>(mpf, vpf, rW, wso); dma4_ahead<2 * 32 * F_ * 2>(mpf, vpf, rW, wso); dma4_ahead<3 * 32 * F_ * 2>(mpf, vpf, rW, wso);
  dma4_ahead<4 * 32 * F_ * 2>(mpf, vpf, rW, wso); dma4_ahead<5 * 32 * F_ * 2>(mpf, vpf, rW, wso);
  if (w == 1) dma4(mpf, (gi * 64 + l) * 128, mkrsrc(wq, D_ * D_ * 2), 0);
  if (w >= 2 && PFROWS > 0) { constexpr int NL = PFROWS * 56 / 512;
#pragma unroll
    for (int i = 0; i < (NL + 63) / 64; ++i) dma4(mpf, i * 64 + l < NL ? 256 * F_ * 2 + ((gi * 2 + (w - 2)) * NL + i * 64 + l) * 128 : 0x80000000u, mkrsrc(w1, D_ * F_ * 2), 0);
  }
  if (w >= 2) dma4(lds0 + L_B1 + (w - 2) * 256, 448 * j + ((w - 2) * 64 + l) * 4, mkrsrc(b1, F_ * 2), 0);
  asm volatile("s_waitcnt vmcnt(17)" : "+v"(eraw));
  const u64 tag = take_tag(ws, eraw, w);
  if (w == 0) await(ws, l < 16 ? FL_XN + 8 * p + (l & 7) + 128 * (l >> 3) : -1, tag);
  asm volatile("s_barrier" ::: "memory");
  STAMP(8);
  if (w < 2) dma4_c(lds0 + L_R2 + w * 256, (128 * p + 64 * w + l) * 4, mkrsrc(ws + WS_R2, 8192), 0);
  PC1(0, 0); PC1(0, 1); PC1(1, 0); PC1(1, 1); PC1(2, 0); PC1(2, 1);
  dma4(mpf, 0x80000000u, rW, 0);
  f32x4 acc[4][7];
#pragma unroll
  for (int m = 0; m < 4; ++m)
#pragma unroll
    for (int n = 0; n < 7; ++n) acc[m][n] = (f32x4){0.f, 0.f, 0.f, 0.f};
  const LAS char* xrd = lds + 4 * wm * 1024 + l * 16;
  const LAS char* wrb[5];
  { const int off = 6 * wn + 2 * (j & 1), nl = 12 + wn + 2 * (j & 1);
#pragma unroll
    for (int i = 0; i < 4; ++i) wrb[i] = lds + 8192 + (g * 4 + ((i + off) >> 2)) * 1024 + wrd_off(l, (i + off) & 3);
    wrb[4] = lds + 8192 + (g * 4 + (nl >> 2)) * 1024 + wrd_off(l, nl & 3); }
  bf16x8 xa[4], wa[7], xb[4], wb[7];
#define RDX(X, m, so) X[m] = ldsfrag(xrd + (so) + (m) * 1024)
#define RDW(W, n, so) W[n] = ((n) < 6 ? ldstr(wrb[(n) & 3] + (so) + ((n) >> 2) * 1024, wrb[(n) & 3] + (so) + ((n) >> 2) * 1024 + 512) : ldstr(wrb[4] + (so), wrb[4] + (so) + 512))
#define M1(X, W, m, n) acc[m][n] = MFMA16(W[n], X[m], acc[m][n])
#define STEP1(X, W, XN, WN, RD, RS, DMA, DS) do { \
    M1(X, W, 0, 0); if (RD) RDW(WN, 0, (RS) * SLOT); SB(); M1(X, W, 1, 0); if (RD) RDX(XN, 0, (RS) * SLOT); SB(); M1(X, W, 2, 0); if (RD) RDW(WN, 1, (RS) * SLOT); SB(); M1(X, W, 3, 0); if (DMA) PC1(DS, 0); SB(); \
    M1(X, W, 0, 1); if (RD) RDX(XN, 1, (RS) * SLOT); SB(); M1(X, W, 1, 1); if (RD) RDW(WN, 2, (RS) * SLOT); SB(); M1(X, W, 2, 1); if (RD) RDX(XN, 2, (RS) * SLOT); SB(); M1(X, W, 3, 1); if (DMA) PC1(DS, 1); SB(); \
    M1(X, W, 0, 2); if (RD) RDW(WN, 3, (RS) * SLOT); SB(); M1(X, W, 1, 2); if (RD) RDX(XN, 3, (RS) * SLOT); SB(); M1(X, W, 2, 2); if (RD) RDW(WN, 4, (RS) * SLOT); SB(); M1(X, W, 3, 2); if (DMA) PC1(DS, 2); SB(); \
    M1(X, W, 0, 3); if (RD) RDW(WN, 5, (RS) * SLOT); SB(); M1(X, W, 1, 3); SB(); M1(X, W, 2, 3); if (RD) RDW(WN, 6, (RS) * SLOT); SB(); M1(X, W, 3, 3); if (DMA) PC1(DS, 3); SB(); \
    M1(X, W, 0, 4); SB(); M1(X, W, 1, 4); SB(); M1(X, W, 2, 4); SB(); M1(X, W, 3, 4); if (DMA) PC1(DS, 4); SB(); \
    M1(X, W, 0, 5); SB(); M1(X, W, 1, 5); SB(); M1(X, W, 2, 5); SB(); M1(X, W, 3, 5); if (DMA) { PC1(DS, 5); dma4_ahead<5 * 32 * F_ * 2>(mpf, vpf, rW, wso); } SB(); \
    M1(X, W, 0, 6); SB(); M1(X, W, 1, 6); SB(); M1(X, W, 2, 6); SB(); M1(X, W, 3, 6); SB(); } while (0)
  WAIT_BAR(3);
#pragma unroll
  for (int m = 0; m < 4; ++m) RDX(xa, m, 0);
#pragma unroll
  for (int n = 0; n < 7; ++n) RDW(wa, n, 0);
#define EV(SC, SN, W_, RD, DMA) do { WAIT_BAR(W_); SB(); STEP1(xa, wa, xb, wb, RD, SN, DMA, SC); } while (0)
#define OD(SC, SN, W_, RD, DMA) do { WAIT_BAR(W_); SB(); STEP1(xb, wb, xa, wa, RD, SN, DMA, SC); } while (0)
#define PR1(i) do { if constexpr ((i) == 0 || (((G1PAT >> (i)) & 1) != ((G1PAT >> ((i) - 1 < 0 ? 0 : (i) - 1)) & 1))) __builtin_amdgcn_s_setprio(((G1PAT >> (i)) & 1) ? 2 : 0); } while (0)
#define SIX() do { PR1(0); EV(0, 1, 8, true, true); PR1(1); OD(1, 2, 8, true, true); PR1(2); EV(2, 0, 8, true, true); PR1(3); OD(0, 1, 8, true, true); PR1(4); EV(1, 2, 8, true, true); PR1(5); OD(2, 0, 8, true, true); } while (0)
  for (int it = 0; it < 4; ++it) { SIX(); STAMP(4 + it); }
  EV(0, 1, 8, true, true); OD(1, 2, 8, true, true); EV(2, 0, 8, true, true); OD(0, 1, 8, true, true); EV(1, 2, 8, true, true);
  OD(2, 0, 8, true, false); EV(0, 1, 0, true, false); OD(1, 2, 0, false, false);
  STAMP(1);
  __builtin_amdgcn_s_setprio(PRIO_GE);
  w2_weights_early(gi, w2, lds, w);
  const int le = lane_id(), ge = le >> 4;
  float r2[4];
#pragma unroll
  for (int m = 0; m < 4; ++m) r2[m] = *(const LAS float*)(lds + L_R2 + (64 * wm + 16 * m + (le & 15)) * 4);
  const __amdgpu_buffer_rsrc_t rH = mkrsrc(ws + WS_H, 112 * 128 * 1024);
  const int cch = ((ge & 1) << 1) | (ge >> 1);
  unsigned last[4][2];
  float c1v = -2.0f * 0.7978845608028654f * 1.4426950408889634f; PIN(c1v);
#pragma unroll
  for (int n = 0; n < 7; ++n) {
    const int nb = n < 6 ? 6 * wn + n : 12 + wn;
    const u32x2 braw = *(const LAS u32x2*)(lds + L_B1 + (16 * nb + 4 * ge) * 2);
    const f32x4 bb = {lo2f(braw[0]), hi2f(braw[0]), lo2f(braw[1]), hi2f(braw[1])};
#pragma unroll
    for (int m = 0; m < 4; ++m) acc[m][n] = gelu4(acc[m][n], r2[m], bb, c1v);
    if (n < 6) { if (n & 1) {
#pragma unroll
        for (int m = 0; m < 4; ++m) {
          const u32x4 ch = chunk_of(cvtpk(acc[m][n - 1][0], acc[m][n - 1][1]), cvtpk(acc[m][n - 1][2], acc[m][n - 1][3]), cvtpk(acc[m][n][0], acc[m][n][1]), cvtpk(acc[m][n][2], acc[m][n][3]));
          st_wt16(rH, ((7 * j + 3 * wn + (n >> 1)) * 128 + 8 * p + 4 * wm + m) * 1024 + cch * 256 + (le & 15) * 16, ch);
        } }
    } else {
#pragma unroll
      for (int m = 0; m < 4; ++m) { last[m][0] = cvtpk(acc[m][n][0], acc[m][n][1]); last[m][1] = cvtpk(acc[m][n][2], acc[m][n][3]); }
    }
  }
#pragma unroll
  for (int m = 0; m < 4; m += 2) {
    const u32x4 ch = chunk_of(last[m][0], last[m][1], last[m + 1][0], last[m + 1][1]);
    st_wt16(rH, ((7 * j + 6) * 128 + 8 * p + 4 * wm + m + (ge & 1)) * 1024 + (2 * wn + (ge >> 1)) * 256 + (le & 15) * 16, ch);
  }
  STAMP(2);
  __builtin_amdgcn_s_setprio(PRIO_G2);
  asm volatile("s_waitcnt vmcnt(0)\n\ts_barrier" ::: "memory");
  if (w == 0 && lane_id() == 0) signal(ws, FL_H + p * 16 + j, tag);
  STAMP(3);
#undef RDX
#undef RDW
#undef EV
#undef OD
  return tag;
}

DI void role_w2(int gi, const u16* __restrict__ w2, const u16* __restrict__ wo, const u16* __restrict__ b2, const u16* __restrict__ x, u16* __restrict__ out, char* __restrict__ ws, LAS char* lds, const int w, const u64 tag) {
  const int l = lane_id();
  const int xcd = gi & 7, idx = gi >> 3, kh = xcd & 1, p = (xcd >> 1) * 4 + (idx & 3), nt = idx >> 2;
  const int partner = gi ^ 1;
  const int g = l >> 4, wm = w >> 1, wn = w & 1; const int stamp_base = 512; (void)stamp_base;
  const unsigned lds0 = (unsigned)(uintptr_t)lds;
  constexpr int SLOT = 16384;
#define AINC_ 131072
#define WINC_ 65536
  const __amdgpu_buffer_rsrc_t rA = mkrsrc(ws + WS_H, 144 * 128 * 1024);
  __amdgpu_buffer_rsrc_t rW = mkrsrc((const char*)w2 - 1024, F_ * D_ * 2 + 1024);
  const unsigned vlin = l * 16;
  const unsigned vw0 = wdma_off(l, w, 2048) + nt * 256 + w * 8 * 2048 + 1024, vw1 = vw0 + 128 - 1024;
  if (w == 0) await(ws, l < 8 ? FL_H + p * 16 + 8 * kh + l : -1, tag);
  asm volatile("s_barrier" ::: "memory");
  STAMP(0);
  unsigned aso = (56 * kh * 128 + 8 * p + 2 * w) * 1024 - AINC_, wso = 56 * kh * 65536 + 3 * 65536;
  const unsigned ma = lds0 + w * 2048, mw = lds0 + 8192 + w * 2048;
#define AINC 131072
#define WINC 65536
#define PC2(SL, i) do { if constexpr ((i) == 0) dma1a_c<(SL) * SLOT, AINC>(ma, vlin, rA, aso); else if constexpr ((i) == 1) dma1b_c<1024>(vlin, rA, aso); \
    else if constexpr ((i) == 2) dma1a<(SL) * SLOT, WINC>(mw, vw0, rW, wso); else dma1b<1024>(vw1, rW, wso); } while (0)
#define STG2(SL) do { PC2(SL, 0); PC2(SL, 1); PC2(SL, 2); PC2(SL, 3); } while (0)
  PC2(0, 0); PC2(0, 1); PC2(1, 0); PC2(1, 1); PC2(2, 0); PC2(2, 1); PC2(3, 0); PC2(3, 1); STG2(4);
  f32x4 acc[4][4];
#pragma unroll
  for (int m = 0; m < 4; ++m)
#pragma unroll
    for (int n = 0; n < 4; ++n) acc[m][n] = (f32x4){0.f, 0.f, 0.f, 0.f};
  const LAS char* xrd = lds + 4 * wm * 1024 + l * 16;
  const LAS char* wrb[4];
#pragma unroll
  for (int i = 0; i < 4; ++i) wrb[i] = lds + 8192 + (g * 2 + wn) * 1024 + wrd_off(l, i);
  bf16x8 xa[4], wa[4], xb[4], wb[4];
#define RDX(X, m, so) X[m] = ldsfrag(xrd + (so) + (m) * 1024)
#define RDW(W, n, so) W[n] = ldstr(wrb[n] + (so), wrb[n] + (so) + 512)
#define M2(X, W, m, n) acc[m][n] = MFMA16(W[n], X[m], acc[m][n])
#define STEP2(X, W, XN, WN, RD, RS, DMA, DS) do { \
    M2(X, W, 0, 0); if (RD) RDW(WN, 0, (RS) * SLOT); SB(); M2(X, W, 1, 0); if (RD) RDX(XN, 0, (RS) * SLOT); SB(); M2(X, W, 2, 0); if (RD) RDX(XN, 1, (RS) * SLOT); SB(); M2(X, W, 3, 0); if (DMA) PC2(DS, 0); SB(); \
    M2(X, W, 0, 1); if (RD) RDW(WN, 1, (RS) * SLOT); SB(); M2(X, W, 1, 1); if (RD) RDX(XN, 2, (RS) * SLOT); SB(); M2(X, W, 2, 1); if (RD) RDX(XN, 3, (RS) * SLOT); SB(); M2(X, W, 3, 1); if (DMA) PC2(DS, 1); SB(); \
    M2(X, W, 0, 2); if (RD) RDW(WN, 2, (RS) * SLOT); SB(); M2(X, W, 1, 2); if (RD) RDW(WN, 3, (RS) * SLOT); SB(); M2(X, W, 2, 2); SB(); M2(X, W, 3, 2); if (DMA) PC2(DS, 2); SB(); \
    M2(X, W, 0, 3); SB(); M2(X, W, 1, 3); SB(); M2(X, W, 2, 3); SB(); M2(X, W, 3, 3); if (DMA) PC2(DS, 3); SB(); } while (0)
  WAIT_BAR(6);
#pragma unroll
  for (int i = 0; i < 4; ++i) { RDX(xa, i, 0); RDW(wa, i, 0); }
#define EV(SC, SN, W_, RD, DMA) do { WAIT_BAR(W_); SB(); STEP2(xa, wa, xb, wb, RD, SN, DMA, SC); } while (0)
#define OD(SC, SN, W_, RD, DMA) do { WAIT_BAR(W_); SB(); STEP2(xb, wb, xa, wa, RD, SN, DMA, SC); } while (0)
#define PR2(i) do { if constexpr ((i) == 0 || (((G2PAT >> (i)) & 1) != ((G2PAT >> ((i) - 1 < 0 ? 0 : (i) - 1)) & 1))) __builtin_amdgcn_s_setprio(((G2PAT >> (i)) & 1) ? 2 : 0); } while (0)
  u64 oflag = 0;
  for (int it = 0; it < 6; ++it) {
    STAMP(4 + it);
    PR2(0);
    EV(0, 1, 12, true, true);
    if (it == 5) {
      if (w == 0) {
        asm volatile("" : "+v"(oflag));
        if (!__all(l >= 8 || oflag == tag)) await(ws, l < 8 ? FL_O + (2 * p + (l >> 2)) * 8 + 4 * kh + (l & 3) : -1, tag);
      }
      rW = mkrsrc((const char*)wo - 1024, D_ * D_ * 2 + 1024);
      aso = ((112 + 16 * kh) * 128 + 8 * p + 2 * w) * 1024 - 131072; wso = 16 * kh * 65536 - 65536;
    }
    PR2(1); OD(1, 2, 12, true, true); PR2(2); EV(2, 3, 12, true, true); PR2(3); OD(3, 4, 12, true, true); PR2(4); EV(4, 0, 12, true, true);
    PR2(5); OD(0, 1, 12, true, true); PR2(6); EV(1, 2, 12, true, true);
    if (it == 4 && w == 0) {
      const GAS u64* fo = flagp(ws, FL_O + (2 * p + ((l >> 2) & 1)) * 8 + 4 * kh + (l & 3));
      asm volatile("global_load_dwordx2 %0, %1, off sc1" : "=&v"(oflag) : "v"(fo));
    }
    PR2(7); OD(2, 3, 12, true, true); PR2(8); EV(3, 4, 12, true, true); PR2(9); OD(4, 0, 12, true, true);
  }
  EV(0, 1, 12, true, true); OD(1, 2, 12, true, true); EV(2, 3, 12, true, true); OD(3, 4, 12, true, true); EV(4, 0, 12, true, true); OD(0, 1, 12, true, true); EV(1, 2, 12, true, true);
  u32x2 xres[2][4], bres[4];
  { const int le = lane_id();
    const u16* xp = x + (size_t)(128 * p + 64 * wm + 32 * kh + (le & 15)) * D_ + 128 * nt + 64 * wn + 4 * (le >> 4); const u16* xq = xp + 16 * D_; const u16* bp = b2 + 128 * nt + 64 * wn + 4 * (le >> 4);
    asm volatile("global_load_dwordx2 %0, %8, off\n\tglobal_load_dwordx2 %1, %8, off offset:32\n\tglobal_load_dwordx2 %2, %8, off offset:64\n\tglobal_load_dwordx2 %3, %8, off offset:96\n\t"
                 "global_load_dwordx2 %4, %9, off\n\tglobal_load_dwordx2 %5, %9, off offset:32\n\tglobal_load_dwordx2 %6, %9, off offset:64\n\tglobal_load_dwordx2 %7, %9, off offset:96"
                 : "=&v"(xres[0][0]), "=&v"(xres[0][1]), "=&v"(xres[0][2]), "=&v"(xres[0][3]), "=&v"(xres[1][0]), "=&v"(xres[1][1]), "=&v"(xres[1][2]), "=&v"(xres[1][3]) : "v"(xp), "v"(xq));
    asm volatile("global_load_dwordx2 %0, %4, off\n\tglobal_load_dwordx2 %1, %4, off offset:32\n\tglobal_load_dwordx2 %2, %4, off offset:64\n\tglobal_load_dwordx2 %3, %4, off offset:96"
                 : "=&v"(bres[0]), "=&v"(bres[1]), "=&v"(bres[2]), "=&v"(bres[3]) : "v"(bp)); }
  OD(2, 3, 24, true, false); EV(3, 4, 20, true, false); OD(4, 0, 16, true, false); EV(0, 1, 12, true, false); OD(1, 2, 12, false, false);
  STAMP(1);
  if (kh) {
#pragma unroll
    for (int n = 0; n < 4; ++n) { f32x4 t = acc[0][n]; acc[0][n] = acc[2][n]; acc[2][n] = t; t = acc[1][n]; acc[1][n] = acc[3][n]; acc[3][n] = t; }
#pragma unroll
    for (int n = 0; n < 4; ++n) { PIN(acc[0][n]); PIN(acc[1][n]); PIN(acc[2][n]); PIN(acc[3][n]); }
  }
  const int le = lane_id(), ge = le >> 4;
  const int token0 = 128 * p + 64 * wm + 32 * kh + (le & 15);
  {
    const __amdgpu_buffer_rsrc_t rP = mkrsrc(ws + WS_PART + (size_t)gi * 32768, 16384);
#pragma unroll
    for (int m = 0; m < 2; ++m)
#pragma unroll
      for (int n2 = 0; n2 < 2; ++n2)
        st_wt16(rP, ((w * 2 + m) * 2 + n2) * 1024 + le * 16, (u32x4){cvtpk(acc[2 + m][2 * n2][0], acc[2 + m][2 * n2][1]), cvtpk(acc[2 + m][2 * n2][2], acc[2 + m][2 * n2][3]),
                                                                       cvtpk(acc[2 + m][2 * n2 + 1][0], acc[2 + m][2 * n2 + 1][1]), cvtpk(acc[2 + m][2 * n2 + 1][2], acc[2 + m][2 * n2 + 1][3])});
    asm volatile("s_waitcnt vmcnt(0)\n\ts_barrier" : "+v"(xres[0][0]), "+v"(xres[0][1]), "+v"(xres[0][2]), "+v"(xres[0][3]), "+v"(xres[1][0]), "+v"(xres[1][1]), "+v"(xres[1][2]), "+v"(xres[1][3]),
                 "+v"(bres[0]), "+v"(bres[1]), "+v"(bres[2]), "+v"(bres[3]) :: "memory");
    if (w == 0) { if (le == 0) signal(ws, FL_X + gi, tag); await(ws, FL_X + partner, tag); }
    asm volatile("s_barrier" ::: "memory");
  }
  STAMP(2);
  const __amdgpu_buffer_rsrc_t rPP = mkrsrc(ws + WS_PART + (size_t)partner * 32768, 16384);
  u32x4 pv[2][2];
#pragma unroll
  for (int m = 0; m < 2; ++m)
#pragma unroll
    for (int n2 = 0; n2 < 2; ++n2) pv[m][n2] = __builtin_amdgcn_raw_buffer_load_b128(rPP, ((w * 2 + m) * 2 + n2) * 1024 + le * 16, 0, 16);
#pragma unroll
  for (int m = 0; m < 2; ++m) {
    unsigned d[4][2];
#pragma unroll
    for (int n = 0; n < 4; ++n) {
      const unsigned p0 = pv[m][n >> 1][2 * (n & 1)], p1 = pv[m][n >> 1][2 * (n & 1) + 1];
      const f32x4 a4 = acc[m][n];
      d[n][0] = cvtpk(a4[0] + lo2f(p0) + lo2f(bres[n][0]) + lo2f(xres[m][n][0]), a4[1] + hi2f(p0) + hi2f(bres[n][0]) + hi2f(xres[m][n][0]));
      d[n][1] = cvtpk(a4[2] + lo2f(p1) + lo2f(bres[n][1]) + lo2f(xres[m][n][1]), a4[3] + hi2f(p1) + hi2f(bres[n][1]) + hi2f(xres[m][n][1]));
    }
    const int cch = ((ge & 1) << 1) | (ge >> 1);
#pragma unroll
    for (int n2 = 0; n2 < 2; ++n2) {
      const u32x4 ch = chunk_of(d[2 * n2][0], d[2 * n2][1], d[2 * n2 + 1][0], d[2 * n2 + 1][1]);
      __builtin_amdgcn_raw_buffer_store_b128(ch, mkrsrc(out, S_ * D_ * 2), ((token0 + 16 * m) * D_ + 128 * nt + 64 * wn + 32 * n2 + 8 * cch) * 2, 0, OUTAUX);
    }
  }
  STAMP(3);
#undef RDX
#undef RDW
}

constexpr int LDS_BYTES = 81920;
#define MODE 0
__global__ __launch_bounds__(256, 2) void k_main(const u16* __restrict__ x, const u16* __restrict__ wq, const u16* __restrict__ wk, const u16* __restrict__ wv, const u16* __restrict__ wo,
                                                  const u16* __restrict__ w1, const u16* __restrict__ b1, const u16* __restrict__ w2, const u16* __restrict__ b2, u16* __restrict__ out, char* __restrict__ ws) {
  extern __shared__ __attribute__((aligned(16))) char lds_[];
  LAS char* lds = (LAS char*)lds_;
  const int w = __builtin_amdgcn_readfirstlane(threadIdx.x >> 6);
  u64 eraw;
  { const GAS u64* ep = flagp(ws, FL_E); asm volatile("global_load_dwordx2 %0, %1, off sc1" : "=&v"(eraw) : "v"(ep)); }
  if ((blockIdx.x >> 8) == 0) {
    __builtin_amdgcn_s_setprio(PRIO_Q);
    const u64 tag = phase0(blockIdx.x, x, wk, wv, ws, lds, w, eraw);
    role_attn(blockIdx.x, wq, w2, wo, ws, lds, w, tag);
  } else {
    __builtin_amdgcn_s_setprio(PRIO_G1);
    const u64 tag = role_w1(blockIdx.x & 255, wq, w1, b1, w2, ws, lds, w, eraw);
    role_w2(blockIdx.x & 255, w2, wo, b2, x, out, ws, lds, w, tag);
    if (blockIdx.x == 256 && w == 0) {
      const int l = lane_id();
#pragma unroll 1
      for (int i = 0; i < 8; ++i) await(ws, FL_RD + 64 * i + l, tag);
      if (l == 0) signal(ws, FL_E, tag);
    }
  }
}

extern "C" void kernel_launch(void* const* d_in, const int* in_sizes, int n_in, void* d_out, int out_size, void* d_ws, size_t ws_size, hipStream_t stream) {
  const u16* x = (const u16*)d_in[0]; const u16* wq = (const u16*)d_in[1]; const u16* wk = (const u16*)d_in[2];
  const u16* wv = (const u16*)d_in[3]; const u16* wo = (const u16*)d_in[4];
  const u16* w1 = (const u16*)d_in[6]; const u16* b1 = (const u16*)d_in[7]; const u16* w2 = (const u16*)d_in[8]; const u16* b2 = (const u16*)d_in[9];
  (void)hipFuncSetAttribute((const void*)k_main, hipFuncAttributeMaxDynamicSharedMemorySize, LDS_BYTES);
  k_main<<<512, 256, LDS_BYTES, stream>>>(x, wq, wk, wv, wo, w1, b1, w2, b2, (u16*)d_out, (char*)d_ws);
}
```

```cpp
#include <hip/hip_runtime.h>
#include <stdint.h>

typedef unsigned short u16;
typedef __bf16 bf16x8 __attribute__((ext_vector_type(8)));
typedef __bf16 bf16x2 __attribute__((ext_vector_type(2)));
typedef short s16x8 __attribute__((ext_vector_type(8)));
typedef float f32x4 __attribute__((ext_vector_type(4)));
typedef float f32x2 __attribute__((ext_vector_type(2)));
typedef unsigned u32x4 __attribute__((ext_vector_type(4)));
typedef unsigned u32x2 __attribute__((ext_vector_type(2)));
#define LAS __attribute__((address_space(3)))
#define GAS __attribute__((address_space(1)))
#define DI __device__ __forceinline__
#define SB() __builtin_amdgcn_sched_barrier(0)
#define PIN(x) asm volatile("" : "+v"(x))
#define MFMA16(a, b, c) __builtin_amdgcn_mfma_f32_16x16x32_bf16((a), (b), (c), 0, 0, 0)
#define BF(x) __builtin_bit_cast(bf16x8, x)

#define PF0ROWS 1024
constexpr int S_ = 2048, D_ = 1024, F_ = 3584;
constexpr size_t WS_FLAGS = 0;
constexpr size_t WS_R2   = 131072;
constexpr size_t WS_KV   = WS_R2 + 8192;
constexpr size_t WS_XN   = WS_KV + 262144;
constexpr size_t WS_H    = WS_XN + 4194304;
constexpr size_t WS_O    = WS_H + (size_t)112 * 128 * 1024;
constexpr size_t WS_PART = WS_O + 4194304;
constexpr size_t WS_STAMP = WS_PART + (size_t)2 * 256 * 32768;

constexpr float QSCALE = 0.17677669529663687f * 1.4426950408889634f;

DI int lane_id() { int l; asm volatile("v_mbcnt_lo_u32_b32 %0, -1, 0\n\tv_mbcnt_hi_u32_b32 %0, -1, %0" : "=v"(l)); return l; }
DI float bf2f(u16 v) { return __uint_as_float(((unsigned)v) << 16); }
DI unsigned cvtpk(float lo, float hi) { f32x2 v = {lo, hi}; bf16x2 b = __builtin_convertvector(v, bf16x2); return __builtin_bit_cast(unsigned, b); }
DI float lo2f(unsigned u) { return __uint_as_float(u << 16); }
DI float hi2f(unsigned u) { return __uint_as_float(u & 0xffff0000u); }
DI __amdgpu_buffer_rsrc_t mkrsrc(const void* p, unsigned bytes) { return __builtin_amdgcn_make_buffer_rsrc((void*)p, 0, bytes, 0x00020000); }
DI void dma16(unsigned lds_addr, unsigned voff, __amdgpu_buffer_rsrc_t rsrc, unsigned soff) {
  asm volatile("s_mov_b32 m0, %0\n\ts_nop 0\n\tbuffer_load_dwordx4 %1, %2, %3 offen lds" :: "s"(lds_addr), "v"(voff), "s"(rsrc), "s"(soff));
}
template <int IMM, int INC> DI void dma2(unsigned mbase, unsigned v0, unsigned v1, __amdgpu_buffer_rsrc_t rsrc, unsigned& soff) {
  asm volatile("s_add_u32 m0, %3, %5\n\ts_add_u32 %0, %0, %6\n\tbuffer_load_dwordx4 %1, %4, %0 offen lds\n\tbuffer_load_dwordx4 %2, %4, %0 offen offset:1024 lds"
               : "+s"(soff) : "v"(v0), "v"(v1), "s"(mbase), "s"(rsrc), "i"(IMM), "i"(INC) : "scc");
}
template <int IMM, int INC> DI void dma1a(unsigned mbase, unsigned v0, __amdgpu_buffer_rsrc_t rsrc, unsigned& soff) {
  asm volatile("s_add_u32 m0, %2, %4\n\ts_add_u32 %0, %0, %5\n\tbuffer_load_dwordx4 %1, %3, %0 offen lds" : "+s"(soff) : "v"(v0), "s"(mbase), "s"(rsrc), "i"(IMM), "i"(INC) : "scc");
}
template <int OFF> DI void dma1b(unsigned v0, __amdgpu_buffer_rsrc_t rsrc, unsigned soff) {
  asm volatile("buffer_load_dwordx4 %0, %1, %2 offen offset:%3 lds" :: "v"(v0), "s"(rsrc), "s"(soff), "i"(OFF));
}
template <int IMM, int INC> DI void dma1a_c(unsigned mbase, unsigned v0, __amdgpu_buffer_rsrc_t rsrc, unsigned& soff) {
  asm volatile("s_add_u32 m0, %2, %4\n\ts_add_u32 %0, %0, %5\n\tbuffer_load_dwordx4 %1, %3, %0 offen sc1 lds" : "+s"(soff) : "v"(v0), "s"(mbase), "s"(rsrc), "i"(IMM), "i"(INC) : "scc");
}
template <int OFF> DI void dma1b_c(unsigned v0, __amdgpu_buffer_rsrc_t rsrc, unsigned soff) {
  asm volatile("buffer_load_dwordx4 %0, %1, %2 offen offset:%3 sc1 lds" :: "v"(v0), "s"(rsrc), "s"(soff), "i"(OFF));
}
DI void dma4(unsigned lds_addr, unsigned voff, __amdgpu_buffer_rsrc_t rsrc, unsigned soff) {
  asm volatile("s_mov_b32 m0, %0\n\ts_nop 0\n\tbuffer_load_dword %1, %2, %3 offen lds" :: "s"(lds_addr), "v"(voff), "s"(rsrc), "s"(soff));
}
DI void dma4_c(unsigned lds_addr, unsigned voff, __amdgpu_buffer_rsrc_t rsrc, unsigned soff) {
  asm volatile("s_mov_b32 m0, %0\n\ts_nop 0\n\tbuffer_load_dword %1, %2, %3 offen sc1 lds" :: "s"(lds_addr), "v"(voff), "s"(rsrc), "s"(soff));
}
template <int ADD> DI void dma4_ahead(unsigned lds_addr, unsigned voff, __amdgpu_buffer_rsrc_t rsrc, unsigned soff) {
  unsigned t;
  asm volatile("s_mov_b32 m0, %1\n\ts_add_u32 %0, %4, %5\n\tbuffer_load_dword %2, %3, %0 offen lds" : "=&s"(t) : "s"(lds_addr), "v"(voff), "s"(rsrc), "s"(soff), "i"(ADD) : "scc");
}
#define WAIT_BAR(N) asm volatile("s_waitcnt vmcnt(" #N ") lgkmcnt(0)\n\ts_barrier" ::: "memory")
DI bf16x8 ldsfrag(const LAS char* p) { return *(const LAS bf16x8*)p; }
typedef short v4i16_t __attribute__((ext_vector_type(4)));
DI bf16x8 ldstr(const LAS char* p0, const LAS char* p1) {
  v4i16_t a = __builtin_amdgcn_ds_read_tr16_b64_v4i16((LAS v4i16_t*)p0);
  v4i16_t b = __builtin_amdgcn_ds_read_tr16_b64_v4i16((LAS v4i16_t*)p1);
  s16x8 v = __builtin_shufflevector(a, b, 0, 1, 2, 3, 4, 5, 6, 7);
  return __builtin_bit_cast(bf16x8, v);
}
DI void st_wt16(__amdgpu_buffer_rsrc_t r, unsigned voff, u32x4 v) { __builtin_amdgcn_raw_buffer_store_b128(v, r, voff, 0, 16); }
DI u32x4 chunk_of(unsigned x0, unsigned x1, unsigned y0, unsigned y1) {
  auto s0 = __builtin_amdgcn_permlane16_swap(x0, y0, false, false);
  auto s1 = __builtin_amdgcn_permlane16_swap(x1, y1, false, false);
  return (u32x4){s0[0], s1[0], s0[1], s1[1]};
}
DI unsigned wdma_off(int l, int kblk, int ldbytes) {
  const int row = l >> 3, c8 = l & 7, sw = ((row >> 1) & 1) | ((kblk & 1) << 1);
  return row * ldbytes + (((c8 >> 1) ^ sw) * 32) + (c8 & 1) * 16;
}
DI unsigned wrd_off(int l, int seg) {
  const int g = l >> 4, q = (l & 15) >> 2, sw = ((q >> 1) & 1) | ((g & 1) << 1);
  return q * 128 + ((seg ^ sw) * 32) + (l & 3) * 8;
}
DI float xsum4(float v) {
  auto a = __builtin_amdgcn_permlane16_swap(__float_as_uint(v), __float_as_uint(v), false, false); v = __uint_as_float(a[0]) + __uint_as_float(a[1]);
  auto b = __builtin_amdgcn_permlane32_swap(__float_as_uint(v), __float_as_uint(v), false, false); return __uint_as_float(b[0]) + __uint_as_float(b[1]);
}
DI float xmax4(float v) {
  auto a = __builtin_amdgcn_permlane16_swap(__float_as_uint(v), __float_as_uint(v), false, false); v = fmaxf(__uint_as_float(a[0]), __uint_as_float(a[1]));
  auto b = __builtin_amdgcn_permlane32_swap(__float_as_uint(v), __float_as_uint(v), false, false); return fmaxf(__uint_as_float(b[0]), __uint_as_float(b[1]));
}
#define STAMP(i)

typedef unsigned long long u64;
constexpr int FL_E = 0, FL_RD = 16, FL_XN = 528, FL_KV = 784, FL_H = 1040, FL_O = 1296, FL_X = 1552;
DI GAS u64* flagp(char* ws, int i) { return (GAS u64*)(ws + WS_FLAGS + (size_t)i * 64); }
DI void signal(char* ws, int i, u64 tag) { __hip_atomic_store(flagp(ws, i), tag, __ATOMIC_RELAXED, __HIP_MEMORY_SCOPE_AGENT); }
DI u64 take_tag(char* ws, u64 eraw, int w) {
  asm volatile("" : "+v"(eraw));
  const u64 tag = (((u64)(unsigned)__builtin_amdgcn_readfirstlane((unsigned)(eraw >> 32))) << 32 | (unsigned)__builtin_amdgcn_readfirstlane((unsigned)eraw)) + 1;
  if (w == 0 && lane_id() == 0) signal(ws, FL_RD + blockIdx.x, tag);
  return tag;
}
DI void await(char* ws, int idx, u64 tag) {
  GAS u64* f = flagp(ws, idx < 0 ? FL_E : idx);
  for (unsigned spins = 0; spins < (1u << 20); ++spins) {
    const u64 v = __hip_atomic_load(f, __ATOMIC_RELAXED, __HIP_MEMORY_SCOPE_AGENT);
    if (__all(idx < 0 || v == tag)) break;
    __builtin_amdgcn_s_sleep(2);
  }
  asm volatile("" ::: "memory");
}

DI u64 phase0(int a, const u16* __restrict__ x, const u16* __restrict__ wk, const u16* __restrict__ wv, char* __restrict__ ws, LAS char* lds, const int w, const u64 eraw) {
  const int l = lane_id(), tid = w * 64 + l;
  LAS float* part = (LAS float*)(lds + 65536);
  LAS float* fin = (LAS float*)(lds + 65536 + 8192);
  LAS float* red = (LAS float*)(lds + 65536 + 8192 + 2048);
  const int rb = a & 127, hf = a >> 7;
  const int r = l & 15, c = l >> 4; const int stamp_base = 768; (void)stamp_base;
  STAMP(0);
  u32x4 xv[8];
  { const u16* xr = x + (size_t)(rb * 16 + r) * D_ + w * 256 + c * 8;
#define XL(i) asm volatile("global_load_dwordx4 %0, %1, off offset:" #i "*64" : "=&v"(xv[i]) : "v"(xr))
    XL(0); XL(1); XL(2); XL(3); XL(4); XL(5); XL(6); XL(7); }
  const unsigned lds0 = (unsigned)(uintptr_t)lds;
  {
    __amdgpu_buffer_rsrc_t rw = mkrsrc(hf ? wv : wk, 65536);
#pragma unroll
    for (int i = 0; i < 16; ++i) dma16(lds0 + w * 16384 + i * 1024, l * 16, rw, w * 16384 + i * 1024);
  }
  asm volatile("s_waitcnt vmcnt(16)" : "+v"(xv[0]), "+v"(xv[1]), "+v"(xv[2]), "+v"(xv[3]), "+v"(xv[4]), "+v"(xv[5]), "+v"(xv[6]), "+v"(xv[7]));
  STAMP(1);
  const u64 tag = take_tag(ws, eraw, w);
  float s = 0.f;
#pragma unroll
  for (int i = 0; i < 8; ++i)
#pragma unroll
    for (int j = 0; j < 4; ++j) s += lo2f(xv[i][j]) + hi2f(xv[i][j]);
  s = xsum4(s);
  if (c == 0) red[w * 16 + r] = s;
  asm volatile("s_waitcnt lgkmcnt(0)\n\ts_barrier" ::: "memory");
  const float mean = ((red[r] + red[16 + r]) + (red[32 + r] + red[48 + r])) * (1.0f / D_);
  float q = 0.f;
#pragma unroll
  for (int i = 0; i < 8; ++i)
#pragma unroll
    for (int j = 0; j < 4; ++j) { const float u = lo2f(xv[i][j]) - mean, v = hi2f(xv[i][j]) - mean; q += u * u + v * v; }
  q = xsum4(q);
  if (c == 0) red[64 + w * 16 + r] = q;
  asm volatile("s_waitcnt lgkmcnt(0)\n\ts_barrier" ::: "memory");
  const float var = ((red[64 + r] + red[80 + r]) + (red[96 + r] + red[112 + r])) * (1.0f / D_);
  const float rstd = 1.0f / sqrtf(var + 1e-5f);
#pragma unroll
  for (int i = 0; i < 8; ++i)
#pragma unroll
    for (int j = 0; j < 4; ++j) xv[i][j] = cvtpk((lo2f(xv[i][j]) - mean) * rstd, (hi2f(xv[i][j]) - mean) * rstd);
  const __amdgpu_buffer_rsrc_t rXN = mkrsrc(ws + WS_XN, 4194304);
#pragma unroll
  for (int i = 0; i < 4; ++i) st_wt16(rXN, ((8 * w + 2 * i + hf) * 128 + rb) * 1024 + l * 16, hf ? xv[2 * i + 1] : xv[2 * i]);
  if (hf == 0 && w == 0 && c == 0) __hip_atomic_store((GAS float*)(ws + WS_R2) + rb * 16 + r, 1.0f / sqrtf(var * rstd * rstd + 1e-5f), __ATOMIC_RELAXED, __HIP_MEMORY_SCOPE_AGENT);
  asm volatile("s_waitcnt vmcnt(4)" ::: "memory");
  f32x4 acc[2] = {{0.f, 0.f, 0.f, 0.f}, {0.f, 0.f, 0.f, 0.f}};
  const LAS char* wb = (const LAS char*)lds + w * 16384 + (8 * c + ((l & 15) >> 2)) * 64 + (l & 3) * 8;
#pragma unroll
  for (int i = 0; i < 8; ++i)
#pragma unroll
    for (int b = 0; b < 2; ++b) {
      bf16x8 wf = ldstr(wb + i * 2048 + b * 32, wb + i * 2048 + b * 32 + 256);
      acc[b] = MFMA16(BF(xv[i]), wf, acc[b]);
    }
#pragma unroll
  for (int b = 0; b < 2; ++b)
#pragma unroll
    for (int j = 0; j < 4; ++j) part[(w * 16 + 4 * c + j) * 32 + 16 * b + r] = acc[b][j];
  asm volatile("s_waitcnt vmcnt(0) lgkmcnt(0)\n\ts_barrier" ::: "memory");
  if (tid == 0) signal(ws, FL_XN + a, tag);
  STAMP(2);
#pragma unroll
  for (int u = 0; u < 2; ++u) { const int e = tid + 256 * u; fin[e] = (part[e] + part[512 + e]) + (part[1024 + e] + part[1536 + e]); }
  asm volatile("s_waitcnt lgkmcnt(0)\n\ts_barrier" ::: "memory");
  if (w == 0) {
    float o[8];
    unsigned dst = (rb >> 1) * 4096;
    const int half = rb & 1;
    if (hf == 0) { const int kb = l >> 5, g = (l >> 3) & 3, rr = l & 7;
      const int row = 8 * half + rr, key = 8 * (row >> 2) + 4 * kb + (row & 3) - 16 * half;
#pragma unroll
      for (int j = 0; j < 8; ++j) o[j] = fin[key * 32 + 16 * (j >> 2) + 4 * g + (j & 3)];
      dst += kb * 1024 + (g * 16 + row) * 16;
    } else { const int db = l >> 5, gl = (l >> 4) & 1, rr = l & 15;
#pragma unroll
      for (int j = 0; j < 8; ++j) o[j] = fin[(8 * gl + j) * 32 + 16 * db + rr];
      dst += 2048 + db * 1024 + ((2 * half + gl) * 16 + rr) * 16;
    }
    st_wt16(mkrsrc(ws + WS_KV, 262144), dst, (u32x4){cvtpk(o[0], o[1]), cvtpk(o[2], o[3]), cvtpk(o[4], o[5]), cvtpk(o[6], o[7])});
    asm volatile("s_waitcnt vmcnt(0)" ::: "memory");
    if (l == 0) signal(ws, FL_KV + a, tag);
  }
  STAMP(3);
  return tag;
}

#define MERGED 1
#define PRIO_A 1
#define G1PAT 0
#define G2PAT 0
#define PFSLEEP 80
#define GDELAY 0
#define PFROWS 256
#define PRIO_Q 1
#define PRIO_G1 0
#define PRIO_GE 2
#define PRIO_G2 0
#define LSUM_MFMA 1
DI float fadd_s(float a, float b) { float r; asm("s_nop 0\n\tv_add_f32_e32 %0, %1, %2" : "=v"(r) : "v"(a), "v"(b)); return r; }

DI void attn_slow(const char* __restrict__ kv, bf16x8 qf, f32x4& o0, f32x4& o1) {
  const int l = lane_id();
  const __amdgpu_buffer_rsrc_t rk = mkrsrc(kv, 262144);
  o0 = (f32x4){0.f, 0.f, 0.f, 0.f}; o1 = o0;
  float m = -3.0e38f, ls = 0.f;
#pragma unroll 1
  for (int t = 0; t < 64; ++t) {
    const int p = t * 4096 + l * 16;
    const bf16x8 k0 = BF(__builtin_amdgcn_raw_buffer_load_b128(rk, p, 0, 16)), k1 = BF(__builtin_amdgcn_raw_buffer_load_b128(rk, p + 1024, 0, 16)),
                 v0 = BF(__builtin_amdgcn_raw_buffer_load_b128(rk, p + 2048, 0, 16)), v1 = BF(__builtin_amdgcn_raw_buffer_load_b128(rk, p + 3072, 0, 16));
    const f32x4 z = {0.f, 0.f, 0.f, 0.f};
    f32x4 s0 = MFMA16(k0, qf, z), s1 = MFMA16(k1, qf, z);
    float mx = fmaxf(fmaxf(fmaxf(s0[0], s0[1]), fmaxf(s0[2], s0[3])), fmaxf(fmaxf(s1[0], s1[1]), fmaxf(s1[2], s1[3])));
    mx = fmaxf(mx, __shfl_xor(mx, 16)); mx = fmaxf(mx, __shfl_xor(mx, 32));
    const float mn = fmaxf(m, mx), al = __builtin_amdgcn_exp2f(m - mn);
    m = mn; float rs = 0.f;
#pragma unroll
    for (int i = 0; i < 4; ++i) { s0[i] = __builtin_amdgcn_exp2f(s0[i] - mn); s1[i] = __builtin_amdgcn_exp2f(s1[i] - mn); rs += s0[i] + s1[i]; o0[i] *= al; o1[i] *= al; }
    rs += __shfl_xor(rs, 16); rs += __shfl_xor(rs, 32); ls = ls * al + rs;
    const u32x4 pp = {cvtpk(s0[0], s0[1]), cvtpk(s0[2], s0[3]), cvtpk(s1[0], s1[1]), cvtpk(s1[2], s1[3])};
    o0 = MFMA16(v0, BF(pp), o0); o1 = MFMA16(v1, BF(pp), o1);
  }
#pragma unroll
  for (int i = 0; i < 4; ++i) { o0[i] /= ls; o1[i] /= ls; }
}

DI void role_attn(int a, const u16* __restrict__ wq, const u16* __restrict__ w2, const u16* __restrict__ wo, char* __restrict__ ws, LAS char* lds, const int w, const u64 tag) {
  const int l = lane_id();
#define AMAP 2
  constexpr int NTB = 32 / AMAP;
  const int xcd = a & 7, idx = a >> 3, tb = (xcd / (8 / AMAP)) * NTB + (idx % NTB), hg = (xcd % (8 / AMAP)) * AMAP + idx / NTB;
  const int g = l >> 4; const int stamp_base = 0; (void)stamp_base;
  const unsigned lds0 = (unsigned)(uintptr_t)lds;
  constexpr int SLOT = 24576;
  const __amdgpu_buffer_rsrc_t rX = mkrsrc(ws + WS_XN, 4194304), rKV = mkrsrc(ws + WS_KV, 262144);
  const unsigned vlin = l * 16;
  constexpr int QS = 12288;
  const __amdgpu_buffer_rsrc_t rWq = mkrsrc((const char*)wq - 1024, D_ * D_ * 2 + 1024);
  const unsigned vq0 = wdma_off(l, w, 2048) + hg * 256 + w * 8 * 2048 + 1024, vq1 = vq0 + 128 - 1024;
  unsigned xso = (4 * tb + w) * 1024 - 131072, qso = 0u - 65536;
  const unsigned mx = lds0 + w * 1024, mq = lds0 + 4096 + w * 2048;
#define PCQ(SL, i) do { if constexpr ((i) == 0) dma1a_c<(SL) * QS, 131072>(mx, vlin, rX, xso); else if constexpr ((i) == 1) dma1a<(SL) * QS, 65536>(mq, vq0, rWq, qso); else dma1b<1024>(vq1, rWq, qso); } while (0)
#define STGQ(SL) do { PCQ(SL, 0); PCQ(SL, 1); PCQ(SL, 2); } while (0)
  if (w == 0) await(ws, l < 8 ? FL_XN + 4 * tb + (l & 3) + 128 * (l >> 2) : -1, tag);
  asm volatile("s_waitcnt lgkmcnt(0)\n\ts_barrier" ::: "memory");
  STAMP(0);
  constexpr int NPL = 64 / (4 * NTB);
  const int pl = ((idx % NTB) * 4 + w) * NPL + l;
  const unsigned vpq = l < NPL ? (pl >> 1) * 2048 + hg * 256 + (pl & 1) * 128 + 1024 : 0x80000000u;
  const unsigned mpq = lds0 + 6 * QS;
  STGQ(0); STGQ(1); STGQ(2); STGQ(3); STGQ(4); STGQ(5);
  dma4_ahead<1 * 65536>(mpq, vpq, rWq, qso); dma4_ahead<2 * 65536>(mpq, vpq, rWq, qso); dma4_ahead<3 * 65536>(mpq, vpq, rWq, qso);
  dma4_ahead<4 * 65536>(mpq, vpq, rWq, qso); dma4_ahead<5 * 65536>(mpq, vpq, rWq, qso); dma4_ahead<6 * 65536>(mpq, vpq, rWq, qso);
  f32x4 qacc[2][4];
#pragma unroll
  for (int i = 0; i < 2; ++i)
#pragma unroll
    for (int j = 0; j < 4; ++j) qacc[i][j] = (f32x4){0.f, 0.f, 0.f, 0.f};
  const LAS char* xrd = lds + l * 16;
  const LAS char* wrd0 = lds + 4096 + (g * 2 + (w >> 1)) * 1024 + wrd_off(l, 2 * (w & 1));
  const LAS char* wrd1 = lds + 4096 + (g * 2 + (w >> 1)) * 1024 + wrd_off(l, 2 * (w & 1) + 1);
  bf16x8 qxa[4], qwa[2], qxb[4], qwb[2];
#define QRX(X, m, SL) X[m] = ldsfrag(xrd + (SL) * QS + (m) * 1024)
#define QRW(W, n, SL) W[n] = ((n) == 0 ? ldstr(wrd0 + (SL) * QS, wrd0 + (SL) * QS + 512) : ldstr(wrd1 + (SL) * QS, wrd1 + (SL) * QS + 512))
#define QM(X, W, m, n) qacc[n][m] = MFMA16(W[n], X[m], qacc[n][m])
#define STEPQ(X, W, XN, WN, RD, RS, DMA, DS) do { \
    QM(X, W, 0, 0); if (RD) { QRW(WN, 0, RS); QRX(XN, 0, RS); } SB(); QM(X, W, 1, 0); if (RD) { QRW(WN, 1, RS); QRX(XN, 1, RS); } SB(); QM(X, W, 2, 0); if (RD) QRX(XN, 2, RS); SB(); QM(X, W, 3, 0); if (RD) QRX(XN, 3, RS); SB(); \
    QM(X, W, 0, 1); if (DMA) PCQ(DS, 0); SB(); QM(X, W, 1, 1); if (DMA) PCQ(DS, 1); SB(); QM(X, W, 2, 1); if (DMA) PCQ(DS, 2); SB(); QM(X, W, 3, 1); if (DMA) dma4_ahead<6 * 65536>(mpq, vpq, rWq, qso); SB(); } while (0)
  WAIT_BAR(21);
#pragma unroll
  for (int m = 0; m < 4; ++m) QRX(qxa, m, 0);
  QRW(qwa, 0, 0); QRW(qwa, 1, 0);
#define QEV(SC, SN, W_, RD, DMA) do { WAIT_BAR(W_); SB(); STEPQ(qxa, qwa, qxb, qwb, RD, SN, DMA, SC); } while (0)
#define QOD(SC, SN, W_, RD, DMA) do { WAIT_BAR(W_); SB(); STEPQ(qxb, qwb, qxa, qwa, RD, SN, DMA, SC); } while (0)
  QEV(0, 1, 18, true, true); QOD(1, 2, 19, true, true); QEV(2, 3, 20, true, true); QOD(3, 4, 21, true, true); QEV(4, 5, 22, true, true); QOD(5, 0, 17, true, true);
  for (int it = 0; it < 3; ++it) { QEV(0, 1, 17, true, true); QOD(1, 2, 17, true, true); QEV(2, 3, 17, true, true); QOD(3, 4, 17, true, true); QEV(4, 5, 17, true, true); QOD(5, 0, 17, true, true); }
  QEV(0, 1, 17, true, true); QOD(1, 2, 17, true, true);
  QEV(2, 3, 17, true, false); QOD(3, 4, 13, true, false); QEV(4, 5, 9, true, false); QOD(5, 0, 5, true, false);
  if (w == 0) { await(ws, FL_KV + l, tag); await(ws, FL_KV + 64 + l, tag); await(ws, FL_KV + 128 + l, tag); await(ws, FL_KV + 192 + l, tag); }
  QEV(0, 1, 0, true, false);
  unsigned kvso = w * 4096 - 16384;
  const unsigned kvm = lds0 + w * 4096;
#define STGKV(SF) do { dma1a_c<(SF) * SLOT, 16384>(kvm, vlin, rKV, kvso); dma1b_c<1024>(vlin, rKV, kvso); dma1b_c<2048>(vlin, rKV, kvso); dma1b_c<3072>(vlin, rKV, kvso); } while (0)
  STGKV(1); STGKV(2);
  {
    const unsigned line = (a * 4 + w) * 64 + l;
    dma4(lds0 + 6 * QS, line * 128, mkrsrc(w2, F_ * D_ * 2), 0);
    dma4(lds0 + 6 * QS, line * 128, mkrsrc(wo, D_ * D_ * 2), 0);
  }
  STEPQ(qxb, qwb, qxa, qwa, false, 0, false, 0);
  STAMP(1);
  __builtin_amdgcn_s_setprio(PRIO_A);
  bf16x8 qf[4];
#pragma unroll
  for (int m = 0; m < 4; ++m)
    qf[m] = BF(((u32x4){cvtpk(qacc[0][m][0] * QSCALE, qacc[0][m][1] * QSCALE), cvtpk(qacc[0][m][2] * QSCALE, qacc[0][m][3] * QSCALE),
                        cvtpk(qacc[1][m][0] * QSCALE, qacc[1][m][1] * QSCALE), cvtpk(qacc[1][m][2] * QSCALE, qacc[1][m][3] * QSCALE)}));
  WAIT_BAR(6);
  const LAS char* kvb = lds + l * 16;
  f32x4 negm[4], S[4][2], O[4][2];
  bf16x8 k0, k1;
  {
    const LAS char* p = kvb + 1 * SLOT;
    k0 = ldsfrag(p); k1 = ldsfrag(p + 1024);
    const f32x4 z = {0.f, 0.f, 0.f, 0.f};
#pragma unroll
    for (int m = 0; m < 4; ++m) {
      const f32x4 s0 = MFMA16(k0, qf[m], z), s1 = MFMA16(k1, qf[m], z);
      float mx = fmaxf(fmaxf(fmaxf(s0[0], s0[1]), fmaxf(s0[2], s0[3])), fmaxf(fmaxf(s1[0], s1[1]), fmaxf(s1[2], s1[3])));
      mx = xmax4(mx);
      negm[m] = (f32x4){-mx, -mx, -mx, -mx}; PIN(negm[m]);
      O[m][0] = z; O[m][1] = z;
    }
    S[0][0] = MFMA16(k0, qf[0], negm[0]); S[0][1] = MFMA16(k1, qf[0], negm[0]);
    S[1][0] = MFMA16(k0, qf[1], negm[1]); S[1][1] = MFMA16(k1, qf[1], negm[1]);
  }
  u32x4 P[4];
  P[2] = (u32x4){0, 0, 0, 0}; P[3] = P[2];
  bf16x8 va0 = BF(P[2]), va1 = va0, vb0, vb1;
  S[3][1] = (f32x4){0.f, 0.f, 0.f, 0.f};
  f32x4 L[4];
#pragma unroll
  for (int m = 0; m < 4; ++m) L[m] = (f32x4){0.f, 0.f, 0.f, 0.f};
  bf16x8 ones; { const short o1 = (l & 15) == 0 ? (short)0x3f80 : (short)0; s16x8 ov = {o1, o1, o1, o1, o1, o1, o1, o1}; ones = __builtin_bit_cast(bf16x8, ov); PIN(ones); }
#define LS(m) do { L[m] = MFMA16(ones, BF(P[m]), L[m]); PIN(L[m]); SB(); } while (0)
#define LADD2(m, kb, i)
#define NOLS(m)
#define EXPF(x) __builtin_amdgcn_exp2f(x)
#define EX2(m, kb, i) do { float t0_ = EXPF(S[m][kb][i]), t1_ = EXPF(S[m][kb][i + 1]); PIN(t0_); PIN(t1_); S[m][kb][i] = t0_; S[m][kb][i + 1] = t1_; SB(); } while (0)
#define CVA(m, kb) do { unsigned c_ = cvtpk(S[m][kb][0], S[m][kb][1]); PIN(c_); P[m][2 * kb] = c_; LADD2(m, kb, 0); SB(); } while (0)
#define CVB(m, kb) do { unsigned c_ = cvtpk(S[m][kb][2], S[m][kb][3]); PIN(c_); P[m][2 * kb + 1] = c_; LADD2(m, kb, 2); SB(); } while (0)
#define GRP(mq, kq, KF, pm, pk, me, ke, mp, db, VF, LSM, X) do { \
    S[mq][kq] = MFMA16(KF, qf[mq], negm[mq]); PIN(S[mq][kq]); SB(); X; CVB(pm, pk); EX2(me, ke, 0); \
    O[mp][db] = MFMA16(VF, BF(P[mp]), O[mp][db]); PIN(O[mp][db]); SB(); EX2(me, ke, 2); LSM(mp); CVA(me, ke); } while (0)
#define TILE(KC0, KC1, KN0, KN1, VP0, VP1, VC0, VC1, kn, vp, D) do { \
    GRP(2, 0, KC0, 3, 1, 0, 0, 2, 0, VP0, NOLS, KN0 = ldsfrag(kn); SB()); \
    GRP(2, 1, KC1, 0, 0, 0, 1, 2, 1, VP1, LS,   KN1 = ldsfrag((kn) + 1024); SB()); \
    GRP(3, 0, KC0, 0, 1, 1, 0, 3, 0, VP0, NOLS, VC0 = ldsfrag(vp); SB()); \
    GRP(3, 1, KC1, 1, 0, 1, 1, 3, 1, VP1, LS,   VC1 = ldsfrag((vp) + 1024); SB()); \
    GRP(0, 0, KN0, 1, 1, 2, 0, 0, 0, VC0, NOLS, D(0)); \
    GRP(0, 1, KN1, 2, 0, 2, 1, 0, 1, VC1, LS,   D(1)); \
    GRP(1, 0, KN0, 2, 1, 3, 0, 1, 0, VC0, NOLS, D(2)); \
    GRP(1, 1, KN1, 3, 0, 3, 1, 1, 1, VC1, LS,   D(3)); \
  } while (0)
#define NOD(i)
  bf16x8 kx0, kx1;
#define STAGE(SC, SN, SF, DM, LAST) do { \
    TILE(k0, k1, kx0, kx1, va0, va1, vb0, vb1, kvb + (SC) * SLOT + 4096, kvb + (SC) * SLOT + 2048, NOD); \
    TILE(kx0, kx1, k0, k1, vb0, vb1, va0, va1, kvb + (SC) * SLOT + 8192, kvb + (SC) * SLOT + 4096 + 2048, NOD); \
    TILE(k0, k1, kx0, kx1, va0, va1, vb0, vb1, kvb + (SC) * SLOT + 12288, kvb + (SC) * SLOT + 8192 + 2048, NOD); \
    WAIT_BAR(0);        \
    TILE(kx0, kx1, k0, k1, vb0, vb1, va0, va1, kvb + ((LAST) ? (SC) : (SN)) * SLOT, kvb + (SC) * SLOT + 12288 + 2048, DM); } while (0)
#define DM_(SF, i) do { if constexpr ((i) == 0) { dma1a_c<(SF) * SLOT, 16384>(kvm, vlin, rKV, kvso); } else { dma1b_c<(i) * 1024>(vlin, rKV, kvso); } SB(); } while (0)
#define DM0(i) DM_(0, i)
#define DM1(i) DM_(1, i)
#define DM2(i) DM_(2, i)
  for (int j = 0; j < 12; j += 3) { STAGE(1, 2, 0, DM0, false); STAGE(2, 0, 1, DM1, false); STAGE(0, 1, 2, DM2, false); STAMP(4 + j / 3); }
  STAGE(1, 2, 0, DM0, false); STAGE(2, 0, 1, DM1, false); STAGE(0, 1, 2, NOD, false); STAGE(1, 2, 0, NOD, true);
  CVB(3, 1);
  O[2][0] = MFMA16(va0, BF(P[2]), O[2][0]); O[2][1] = MFMA16(va1, BF(P[2]), O[2][1]); LS(2);
  O[3][0] = MFMA16(va0, BF(P[3]), O[3][0]); O[3][1] = MFMA16(va1, BF(P[3]), O[3][1]); LS(3);
  STAMP(2);
  const __amdgpu_buffer_rsrc_t rO = mkrsrc(ws + WS_O, 4194304);
#pragma unroll
  for (int m = 0; m < 4; ++m) {
    float ls = __shfl(L[m][0], l & 15);
    float inv = 1.0f / ls;
    f32x4 o0 = O[m][0], o1 = O[m][1];
    const float mo = fmaxf(fmaxf(fmaxf(fabsf(o0[0]), fabsf(o0[1])), fmaxf(fabsf(o0[2]), fabsf(o0[3]))), fmaxf(fmaxf(fabsf(o1[0]), fabsf(o1[1])), fmaxf(fabsf(o1[2]), fabsf(o1[3]))));
    const bool ok = (ls <= 1.0e18f) && (mo <= 1.0e37f);
    if (__builtin_expect(__any(!ok), 0)) { attn_slow(ws + WS_KV, qf[m], o0, o1); inv = 1.0f; }
    const u32x4 ch = chunk_of(cvtpk(o0[0] * inv, o0[1] * inv), cvtpk(o0[2] * inv, o0[3] * inv), cvtpk(o1[0] * inv, o1[1] * inv), cvtpk(o1[2] * inv, o1[3] * inv));
    const int c = ((g & 1) << 1) | (g >> 1);
    st_wt16(rO, ((4 * hg + w) * 128 + 4 * tb + m) * 1024 + c * 256 + (l & 15) * 16, ch);
  }
  asm volatile("s_waitcnt vmcnt(0)\n\ts_barrier" ::: "memory");
  if (w == 0 && lane_id() == 0) signal(ws, FL_O + tb * 8 + hg, tag);
  STAMP(3);
}

DI int w2_kh(int gi) { return gi & 1; }
DI void w2_weights_early(int gi, const u16* __restrict__ w2, LAS char* lds, const int w) {
  const int l = lane_id(), nt = gi >> 5;
  const __amdgpu_buffer_rsrc_t rW = mkrsrc((const char*)w2 - 1024, F_ * D_ * 2 + 1024);
  const unsigned vw0 = wdma_off(l, w, 2048) + nt * 256 + w * 8 * 2048 + 1024, vw1 = vw0 + 128 - 1024;
  unsigned wso = 56 * w2_kh(gi) * 65536 - 65536;
  const unsigned mw = (unsigned)(uintptr_t)lds + 8192 + w * 2048;
  dma1a<0 * 16384, 65536>(mw, vw0, rW, wso); dma1b<1024>(vw1, rW, wso); dma1a<1 * 16384, 65536>(mw, vw0, rW, wso); dma1b<1024>(vw1, rW, wso);
  dma1a<2 * 16384, 65536>(mw, vw0, rW, wso); dma1b<1024>(vw1, rW, wso); dma1a<3 * 16384, 65536>(mw, vw0, rW, wso); dma1b<1024>(vw1, rW, wso);
}

DI f32x4 gelu4(f32x4 acc, float rs, f32x4 b, float c1v) {
  constexpr float c2 = -2.0f * 0.7978845608028654f * 1.4426950408889634f * 0.044715f;
  float x0 = acc[0], x1 = acc[1], x2 = acc[2], x3 = acc[3], t0, t1, t2, t3;
  asm("v_fma_f32 %0, %0, %8, %9\n\tv_fma_f32 %1, %1, %8, %10\n\tv_fma_f32 %2, %2, %8, %11\n\tv_fma_f32 %3, %3, %8, %12\n\t"
      "v_mul_f32 %4, %0, %0\n\tv_mul_f32 %5, %1, %1\n\tv_mul_f32 %6, %2, %2\n\tv_mul_f32 %7, %3, %3\n\t"
      "v_fma_f32 %4, %4, %14, %13\n\tv_fma_f32 %5, %5, %14, %13\n\tv_fma_f32 %6, %6, %14, %13\n\tv_fma_f32 %7, %7, %14, %13\n\t"
      "v_mul_f32 %4, %0, %4\n\tv_mul_f32 %5, %1, %5\n\tv_mul_f32 %6, %2, %6\n\tv_mul_f32 %7, %3, %7\n\t"
      "v_exp_f32 %4, %4\n\tv_exp_f32 %5, %5\n\tv_exp_f32 %6, %6\n\tv_exp_f32 %7, %7\n\t"
      "v_add_f32 %4, 1.0, %4\n\tv_add_f32 %5, 1.0, %5\n\tv_add_f32 %6, 1.0, %6\n\tv_add_f32 %7, 1.0, %7\n\t"
      "v_rcp_f32 %4, %4\n\tv_rcp_f32 %5, %5\n\tv_rcp_f32 %6, %6\n\tv_rcp_f32 %7, %7\n\t"
      "v_mul_f32 %0, %0, %4\n\tv_mul_f32 %1, %1, %5\n\tv_mul_f32 %2, %2, %6\n\tv_mul_f32 %3, %3, %7"
      : "+v"(x0), "+v"(x1), "+v"(x2), "+v"(x3), "=&v"(t0), "=&v"(t1), "=&v"(t2), "=&v"(t3)
      : "v"(rs), "v"(b[0]), "v"(b[1]), "v"(b[2]), "v"(b[3]), "v"(c1v), "s"(c2));
  return (f32x4){x0, x1, x2, x3};
}

DI u64 role_w1(int gi, const u16* __restrict__ wq, const u16* __restrict__ w1, const u16* __restrict__ b1, const u16* __restrict__ w2, char* __restrict__ ws, LAS char* lds, const int w, u64 eraw) {
  const int l = lane_id();
  const int xcd = gi & 7, idx = gi >> 3, p = (xcd >> 2) * 8 + (idx & 7), j = (xcd & 3) * 4 + (idx >> 3);
  const int g = l >> 4, wm = w >> 1, wn = w & 1; const int stamp_base = 256; (void)stamp_base;
  const unsigned lds0 = (unsigned)(uintptr_t)lds;
  constexpr int SLOT = 24576;
  const __amdgpu_buffer_rsrc_t rX = mkrsrc(ws + WS_XN, 4194304), rW = mkrsrc((const char*)w1 - 4096, D_ * F_ * 2 + 4096);
  const unsigned vlin = l * 16;
  unsigned vw[4];
#pragma unroll
  for (int i = 0; i < 4; ++i) vw[i] = wdma_off(l, w, F_ * 2) + 8 * w * (F_ * 2) + 448 * j - 64 * (j & 1) + 128 * i + 4096 - 1024 * i;
  STAMP(0);
  unsigned xso = (8 * p + 2 * w) * 1024 - 131072, wso = 0u - 32 * F_ * 2;
  const unsigned mx = lds0 + w * 2048, mw = lds0 + 8192 + w * 4096;
#define PC1(SL, i) do { if constexpr ((i) == 0) dma1a_c<(SL) * SLOT, 131072>(mx, vlin, rX, xso); else if constexpr ((i) == 1) dma1b_c<1024>(vlin, rX, xso); \
    else if constexpr ((i) == 2) dma1a<(SL) * SLOT, 32 * F_ * 2>(mw, vw[0], rW, wso); else dma1b<((i) - 2) * 1024>(vw[(i) - 2], rW, wso); } while (0)
#define STG1(SL) do { PC1(SL, 0); PC1(SL, 1); PC1(SL, 2); PC1(SL, 3); PC1(SL, 4); PC1(SL, 5); } while (0)
  PC1(0, 2); PC1(0, 3); PC1(0, 4); PC1(0, 5); PC1(1, 2); PC1(1, 3); PC1(1, 4); PC1(1, 5); PC1(2, 2); PC1(2, 3); PC1(2, 4); PC1(2, 5);
  constexpr int L_R2 = 3 * SLOT, L_B1 = L_R2 + 512;
  const unsigned vpf = l < 4 ? (4 * (idx & 7) + w) * (F_ * 2) + 448 * j - 64 * (j & 1) + l * 128 + 4096 : 0x80000000u;
  const unsigned mpf = lds0 + L_B1 + 1024;
  dma4_ahead<1 * 32 * F_ * 2>(mpf, vpf, rW, wso); dma4_ahead<2 * 32 * F_ * 2>(mpf, vpf, rW, wso); dma4_ahead<3 * 32 * F_ * 2>(mpf, vpf, rW, wso);
  dma4_ahead<4 * 32 * F_ * 2>(mpf, vpf, rW, wso); dma4_ahead<5 * 32 * F_ * 2>(mpf, vpf, rW, wso);
  if (w == 1) dma4(mpf, (gi * 64 + l) * 128, mkrsrc(wq, D_ * D_ * 2), 0);
  if (w >= 2 && PFROWS > 0) { constexpr int NL = PFROWS * 56 / 512;
#pragma unroll
    for (int i = 0; i < (NL + 63) / 64; ++i) dma4(mpf, i * 64 + l < NL ? 256 * F_ * 2 + ((gi * 2 + (w - 2)) * NL + i * 64 + l) * 128 : 0x80000000u, mkrsrc(w1, D_ * F_ * 2), 0);
  }
  if (w >= 2) dma4(lds0 + L_B1 + (w - 2) * 256, 448 * j + ((w - 2) * 64 + l) * 4, mkrsrc(b1, F_ * 2), 0);
  asm volatile("s_waitcnt vmcnt(17)" : "+v"(eraw));
  const u64 tag = take_tag(ws, eraw, w);
  if (w == 0) await(ws, l < 16 ? FL_XN + 8 * p + (l & 7) + 128 * (l >> 3) : -1, tag);
  asm volatile("s_barrier" ::: "memory");
  STAMP(8);
  if (w < 2) dma4_c(lds0 + L_R2 + w * 256, (128 * p + 64 * w + l) * 4, mkrsrc(ws + WS_R2, 8192), 0);
  PC1(0, 0); PC1(0, 1); PC1(1, 0); PC1(1, 1); PC1(2, 0); PC1(2, 1);
  dma4(mpf, 0x80000000u, rW, 0);
  f32x4 acc[4][7];
#pragma unroll
  for (int m = 0; m < 4; ++m)
#pragma unroll
    for (int n = 0; n < 7; ++n) acc[m][n] = (f32x4){0.f, 0.f, 0.f, 0.f};
  const LAS char* xrd = lds + 4 * wm * 1024 + l * 16;
  const LAS char* wrb[5];
  { const int off = 6 * wn + 2 * (j & 1), nl = 12 + wn + 2 * (j & 1);
#pragma unroll
    for (int i = 0; i < 4; ++i) wrb[i] = lds + 8192 + (g * 4 + ((i + off) >> 2)) * 1024 + wrd_off(l, (i + off) & 3);
    wrb[4] = lds + 8192 + (g * 4 + (nl >> 2)) * 1024 + wrd_off(l, nl & 3); }
  bf16x8 xa[4], wa[7], xb[4], wb[7];
#define RDX(X, m, so) X[m] = ldsfrag(xrd + (so) + (m) * 1024)
#define RDW(W, n, so) W[n] = ((n) < 6 ? ldstr(wrb[(n) & 3] + (so) + ((n) >> 2) * 1024, wrb[(n) & 3] + (so) + ((n) >> 2) * 1024 + 512) : ldstr(wrb[4] + (so), wrb[4] + (so) + 512))
#define M1(X, W, m, n) acc[m][n] = MFMA16(W[n], X[m], acc[m][n])
#define STEP1(X, W, XN, WN, RD, RS, DMA, DS) do { \
    M1(X, W, 0, 0); if (RD) RDW(WN, 0, (RS) * SLOT); SB(); M1(X, W, 1, 0); if (RD) RDX(XN, 0, (RS) * SLOT); SB(); M1(X, W, 2, 0); if (RD) RDW(WN, 1, (RS) * SLOT); SB(); M1(X, W, 3, 0); if (DMA) PC1(DS, 0); SB(); \
    M1(X, W, 0, 1); if (RD) RDX(XN, 1, (RS) * SLOT); SB(); M1(X, W, 1, 1); if (RD) RDW(WN, 2, (RS) * SLOT); SB(); M1(X, W, 2, 1); if (RD) RDX(XN, 2, (RS) * SLOT); SB(); M1(X, W, 3, 1); if (DMA) PC1(DS, 1); SB(); \
    M1(X, W, 0, 2); if (RD) RDW(WN, 3, (RS) * SLOT); SB(); M1(X, W, 1, 2); if (RD) RDX(XN, 3, (RS) * SLOT); SB(); M1(X, W, 2, 2); if (RD) RDW(WN, 4, (RS) * SLOT); SB(); M1(X, W, 3, 2); if (DMA) PC1(DS, 2); SB(); \
    M1(X, W, 0, 3); if (RD) RDW(WN, 5, (RS) * SLOT); SB(); M1(X, W, 1, 3); SB(); M1(X, W, 2, 3); if (RD) RDW(WN, 6, (RS) * SLOT); SB(); M1(X, W, 3, 3); if (DMA) PC1(DS, 3); SB(); \
    M1(X, W, 0, 4); SB(); M1(X, W, 1, 4); SB(); M1(X, W, 2, 4); SB(); M1(X, W, 3, 4); if (DMA) PC1(DS, 4); SB(); \
    M1(X, W, 0, 5); SB(); M1(X, W, 1, 5); SB(); M1(X, W, 2, 5); SB(); M1(X, W, 3, 5); if (DMA) { PC1(DS, 5); dma4_ahead<5 * 32 * F_ * 2>(mpf, vpf, rW, wso); } SB(); \
    M1(X, W, 0, 6); SB(); M1(X, W, 1, 6); SB(); M1(X, W, 2, 6); SB(); M1(X, W, 3, 6); SB(); } while (0)
  WAIT_BAR(3);
#pragma unroll
  for (int m = 0; m < 4; ++m) RDX(xa, m, 0);
#pragma unroll
  for (int n = 0; n < 7; ++n) RDW(wa, n, 0);
#define EV(SC, SN, W_, RD, DMA) do { WAIT_BAR(W_); SB(); STEP1(xa, wa, xb, wb, RD, SN, DMA, SC); } while (0)
#define OD(SC, SN, W_, RD, DMA) do { WAIT_BAR(W_); SB(); STEP1(xb, wb, xa, wa, RD, SN, DMA, SC); } while (0)
#define PR1(i) do { if constexpr ((i) == 0 || (((G1PAT >> (i)) & 1) != ((G1PAT >> ((i) - 1 < 0 ? 0 : (i) - 1)) & 1))) __builtin_amdgcn_s_setprio(((G1PAT >> (i)) & 1) ? 2 : 0); } while (0)
#define SIX() do { PR1(0); EV(0, 1, 8, true, true); PR1(1); OD(1, 2, 8, true, true); PR1(2); EV(2, 0, 8, true, true); PR1(3); OD(0, 1, 8, true, true); PR1(4); EV(1, 2, 8, true, true); PR1(5); OD(2, 0, 8, true, true); } while (0)
  for (int it = 0; it < 4; ++it) { SIX(); STAMP(4 + it); }
  EV(0, 1, 8, true, true); OD(1, 2, 8, true, true); EV(2, 0, 8, true, true); OD(0, 1, 8, true, true); EV(1, 2, 8, true, true);
  OD(2, 0, 8, true, false); EV(0, 1, 0, true, false); OD(1, 2, 0, false, false);
  STAMP(1);
  __builtin_amdgcn_s_setprio(PRIO_GE);
  w2_weights_early(gi, w2, lds, w);
  const int le = lane_id(), ge = le >> 4;
  float r2[4];
#pragma unroll
  for (int m = 0; m < 4; ++m) r2[m] = *(const LAS float*)(lds + L_R2 + (64 * wm + 16 * m + (le & 15)) * 4);
  const __amdgpu_buffer_rsrc_t rH = mkrsrc(ws + WS_H, 112 * 128 * 1024);
  const int cch = ((ge & 1) << 1) | (ge >> 1);
  unsigned last[4][2];
  float c1v = -2.0f * 0.7978845608028654f * 1.4426950408889634f; PIN(c1v);
#pragma unroll
  for (int n = 0; n < 7; ++n) {
    const int nb = n < 6 ? 6 * wn + n : 12 + wn;
    const u32x2 braw = *(const LAS u32x2*)(lds + L_B1 + (16 * nb + 4 * ge) * 2);
    const f32x4 bb = {lo2f(braw[0]), hi2f(braw[0]), lo2f(braw[1]), hi2f(braw[1])};
#pragma unroll
    for (int m = 0; m < 4; ++m) acc[m][n] = gelu4(acc[m][n], r2[m], bb, c1v);
    if (n < 6) { if (n & 1) {
#pragma unroll
        for (int m = 0; m < 4; ++m) {
          const u32x4 ch = chunk_of(cvtpk(acc[m][n - 1][0], acc[m][n - 1][1]), cvtpk(acc[m][n - 1][2], acc[m][n - 1][3]), cvtpk(acc[m][n][0], acc[m][n][1]), cvtpk(acc[m][n][2], acc[m][n][3]));
          st_wt16(rH, ((7 * j + 3 * wn + (n >> 1)) * 128 + 8 * p + 4 * wm + m) * 1024 + cch * 256 + (le & 15) * 16, ch);
        } }
    } else {
#pragma unroll
      for (int m = 0; m < 4; ++m) { last[m][0] = cvtpk(acc[m][n][0], acc[m][n][1]); last[m][1] = cvtpk(acc[m][n][2], acc[m][n][3]); }
    }
  }
#pragma unroll
  for (int m = 0; m < 4; m += 2) {
    const u32x4 ch = chunk_of(last[m][0], last[m][1], last[m + 1][0], last[m + 1][1]);
    st_wt16(rH, ((7 * j + 6) * 128 + 8 * p + 4 * wm + m + (ge & 1)) * 1024 + (2 * wn + (ge >> 1)) * 256 + (le & 15) * 16, ch);
  }
  STAMP(2);
  __builtin_amdgcn_s_setprio(PRIO_G2);
  asm volatile("s_waitcnt vmcnt(0)\n\ts_barrier" ::: "memory");
  if (w == 0 && lane_id() == 0) signal(ws, FL_H + p * 16 + j, tag);
  STAMP(3);
#undef RDX
#undef RDW
#undef EV
#undef OD
  return tag;
}

DI void role_w2(int gi, const u16* __restrict__ w2, const u16* __restrict__ wo, const u16* __restrict__ b2, const u16* __restrict__ x, u16* __restrict__ out, char* __restrict__ ws, LAS char* lds, const int w, const u64 tag) {
  const int l = lane_id();
  const int xcd = gi & 7, idx = gi >> 3, kh = xcd & 1, p = (xcd >> 1) * 4 + (idx & 3), nt = idx >> 2;
  const int partner = gi ^ 1;
  const int g = l >> 4, wm = w >> 1, wn = w & 1; const int stamp_base = 512; (void)stamp_base;
  const unsigned lds0 = (unsigned)(uintptr_t)lds;
  constexpr int SLOT = 16384;
#define AINC_ 131072
#define WINC_ 65536
  const __amdgpu_buffer_rsrc_t rA = mkrsrc(ws + WS_H, 144 * 128 * 1024);
  __amdgpu_buffer_rsrc_t rW = mkrsrc((const char*)w2 - 1024, F_ * D_ * 2 + 1024);
  const unsigned vlin = l * 16;
  const unsigned vw0 = wdma_off(l, w, 2048) + nt * 256 + w * 8 * 2048 + 1024, vw1 = vw0 + 128 - 1024;
  if (gi == 0 && w == 0) {
    for (unsigned spins = 0; spins < (1u << 16); ++spins) {
      bool ok = true;
#pragma unroll
      for (int i = 0; i < 8; ++i) ok &= __hip_atomic_load(flagp(ws, FL_RD + 64 * i + l), __ATOMIC_RELAXED, __HIP_MEMORY_SCOPE_AGENT) == tag;
      if (__all(ok)) break;
      __builtin_amdgcn_s_sleep(2);
    }
    if (l == 0) signal(ws, FL_E, tag);
  }
  if (w == 0) await(ws, l < 8 ? FL_H + p * 16 + 8 * kh + l : -1, tag);
  asm volatile("s_barrier" ::: "memory");
  STAMP(0);
  unsigned aso = (56 * kh * 128 + 8 * p + 2 * w) * 1024 - AINC_, wso = 56 * kh * 65536 + 3 * 65536;
  const unsigned ma = lds0 + w * 2048, mw = lds0 + 8192 + w * 2048;
#define AINC 131072
#define WINC 65536
#define PC2(SL, i) do { if constexpr ((i) == 0) dma1a_c<(SL) * SLOT, AINC>(ma, vlin, rA, aso); else if constexpr ((i) == 1) dma1b_c<1024>(vlin, rA, aso); \
    else if constexpr ((i) == 2) dma1a<(SL) * SLOT, WINC>(mw, vw0, rW, wso); else dma1b<1024>(vw1, rW, wso); } while (0)
#define STG2(SL) do { PC2(SL, 0); PC2(SL, 1); PC2(SL, 2); PC2(SL, 3); } while (0)
  PC2(0, 0); PC2(0, 1); PC2(1, 0); PC2(1, 1); PC2(2, 0); PC2(2, 1); PC2(3, 0); PC2(3, 1); STG2(4);
  f32x4 acc[4][4];
#pragma unroll
  for (int m = 0; m < 4; ++m)
#pragma unroll
    for (int n = 0; n < 4; ++n) acc[m][n] = (f32x4){0.f, 0.f, 0.f, 0.f};
  const LAS char* xrd = lds + 4 * wm * 1024 + l * 16;
  const LAS char* wrb[4];
#pragma unroll
  for (int i = 0; i < 4; ++i) wrb[i] = lds + 8192 + (g * 2 + wn) * 1024 + wrd_off(l, i);
  bf16x8 xa[4], wa[4], xb[4], wb[4];
#define RDX(X, m, so) X[m] = ldsfrag(xrd + (so) + (m) * 1024)
#define RDW(W, n, so) W[n] = ldstr(wrb[n] + (so), wrb[n] + (so) + 512)
#define M2(X, W, m, n) acc[m][n] = MFMA16(W[n], X[m], acc[m][n])
#define STEP2(X, W, XN, WN, RD, RS, DMA, DS) do { \
    M2(X, W, 0, 0); if (RD) RDW(WN, 0, (RS) * SLOT); SB(); M2(X, W, 1, 0); if (RD) RDX(XN, 0, (RS) * SLOT); SB(); M2(X, W, 2, 0); if (RD) RDX(XN, 1, (RS) * SLOT); SB(); M2(X, W, 3, 0); if (DMA) PC2(DS, 0); SB(); \
    M2(X, W, 0, 1); if (RD) RDW(WN, 1, (RS) * SLOT); SB(); M2(X, W, 1, 1); if (RD) RDX(XN, 2, (RS) * SLOT); SB(); M2(X, W, 2, 1); if (RD) RDX(XN, 3, (RS) * SLOT); SB(); M2(X, W, 3, 1); if (DMA) PC2(DS, 1); SB(); \
    M2(X, W, 0, 2); if (RD) RDW(WN, 2, (RS) * SLOT); SB(); M2(X, W, 1, 2); if (RD) RDW(WN, 3, (RS) * SLOT); SB(); M2(X, W, 2, 2); SB(); M2(X, W, 3, 2); if (DMA) PC2(DS, 2); SB(); \
    M2(X, W, 0, 3); SB(); M2(X, W, 1, 3); SB(); M2(X, W, 2, 3); SB(); M2(X, W, 3, 3); if (DMA) PC2(DS, 3); SB(); } while (0)
  WAIT_BAR(6);
#pragma unroll
  for (int i = 0; i < 4; ++i) { RDX(xa, i, 0); RDW(wa, i, 0); }
#define EV(SC, SN, W_, RD, DMA) do { WAIT_BAR(W_); SB(); STEP2(xa, wa, xb, wb, RD, SN, DMA, SC); } while (0)
#define OD(SC, SN, W_, RD, DMA) do { WAIT_BAR(W_); SB(); STEP2(xb, wb, xa, wa, RD, SN, DMA, SC); } while (0)
#define PR2(i) do { if constexpr ((i) == 0 || (((G2PAT >> (i)) & 1) != ((G2PAT >> ((i) - 1 < 0 ? 0 : (i) - 1)) & 1))) __builtin_amdgcn_s_setprio(((G2PAT >> (i)) & 1) ? 2 : 0); } while (0)
  u64 oflag = 0;
  for (int it = 0; it < 6; ++it) {
    STAMP(4 + it);
    PR2(0);
    EV(0, 1, 12, true, true);
    if (it == 5) {
      if (w == 0) {
        asm volatile("" : "+v"(oflag));
        if (!__all(l >= 8 || oflag == tag)) await(ws, l < 8 ? FL_O + (2 * p + (l >> 2)) * 8 + 4 * kh + (l & 3) : -1, tag);
      }
      rW = mkrsrc((const char*)wo - 1024, D_ * D_ * 2 + 1024);
      aso = ((112 + 16 * kh) * 128 + 8 * p + 2 * w) * 1024 - 131072; wso = 16 * kh * 65536 - 65536;
    }
    PR2(1); OD(1, 2, 12, true, true); PR2(2); EV(2, 3, 12, true, true); PR2(3); OD(3, 4, 12, true, true); PR2(4); EV(4, 0, 12, true, true);
    PR2(5); OD(0, 1, 12, true, true); PR2(6); EV(1, 2, 12, true, true);
    if (it == 4 && w == 0) {
      const GAS u64* fo = flagp(ws, FL_O + (2 * p + ((l >> 2) & 1)) * 8 + 4 * kh + (l & 3));
      asm volatile("global_load_dwordx2 %0, %1, off sc1" : "=&v"(oflag) : "v"(fo));
    }
    PR2(7); OD(2, 3, 12, true, true); PR2(8); EV(3, 4, 12, true, true); PR2(9); OD(4, 0, 12, true, true);
  }
  EV(0, 1, 12, true, true); OD(1, 2, 12, true, true); EV(2, 3, 12, true, true); OD(3, 4, 12, true, true); EV(4, 0, 12, true, true); OD(0, 1, 12, true, true); EV(1, 2, 12, true, true);
  u32x2 xres[2][4], bres[4];
  { const int le = lane_id();
    const u16* xp = x + (size_t)(128 * p + 64 * wm + 32 * kh + (le & 15)) * D_ + 128 * nt + 64 * wn + 4 * (le >> 4); const u16* xq = xp + 16 * D_; const u16* bp = b2 + 128 * nt + 64 * wn + 4 * (le >> 4);
    asm volatile("global_load_dwordx2 %0, %8, off\n\tglobal_load_dwordx2 %1, %8, off offset:32\n\tglobal_load_dwordx2 %2, %8, off offset:64\n\tglobal_load_dwordx2 %3, %8, off offset:96\n\t"
                 "global_load_dwordx2 %4, %9, off\n\tglobal_load_dwordx2 %5, %9, off offset:32\n\tglobal_load_dwordx2 %6, %9, off offset:64\n\tglobal_load_dwordx2 %7, %9, off offset:96"
                 : "=&v"(xres[0][0]), "=&v"(xres[0][1]), "=&v"(xres[0][2]), "=&v"(xres[0][3]), "=&v"(xres[1][0]), "=&v"(xres[1][1]), "=&v"(xres[1][2]), "=&v"(xres[1][3]) : "v"(xp), "v"(xq));
    asm volatile("global_load_dwordx2 %0, %4, off\n\tglobal_load_dwordx2 %1, %4, off offset:32\n\tglobal_load_dwordx2 %2, %4, off offset:64\n\tglobal_load_dwordx2 %3, %4, off offset:96"
                 : "=&v"(bres[0]), "=&v"(bres[1]), "=&v"(bres[2]), "=&v"(bres[3]) : "v"(bp)); }
  OD(2, 3, 24, true, false); EV(3, 4, 20, true, false); OD(4, 0, 16, true, false); EV(0, 1, 12, true, false); OD(1, 2, 12, false, false);
  STAMP(1);
  if (kh) {
#pragma unroll
    for (int n = 0; n < 4; ++n) { f32x4 t = acc[0][n]; acc[0][n] = acc[2][n]; acc[2][n] = t; t = acc[1][n]; acc[1][n] = acc[3][n]; acc[3][n] = t; }
#pragma unroll
    for (int n = 0; n < 4; ++n) { PIN(acc[0][n]); PIN(acc[1][n]); PIN(acc[2][n]); PIN(acc[3][n]); }
  }
  const int le = lane_id(), ge = le >> 4;
  const int token0 = 128 * p + 64 * wm + 32 * kh + (le & 15);
  {
    const __amdgpu_buffer_rsrc_t rP = mkrsrc(ws + WS_PART + (size_t)gi * 32768, 16384);
#pragma unroll
    for (int m = 0; m < 2; ++m)
#pragma unroll
      for (int n2 = 0; n2 < 2; ++n2)
        st_wt16(rP, ((w * 2 + m) * 2 + n2) * 1024 + le * 16, (u32x4){cvtpk(acc[2 + m][2 * n2][0], acc[2 + m][2 * n2][1]), cvtpk(acc[2 + m][2 * n2][2], acc[2 + m][2 * n2][3]),
                                                                       cvtpk(acc[2 + m][2 * n2 + 1][0], acc[2 + m][2 * n2 + 1][1]), cvtpk(acc[2 + m][2 * n2 + 1][2], acc[2 + m][2 * n2 + 1][3])});
    asm volatile("s_waitcnt vmcnt(0)\n\ts_barrier" : "+v"(xres[0][0]), "+v"(xres[0][1]), "+v"(xres[0][2]), "+v"(xres[0][3]), "+v"(xres[1][0]), "+v"(xres[1][1]), "+v"(xres[1][2]), "+v"(xres[1][3]),
                 "+v"(bres[0]), "+v"(bres[1]), "+v"(bres[2]), "+v"(bres[3]) :: "memory");
    if (w == 0) { if (le == 0) signal(ws, FL_X + gi, tag); await(ws, FL_X + partner, tag); }
    asm volatile("s_barrier" ::: "memory");
  }
  STAMP(2);
  const __amdgpu_buffer_rsrc_t rPP = mkrsrc(ws + WS_PART + (size_t)partner * 32768, 16384);
  u32x4 pv[2][2];
#pragma unroll
  for (int m = 0; m < 2; ++m)
#pragma unroll
    for (int n2 = 0; n2 < 2; ++n2) pv[m][n2] = __builtin_amdgcn_raw_buffer_load_b128(rPP, ((w * 2 + m) * 2 + n2) * 1024 + le * 16, 0, 16);
#pragma unroll
  for (int m = 0; m < 2; ++m) {
    unsigned d[4][2];
#pragma unroll
    for (int n = 0; n < 4; ++n) {
      const unsigned p0 = pv[m][n >> 1][2 * (n & 1)], p1 = pv[m][n >> 1][2 * (n & 1) + 1];
      const f32x4 a4 = acc[m][n];
      d[n][0] = cvtpk(a4[0] + lo2f(p0) + lo2f(bres[n][0]) + lo2f(xres[m][n][0]), a4[1] + hi2f(p0) + hi2f(bres[n][0]) + hi2f(xres[m][n][0]));
      d[n][1] = cvtpk(a4[2] + lo2f(p1) + lo2f(bres[n][1]) + lo2f(xres[m][n][1]), a4[3] + hi2f(p1) + hi2f(bres[n][1]) + hi2f(xres[m][n][1]));
    }
    const int cch = ((ge & 1) << 1) | (ge >> 1);
#pragma unroll
    for (int n2 = 0; n2 < 2; ++n2) {
      const u32x4 ch = chunk_of(d[2 * n2][0], d[2 * n2][1], d[2 * n2 + 1][0], d[2 * n2 + 1][1]);
      st_wt16(mkrsrc(out, S_ * D_ * 2), ((token0 + 16 * m) * D_ + 128 * nt + 64 * wn + 32 * n2 + 8 * cch) * 2, ch);
    }
  }
  STAMP(3);
#undef RDX
#undef RDW
}

constexpr int LDS_BYTES = 81920;
#define MODE 0
__global__ __launch_bounds__(256, 2) void k_main(const u16* __restrict__ x, const u16* __restrict__ wq, const u16* __restrict__ wk, const u16* __restrict__ wv, const u16* __restrict__ wo,
                                                  const u16* __restrict__ w1, const u16* __restrict__ b1, const u16* __restrict__ w2, const u16* __restrict__ b2, u16* __restrict__ out, char* __restrict__ ws) {
  extern __shared__ __attribute__((aligned(16))) char lds_[];
  LAS char* lds = (LAS char*)lds_;
  const int w = __builtin_amdgcn_readfirstlane(threadIdx.x >> 6);
  u64 eraw;
  { const GAS u64* ep = flagp(ws, FL_E); asm volatile("global_load_dwordx2 %0, %1, off sc1" : "=&v"(eraw) : "v"(ep)); }
  if ((blockIdx.x >> 8) == 0) {
    __builtin_amdgcn_s_setprio(PRIO_Q);
    const u64 tag = phase0(blockIdx.x, x, wk, wv, ws, lds, w, eraw);
    role_attn(blockIdx.x, wq, w2, wo, ws, lds, w, tag);
  } else {
    __builtin_amdgcn_s_setprio(PRIO_G1);
    const u64 tag = role_w1(blockIdx.x & 255, wq, w1, b1, w2, ws, lds, w, eraw);
    role_w2(blockIdx.x & 255, w2, wo, b2, x, out, ws, lds, w, tag);
  }
}

extern "C" void kernel_launch(void* const* d_in, const int* in_sizes, int n_in, void* d_out, int out_size, void* d_ws, size_t ws_size, hipStream_t stream) {
  const u16* x = (const u16*)d_in[0]; const u16* wq = (const u16*)d_in[1]; const u16* wk = (const u16*)d_in[2];
  const u16* wv = (const u16*)d_in[3]; const u16* wo = (const u16*)d_in[4];
  const u16* w1 = (const u16*)d_in[6]; const u16* b1 = (const u16*)d_in[7]; const u16* w2 = (const u16*)d_in[8]; const u16* b2 = (const u16*)d_in[9];
  (void)hipFuncSetAttribute((const void*)k_main, hipFuncAttributeMaxDynamicSharedMemorySize, LDS_BYTES);
  k_main<<<512, 256, LDS_BYTES, stream>>>(x, wq, wk, wv, wo, w1, b1, w2, b2, (u16*)d_out, (char*)d_ws);
}
```

```cpp
#include <hip/hip_runtime.h>
#include <stdint.h>

typedef unsigned short u16;
typedef __bf16 bf16x8 __attribute__((ext_vector_type(8)));
typedef __bf16 bf16x2 __attribute__((ext_vector_type(2)));
typedef short s16x8 __attribute__((ext_vector_type(8)));
typedef float f32x4 __attribute__((ext_vector_type(4)));
typedef float f32x2 __attribute__((ext_vector_type(2)));
typedef unsigned u32x4 __attribute__((ext_vector_type(4)));
typedef unsigned u32x2 __attribute__((ext_vector_type(2)));
#define LAS __attribute__((address_space(3)))
#define GAS __attribute__((address_space(1)))
#define DI __device__ __forceinline__
#define SB() __builtin_amdgcn_sched_barrier(0)
#define PIN(x) asm volatile("" : "+v"(x))
#define MFMA16(a, b, c) __builtin_amdgcn_mfma_f32_16x16x32_bf16((a), (b), (c), 0, 0, 0)
#define BF(x) __builtin_bit_cast(bf16x8, x)

#define PF0ROWS 1024
constexpr int S_ = 2048, D_ = 1024, F_ = 3584;
constexpr size_t WS_FLAGS = 0;
constexpr size_t WS_R2   = 131072;
constexpr size_t WS_KV   = WS_R2 + 8192;
constexpr size_t WS_XN   = WS_KV + 262144;
constexpr size_t WS_H    = WS_XN + 4194304;
constexpr size_t WS_O    = WS_H + (size_t)112 * 128 * 1024;
constexpr size_t WS_PART = WS_O + 4194304;
constexpr size_t WS_STAMP = WS_PART + (size_t)2 * 256 * 32768;

constexpr float QSCALE = 0.17677669529663687f * 1.4426950408889634f;

DI int lane_id() { int l; asm volatile("v_mbcnt_lo_u32_b32 %0, -1, 0\n\tv_mbcnt_hi_u32_b32 %0, -1, %0" : "=v"(l)); return l; }
DI float bf2f(u16 v) { return __uint_as_float(((unsigned)v) << 16); }
DI unsigned cvtpk(float lo, float hi) { f32x2 v = {lo, hi}; bf16x2 b = __builtin_convertvector(v, bf16x2); return __builtin_bit_cast(unsigned, b); }
DI float lo2f(unsigned u) { return __uint_as_float(u << 16); }
DI float hi2f(unsigned u) { return __uint_as_float(u & 0xffff0000u); }
DI __amdgpu_buffer_rsrc_t mkrsrc(const void* p, unsigned bytes) { return __builtin_amdgcn_make_buffer_rsrc((void*)p, 0, bytes, 0x00020000); }
DI void dma16(unsigned lds_addr, unsigned voff, __amdgpu_buffer_rsrc_t rsrc, unsigned soff) {
  asm volatile("s_mov_b32 m0, %0\n\ts_nop 0\n\tbuffer_load_dwordx4 %1, %2, %3 offen lds" :: "s"(lds_addr), "v"(voff), "s"(rsrc), "s"(soff));
}
template <int IMM, int INC> DI void dma2(unsigned mbase, unsigned v0, unsigned v1, __amdgpu_buffer_rsrc_t rsrc, unsigned& soff) {
  asm volatile("s_add_u32 m0, %3, %5\n\ts_add_u32 %0, %0, %6\n\tbuffer_load_dwordx4 %1, %4, %0 offen lds\n\tbuffer_load_dwordx4 %2, %4, %0 offen offset:1024 lds"
               : "+s"(soff) : "v"(v0), "v"(v1), "s"(mbase), "s"(rsrc), "i"(IMM), "i"(INC) : "scc");
}
template <int IMM, int INC> DI void dma1a(unsigned mbase, unsigned v0, __amdgpu_buffer_rsrc_t rsrc, unsigned& soff) {
  asm volatile("s_add_u32 m0, %2, %4\n\ts_add_u32 %0, %0, %5\n\tbuffer_load_dwordx4 %1, %3, %0 offen lds" : "+s"(soff) : "v"(v0), "s"(mbase), "s"(rsrc), "i"(IMM), "i"(INC) : "scc");
}
template <int OFF> DI void dma1b(unsigned v0, __amdgpu_buffer_rsrc_t rsrc, unsigned soff) {
  asm volatile("buffer_load_dwordx4 %0, %1, %2 offen offset:%3 lds" :: "v"(v0), "s"(rsrc), "s"(soff), "i"(OFF));
}
template <int IMM, int INC> DI void dma1a_c(unsigned mbase, unsigned v0, __amdgpu_buffer_rsrc_t rsrc, unsigned& soff) {
  asm volatile("s_add_u32 m0, %2, %4\n\ts_add_u32 %0, %0, %5\n\tbuffer_load_dwordx4 %1, %3, %0 offen sc1 lds" : "+s"(soff) : "v"(v0), "s"(mbase), "s"(rsrc), "i"(IMM), "i"(INC) : "scc");
}
template <int OFF> DI void dma1b_c(unsigned v0, __amdgpu_buffer_rsrc_t rsrc, unsigned soff) {
  asm volatile("buffer_load_dwordx4 %0, %1, %2 offen offset:%3 sc1 lds" :: "v"(v0), "s"(rsrc), "s"(soff), "i"(OFF));
}
DI void dma4(unsigned lds_addr, unsigned voff, __amdgpu_buffer_rsrc_t rsrc, unsigned soff) {
  asm volatile("s_mov_b32 m0, %0\n\ts_nop 0\n\tbuffer_load_dword %1, %2, %3 offen lds" :: "s"(lds_addr), "v"(voff), "s"(rsrc), "s"(soff));
}
DI void dma4_c(unsigned lds_addr, unsigned voff, __amdgpu_buffer_rsrc_t rsrc, unsigned soff) {
  asm volatile("s_mov_b32 m0, %0\n\ts_nop 0\n\tbuffer_load_dword %1, %2, %3 offen sc1 lds" :: "s"(lds_addr), "v"(voff), "s"(rsrc), "s"(soff));
}
template <int ADD> DI void dma4_ahead(unsigned lds_addr, unsigned voff, __amdgpu_buffer_rsrc_t rsrc, unsigned soff) {
  unsigned t;
  asm volatile("s_mov_b32 m0, %1\n\ts_add_u32 %0, %4, %5\n\tbuffer_load_dword %2, %3, %0 offen lds" : "=&s"(t) : "s"(lds_addr), "v"(voff), "s"(rsrc), "s"(soff), "i"(ADD) : "scc");
}
#define WAIT_BAR(N) asm volatile("s_waitcnt vmcnt(" #N ") lgkmcnt(0)\n\ts_barrier" ::: "memory")
DI bf16x8 ldsfrag(const LAS char* p) { return *(const LAS bf16x8*)p; }
typedef short v4i16_t __attribute__((ext_vector_type(4)));
DI bf16x8 ldstr(const LAS char* p0, const LAS char* p1) {
  v4i16_t a = __builtin_amdgcn_ds_read_tr16_b64_v4i16((LAS v4i16_t*)p0);
  v4i16_t b = __builtin_amdgcn_ds_read_tr16_b64_v4i16((LAS v4i16_t*)p1);
  s16x8 v = __builtin_shufflevector(a, b, 0, 1, 2, 3, 4, 5, 6, 7);
  return __builtin_bit_cast(bf16x8, v);
}
DI void st_wt16(__amdgpu_buffer_rsrc_t r, unsigned voff, u32x4 v) { __builtin_amdgcn_raw_buffer_store_b128(v, r, voff, 0, 16); }
DI u32x4 chunk_of(unsigned x0, unsigned x1, unsigned y0, unsigned y1) {
  auto s0 = __builtin_amdgcn_permlane16_swap(x0, y0, false, false);
  auto s1 = __builtin_amdgcn_permlane16_swap(x1, y1, false, false);
  return (u32x4){s0[0], s1[0], s0[1], s1[1]};
}
DI unsigned wdma_off(int l, int kblk, int ldbytes) {
  const int row = l >> 3, c8 = l & 7, sw = ((row >> 1) & 1) | ((kblk & 1) << 1);
  return row * ldbytes + (((c8 >> 1) ^ sw) * 32) + (c8 & 1) * 16;
}
DI unsigned wrd_off(int l, int seg) {
  const int g = l >> 4, q = (l & 15) >> 2, sw = ((q >> 1) & 1) | ((g & 1) << 1);
  return q * 128 + ((seg ^ sw) * 32) + (l & 3) * 8;
}
DI float xsum4(float v) {
  auto a = __builtin_amdgcn_permlane16_swap(__float_as_uint(v), __float_as_uint(v), false, false); v = __uint_as_float(a[0]) + __uint_as_float(a[1]);
  auto b = __builtin_amdgcn_permlane32_swap(__float_as_uint(v), __float_as_uint(v), false, false); return __uint_as_float(b[0]) + __uint_as_float(b[1]);
}
DI float xmax4(float v) {
  auto a = __builtin_amdgcn_permlane16_swap(__float_as_uint(v), __float_as_uint(v), false, false); v = fmaxf(__uint_as_float(a[0]), __uint_as_float(a[1]));
  auto b = __builtin_amdgcn_permlane32_swap(__float_as_uint(v), __float_as_uint(v), false, false); return fmaxf(__uint_as_float(b[0]), __uint_as_float(b[1]));
}
#define STAMP(i)

typedef unsigned long long u64;
constexpr int FL_E = 0, FL_RD = 16, FL_XN = 528, FL_KV = 784, FL_H = 1040, FL_O = 1296, FL_X = 1552;
DI GAS u64* flagp(char* ws, int i) { return (GAS u64*)(ws + WS_FLAGS + (size_t)i * 64); }
DI void signal(char* ws, int i, u64 tag) { __hip_atomic_store(flagp(ws, i), tag, __ATOMIC_RELAXED, __HIP_MEMORY_SCOPE_AGENT); }
DI u64 take_tag(char* ws, u64 eraw, int w) {
  asm volatile("" : "+v"(eraw));
  const u64 tag = (((u64)(unsigned)__builtin_amdgcn_readfirstlane((unsigned)(eraw >> 32))) << 32 | (unsigned)__builtin_amdgcn_readfirstlane((unsigned)eraw)) + 1;
  if (w == 0 && lane_id() == 0) signal(ws, FL_RD + blockIdx.x, tag);
  return tag;
}
DI void await(char* ws, int idx, u64 tag) {
  GAS u64* f = flagp(ws, idx < 0 ? FL_E : idx);
  for (unsigned spins = 0; spins < (1u << 20); ++spins) {
    const u64 v = __hip_atomic_load(f, __ATOMIC_RELAXED, __HIP_MEMORY_SCOPE_AGENT);
    if (__all(idx < 0 || v == tag)) break;
    __builtin_amdgcn_s_sleep(2);
  }
  asm volatile("" ::: "memory");
}

DI u64 phase0(int a, const u16* __restrict__ x, const u16* __restrict__ wk, const u16* __restrict__ wv, char* __restrict__ ws, LAS char* lds, const int w, const u64 eraw) {
  const int l = lane_id(), tid = w * 64 + l;
  LAS float* part = (LAS float*)(lds + 65536);
  LAS float* fin = (LAS float*)(lds + 65536 + 8192);
  LAS float* red = (LAS float*)(lds + 65536 + 8192 + 2048);
  const int rb = a & 127, hf = a >> 7;
  const int r = l & 15, c = l >> 4; const int stamp_base = 768; (void)stamp_base;
  STAMP(0);
  u32x4 xv[8];
  { const u16* xr = x + (size_t)(rb * 16 + r) * D_ + w * 256 + c * 8;
#define XL(i) asm volatile("global_load_dwordx4 %0, %1, off offset:" #i "*64" : "=&v"(xv[i]) : "v"(xr))
    XL(0); XL(1); XL(2); XL(3); XL(4); XL(5); XL(6); XL(7); }
  const unsigned lds0 = (unsigned)(uintptr_t)lds;
  {
    __amdgpu_buffer_rsrc_t rw = mkrsrc(hf ? wv : wk, 65536);
#pragma unroll
    for (int i = 0; i < 16; ++i) dma16(lds0 + w * 16384 + i * 1024, l * 16, rw, w * 16384 + i * 1024);
  }
  asm volatile("s_waitcnt vmcnt(16)" : "+v"(xv[0]), "+v"(xv[1]), "+v"(xv[2]), "+v"(xv[3]), "+v"(xv[4]), "+v"(xv[5]), "+v"(xv[6]), "+v"(xv[7]));
  STAMP(1);
  const u64 tag = take_tag(ws, eraw, w);
  f32x2 v[32];
#pragma unroll
  for (int i = 0; i < 8; ++i)
#pragma unroll
    for (int j = 0; j < 4; ++j) v[4 * i + j] = (f32x2){lo2f(xv[i][j]), hi2f(xv[i][j])};
  f32x2 s2 = (v[0] + v[1]) + (v[2] + v[3]);
#pragma unroll
  for (int i = 4; i < 32; i += 4) s2 += (v[i] + v[i + 1]) + (v[i + 2] + v[i + 3]);
  const float s = xsum4(s2[0] + s2[1]);
  if (c == 0) red[w * 16 + r] = s;
  asm volatile("s_waitcnt lgkmcnt(0)\n\ts_barrier" ::: "memory");
  const float mean = ((red[r] + red[16 + r]) + (red[32 + r] + red[48 + r])) * (1.0f / D_);
  f32x2 q2 = {0.f, 0.f}, q3 = {0.f, 0.f};
#pragma unroll
  for (int i = 0; i < 32; i += 2) { v[i] -= mean; v[i + 1] -= mean; q2 += v[i] * v[i]; q3 += v[i + 1] * v[i + 1]; }
  const float q = xsum4((q2[0] + q2[1]) + (q3[0] + q3[1]));
  if (c == 0) red[64 + w * 16 + r] = q;
  asm volatile("s_waitcnt lgkmcnt(0)\n\ts_barrier" ::: "memory");
  const float var = ((red[64 + r] + red[80 + r]) + (red[96 + r] + red[112 + r])) * (1.0f / D_);
  const float rstd = 1.0f / sqrtf(var + 1e-5f);
#pragma unroll
  for (int i = 0; i < 8; ++i)
#pragma unroll
    for (int j = 0; j < 4; ++j) { const f32x2 t = v[4 * i + j] * rstd; xv[i][j] = cvtpk(t[0], t[1]); }
  STAMP(4);
  const __amdgpu_buffer_rsrc_t rXN = mkrsrc(ws + WS_XN, 4194304);
#pragma unroll
  for (int i = 0; i < 4; ++i) st_wt16(rXN, ((8 * w + 2 * i + hf) * 128 + rb) * 1024 + l * 16, hf ? xv[2 * i + 1] : xv[2 * i]);
  if (hf == 0 && w == 0 && c == 0) __hip_atomic_store((GAS float*)(ws + WS_R2) + rb * 16 + r, 1.0f / sqrtf(var * rstd * rstd + 1e-5f), __ATOMIC_RELAXED, __HIP_MEMORY_SCOPE_AGENT);
  asm volatile("s_waitcnt vmcnt(0)\n\ts_barrier" ::: "memory");
  if (tid == 0) signal(ws, FL_XN + a, tag);
  STAMP(5);
  f32x4 acc[2] = {{0.f, 0.f, 0.f, 0.f}, {0.f, 0.f, 0.f, 0.f}};
  const LAS char* wb = (const LAS char*)lds + w * 16384 + (8 * c + ((l & 15) >> 2)) * 64 + (l & 3) * 8;
#pragma unroll
  for (int i = 0; i < 8; ++i)
#pragma unroll
    for (int b = 0; b < 2; ++b) {
      bf16x8 wf = ldstr(wb + i * 2048 + b * 32, wb + i * 2048 + b * 32 + 256);
      acc[b] = MFMA16(BF(xv[i]), wf, acc[b]);
    }
#pragma unroll
  for (int b = 0; b < 2; ++b)
#pragma unroll
    for (int j = 0; j < 4; ++j) part[(w * 16 + 4 * c + j) * 32 + 16 * b + r] = acc[b][j];
  asm volatile("s_waitcnt lgkmcnt(0)\n\ts_barrier" ::: "memory");
  STAMP(2);
#pragma unroll
  for (int u = 0; u < 2; ++u) { const int e = tid + 256 * u; fin[e] = (part[e] + part[512 + e]) + (part[1024 + e] + part[1536 + e]); }
  asm volatile("s_waitcnt lgkmcnt(0)\n\ts_barrier" ::: "memory");
  if (w == 0) {
    float o[8];
    unsigned dst = (rb >> 1) * 4096;
    const int half = rb & 1;
    if (hf == 0) { const int kb = l >> 5, g = (l >> 3) & 3, rr = l & 7;
      const int row = 8 * half + rr, key = 8 * (row >> 2) + 4 * kb + (row & 3) - 16 * half;
#pragma unroll
      for (int j = 0; j < 8; ++j) o[j] = fin[key * 32 + 16 * (j >> 2) + 4 * g + (j & 3)];
      dst += kb * 1024 + (g * 16 + row) * 16;
    } else { const int db = l >> 5, gl = (l >> 4) & 1, rr = l & 15;
#pragma unroll
      for (int j = 0; j < 8; ++j) o[j] = fin[(8 * gl + j) * 32 + 16 * db + rr];
      dst += 2048 + db * 1024 + ((2 * half + gl) * 16 + rr) * 16;
    }
    st_wt16(mkrsrc(ws + WS_KV, 262144), dst, (u32x4){cvtpk(o[0], o[1]), cvtpk(o[2], o[3]), cvtpk(o[4], o[5]), cvtpk(o[6], o[7])});
    asm volatile("s_waitcnt vmcnt(0)" ::: "memory");
    if (l == 0) signal(ws, FL_KV + a, tag);
  }
  STAMP(3);
  return tag;
}

#define MERGED 1
#define PRIO_A 1
#define G1PAT 0
#define G2PAT 0
#define PFSLEEP 30
#define GDELAY 0
#define PFROWS 256
#define PRIO_Q 1
#define PRIO_G1 0
#define PRIO_GE 2
#define PRIO_G2 0
#define LSUM_MFMA 1
DI float fadd_s(float a, float b) { float r; asm("s_nop 0\n\tv_add_f32_e32 %0, %1, %2" : "=v"(r) : "v"(a), "v"(b)); return r; }

DI void attn_slow(const char* __restrict__ kv, bf16x8 qf, f32x4& o0, f32x4& o1) {
  const int l = lane_id();
  const __amdgpu_buffer_rsrc_t rk = mkrsrc(kv, 262144);
  o0 = (f32x4){0.f, 0.f, 0.f, 0.f}; o1 = o0;
  float m = -3.0e38f, ls = 0.f;
#pragma unroll 1
  for (int t = 0; t < 64; ++t) {
    const int p = t * 4096 + l * 16;
    const bf16x8 k0 = BF(__builtin_amdgcn_raw_buffer_load_b128(rk, p, 0, 16)), k1 = BF(__builtin_amdgcn_raw_buffer_load_b128(rk, p + 1024, 0, 16)),
                 v0 = BF(__builtin_amdgcn_raw_buffer_load_b128(rk, p + 2048, 0, 16)), v1 = BF(__builtin_amdgcn_raw_buffer_load_b128(rk, p + 3072, 0, 16));
    const f32x4 z = {0.f, 0.f, 0.f, 0.f};
    f32x4 s0 = MFMA16(k0, qf, z), s1 = MFMA16(k1, qf, z);
    float mx = fmaxf(fmaxf(fmaxf(s0[0], s0[1]), fmaxf(s0[2], s0[3])), fmaxf(fmaxf(s1[0], s1[1]), fmaxf(s1[2], s1[3])));
    mx = fmaxf(mx, __shfl_xor(mx, 16)); mx = fmaxf(mx, __shfl_xor(mx, 32));
    const float mn = fmaxf(m, mx), al = __builtin_amdgcn_exp2f(m - mn);
    m = mn; float rs = 0.f;
#pragma unroll
    for (int i = 0; i < 4; ++i) { s0[i] = __builtin_amdgcn_exp2f(s0[i] - mn); s1[i] = __builtin_amdgcn_exp2f(s1[i] - mn); rs += s0[i] + s1[i]; o0[i] *= al; o1[i] *= al; }
    rs += __shfl_xor(rs, 16); rs += __shfl_xor(rs, 32); ls = ls * al + rs;
    const u32x4 pp = {cvtpk(s0[0], s0[1]), cvtpk(s0[2], s0[3]), cvtpk(s1[0], s1[1]), cvtpk(s1[2], s1[3])};
    o0 = MFMA16(v0, BF(pp), o0); o1 = MFMA16(v1, BF(pp), o1);
  }
#pragma unroll
  for (int i = 0; i < 4; ++i) { o0[i] /= ls; o1[i] /= ls; }
}

DI void role_attn(int a, const u16* __restrict__ wq, const u16* __restrict__ w2, const u16* __restrict__ wo, char* __restrict__ ws, LAS char* lds, const int w, const u64 tag) {
  const int l = lane_id();
#define AMAP 2
  constexpr int NTB = 32 / AMAP;
  const int xcd = a & 7, idx = a >> 3, tb = (xcd / (8 / AMAP)) * NTB + (idx % NTB), hg = (xcd % (8 / AMAP)) * AMAP + idx / NTB;
  const int g = l >> 4; const int stamp_base = 0; (void)stamp_base;
  const unsigned lds0 = (unsigned)(uintptr_t)lds;
  constexpr int SLOT = 24576;
  const __amdgpu_buffer_rsrc_t rX = mkrsrc(ws + WS_XN, 4194304), rKV = mkrsrc(ws + WS_KV, 262144);
  const unsigned vlin = l * 16;
  constexpr int QS = 12288;
  const __amdgpu_buffer_rsrc_t rWq = mkrsrc((const char*)wq - 1024, D_ * D_ * 2 + 1024);
  const unsigned vq0 = wdma_off(l, w, 2048) + hg * 256 + w * 8 * 2048 + 1024, vq1 = vq0 + 128 - 1024;
  unsigned xso = (4 * tb + w) * 1024 - 131072, qso = 0u - 65536;
  const unsigned mx = lds0 + w * 1024, mq = lds0 + 4096 + w * 2048;
#define PCQ(SL, i) do { if constexpr ((i) == 0) dma1a_c<(SL) * QS, 131072>(mx, vlin, rX, xso); else if constexpr ((i) == 1) dma1a<(SL) * QS, 65536>(mq, vq0, rWq, qso); else dma1b<1024>(vq1, rWq, qso); } while (0)
#define STGQ(SL) do { PCQ(SL, 0); PCQ(SL, 1); PCQ(SL, 2); } while (0)
  if (w == 0) await(ws, l < 8 ? FL_XN + 4 * tb + (l & 3) + 128 * (l >> 2) : -1, tag);
  asm volatile("s_waitcnt lgkmcnt(0)\n\ts_barrier" ::: "memory");
  STAMP(0);
  constexpr int NPL = 64 / (4 * NTB);
  const int pl = ((idx % NTB) * 4 + w) * NPL + l;
  const unsigned vpq = l < NPL ? (pl >> 1) * 2048 + hg * 256 + (pl & 1) * 128 + 1024 : 0x80000000u;
  const unsigned mpq = lds0 + 6 * QS;
  STGQ(0); STGQ(1); STGQ(2); STGQ(3); STGQ(4); STGQ(5);
  dma4_ahead<1 * 65536>(mpq, vpq, rWq, qso); dma4_ahead<2 * 65536>(mpq, vpq, rWq, qso); dma4_ahead<3 * 65536>(mpq, vpq, rWq, qso);
  dma4_ahead<4 * 65536>(mpq, vpq, rWq, qso); dma4_ahead<5 * 65536>(mpq, vpq, rWq, qso); dma4_ahead<6 * 65536>(mpq, vpq, rWq, qso);
  f32x4 qacc[2][4];
#pragma unroll
  for (int i = 0; i < 2; ++i)
#pragma unroll
    for (int j = 0; j < 4; ++j) qacc[i][j] = (f32x4){0.f, 0.f, 0.f, 0.f};
  const LAS char* xrd = lds + l * 16;
  const LAS char* wrd0 = lds + 4096 + (g * 2 + (w >> 1)) * 1024 + wrd_off(l, 2 * (w & 1));
  const LAS char* wrd1 = lds + 4096 + (g * 2 + (w >> 1)) * 1024 + wrd_off(l, 2 * (w & 1) + 1);
  bf16x8 qxa[4], qwa[2], qxb[4], qwb[2];
#define QRX(X, m, SL) X[m] = ldsfrag(xrd + (SL) * QS + (m) * 1024)
#define QRW(W, n, SL) W[n] = ((n) == 0 ? ldstr(wrd0 + (SL) * QS, wrd0 + (SL) * QS + 512) : ldstr(wrd1 + (SL) * QS, wrd1 + (SL) * QS + 512))
#define QM(X, W, m, n) qacc[n][m] = MFMA16(W[n], X[m], qacc[n][m])
#define STEPQ(X, W, XN, WN, RD, RS, DMA, DS) do { \
    QM(X, W, 0, 0); if (RD) { QRW(WN, 0, RS); QRX(XN, 0, RS); } SB(); QM(X, W, 1, 0); if (RD) { QRW(WN, 1, RS); QRX(XN, 1, RS); } SB(); QM(X, W, 2, 0); if (RD) QRX(XN, 2, RS); SB(); QM(X, W, 3, 0); if (RD) QRX(XN, 3, RS); SB(); \
    QM(X, W, 0, 1); if (DMA) PCQ(DS, 0); SB(); QM(X, W, 1, 1); if (DMA) PCQ(DS, 1); SB(); QM(X, W, 2, 1); if (DMA) PCQ(DS, 2); SB(); QM(X, W, 3, 1); if (DMA) dma4_ahead<6 * 65536>(mpq, vpq, rWq, qso); SB(); } while (0)
  WAIT_BAR(21);
#pragma unroll
  for (int m = 0; m < 4; ++m) QRX(qxa, m, 0);
  QRW(qwa, 0, 0); QRW(qwa, 1, 0);
#define QEV(SC, SN, W_, RD, DMA) do { WAIT_BAR(W_); SB(); STEPQ(qxa, qwa, qxb, qwb, RD, SN, DMA, SC); } while (0)
#define QOD(SC, SN, W_, RD, DMA) do { WAIT_BAR(W_); SB(); STEPQ(qxb, qwb, qxa, qwa, RD, SN, DMA, SC); } while (0)
  QEV(0, 1, 18, true, true); QOD(1, 2, 19, true, true); QEV(2, 3, 20, true, true); QOD(3, 4, 21, true, true); QEV(4, 5, 22, true, true); QOD(5, 0, 17, true, true);
  for (int it = 0; it < 3; ++it) { QEV(0, 1, 17, true, true); QOD(1, 2, 17, true, true); QEV(2, 3, 17, true, true); QOD(3, 4, 17, true, true); QEV(4, 5, 17, true, true); QOD(5, 0, 17, true, true); }
  QEV(0, 1, 17, true, true); QOD(1, 2, 17, true, true);
  QEV(2, 3, 17, true, false); QOD(3, 4, 13, true, false); QEV(4, 5, 9, true, false); QOD(5, 0, 5, true, false);
  if (w == 0) { await(ws, FL_KV + l, tag); await(ws, FL_KV + 64 + l, tag); await(ws, FL_KV + 128 + l, tag); await(ws, FL_KV + 192 + l, tag); }
  QEV(0, 1, 0, true, false);
  unsigned kvso = w * 4096 - 16384;
  const unsigned kvm = lds0 + w * 4096;
#define STGKV(SF) do { dma1a_c<(SF) * SLOT, 16384>(kvm, vlin, rKV, kvso); dma1b_c<1024>(vlin, rKV, kvso); dma1b_c<2048>(vlin, rKV, kvso); dma1b_c<3072>(vlin, rKV, kvso); } while (0)
  STGKV(1); STGKV(2);
  {
    const unsigned line = (a * 4 + w) * 64 + l;
    dma4(lds0 + 6 * QS, line * 128, mkrsrc(w2, F_ * D_ * 2), 0);
    dma4(lds0 + 6 * QS, line * 128, mkrsrc(wo, D_ * D_ * 2), 0);
  }
  STEPQ(qxb, qwb, qxa, qwa, false, 0, false, 0);
  STAMP(1);
  __builtin_amdgcn_s_setprio(PRIO_A);
  bf16x8 qf[4];
#pragma unroll
  for (int m = 0; m < 4; ++m)
    qf[m] = BF(((u32x4){cvtpk(qacc[0][m][0] * QSCALE, qacc[0][m][1] * QSCALE), cvtpk(qacc[0][m][2] * QSCALE, qacc[0][m][3] * QSCALE),
                        cvtpk(qacc[1][m][0] * QSCALE, qacc[1][m][1] * QSCALE), cvtpk(qacc[1][m][2] * QSCALE, qacc[1][m][3] * QSCALE)}));
  WAIT_BAR(6);
  const LAS char* kvb = lds + l * 16;
  f32x4 negm[4], S[4][2], O[4][2];
  bf16x8 k0, k1;
  {
    const LAS char* p = kvb + 1 * SLOT;
    k0 = ldsfrag(p); k1 = ldsfrag(p + 1024);
    const f32x4 z = {0.f, 0.f, 0.f, 0.f};
#pragma unroll
    for (int m = 0; m < 4; ++m) {
      const f32x4 s0 = MFMA16(k0, qf[m], z), s1 = MFMA16(k1, qf[m], z);
      float mx = fmaxf(fmaxf(fmaxf(s0[0], s0[1]), fmaxf(s0[2], s0[3])), fmaxf(fmaxf(s1[0], s1[1]), fmaxf(s1[2], s1[3])));
      mx = xmax4(mx);
      negm[m] = (f32x4){-mx, -mx, -mx, -mx}; PIN(negm[m]);
      O[m][0] = z; O[m][1] = z;
    }
    S[0][0] = MFMA16(k0, qf[0], negm[0]); S[0][1] = MFMA16(k1, qf[0], negm[0]);
    S[1][0] = MFMA16(k0, qf[1], negm[1]); S[1][1] = MFMA16(k1, qf[1], negm[1]);
  }
  u32x4 P[4];
  P[2] = (u32x4){0, 0, 0, 0}; P[3] = P[2];
  bf16x8 va0 = BF(P[2]), va1 = va0, vb0, vb1;
  S[3][1] = (f32x4){0.f, 0.f, 0.f, 0.f};
  f32x4 L[4];
#pragma unroll
  for (int m = 0; m < 4; ++m) L[m] = (f32x4){0.f, 0.f, 0.f, 0.f};
  bf16x8 ones; { const short o1 = (l & 15) == 0 ? (short)0x3f80 : (short)0; s16x8 ov = {o1, o1, o1, o1, o1, o1, o1, o1}; ones = __builtin_bit_cast(bf16x8, ov); PIN(ones); }
#define LS(m) do { L[m] = MFMA16(ones, BF(P[m]), L[m]); PIN(L[m]); SB(); } while (0)
#define LADD2(m, kb, i)
#define NOLS(m)
#define EXPF(x) __builtin_amdgcn_exp2f(x)
#define EX2(m, kb, i) do { float t0_ = EXPF(S[m][kb][i]), t1_ = EXPF(S[m][kb][i + 1]); PIN(t0_); PIN(t1_); S[m][kb][i] = t0_; S[m][kb][i + 1] = t1_; SB(); } while (0)
#define CVA(m, kb) do { unsigned c_ = cvtpk(S[m][kb][0], S[m][kb][1]); PIN(c_); P[m][2 * kb] = c_; LADD2(m, kb, 0); SB(); } while (0)
#define CVB(m, kb) do { unsigned c_ = cvtpk(S[m][kb][2], S[m][kb][3]); PIN(c_); P[m][2 * kb + 1] = c_; LADD2(m, kb, 2); SB(); } while (0)
#define GRP(mq, kq, KF, pm, pk, me, ke, mp, db, VF, LSM, X) do { \
    S[mq][kq] = MFMA16(KF, qf[mq], negm[mq]); PIN(S[mq][kq]); SB(); X; CVB(pm, pk); EX2(me, ke, 0); \
    O[mp][db] = MFMA16(VF, BF(P[mp]), O[mp][db]); PIN(O[mp][db]); SB(); EX2(me, ke, 2); LSM(mp); CVA(me, ke); } while (0)
#define TILE(KC0, KC1, KN0, KN1, VP0, VP1, VC0, VC1, kn, vp, D) do { \
    GRP(2, 0, KC0, 3, 1, 0, 0, 2, 0, VP0, NOLS, KN0 = ldsfrag(kn); SB()); \
    GRP(2, 1, KC1, 0, 0, 0, 1, 2, 1, VP1, LS,   KN1 = ldsfrag((kn) + 1024); SB()); \
    GRP(3, 0, KC0, 0, 1, 1, 0, 3, 0, VP0, NOLS, VC0 = ldsfrag(vp); SB()); \
    GRP(3, 1, KC1, 1, 0, 1, 1, 3, 1, VP1, LS,   VC1 = ldsfrag((vp) + 1024); SB()); \
    GRP(0, 0, KN0, 1, 1, 2, 0, 0, 0, VC0, NOLS, D(0)); \
    GRP(0, 1, KN1, 2, 0, 2, 1, 0, 1, VC1, LS,   D(1)); \
    GRP(1, 0, KN0, 2, 1, 3, 0, 1, 0, VC0, NOLS, D(2)); \
    GRP(1, 1, KN1, 3, 0, 3, 1, 1, 1, VC1, LS,   D(3)); \
  } while (0)
#define NOD(i)
  bf16x8 kx0, kx1;
#define STAGE(SC, SN, SF, DM, LAST) do { \
    TILE(k0, k1, kx0, kx1, va0, va1, vb0, vb1, kvb + (SC) * SLOT + 4096, kvb + (SC) * SLOT + 2048, NOD); \
    TILE(kx0, kx1, k0, k1, vb0, vb1, va0, va1, kvb + (SC) * SLOT + 8192, kvb + (SC) * SLOT + 4096 + 2048, NOD); \
    TILE(k0, k1, kx0, kx1, va0, va1, vb0, vb1, kvb + (SC) * SLOT + 12288, kvb + (SC) * SLOT + 8192 + 2048, NOD); \
    WAIT_BAR(0);        \
    TILE(kx0, kx1, k0, k1, vb0, vb1, va0, va1, kvb + ((LAST) ? (SC) : (SN)) * SLOT, kvb + (SC) * SLOT + 12288 + 2048, DM); } while (0)
#define DM_(SF, i) do { if constexpr ((i) == 0) { dma1a_c<(SF) * SLOT, 16384>(kvm, vlin, rKV, kvso); } else { dma1b_c<(i) * 1024>(vlin, rKV, kvso); } SB(); } while (0)
#define DM0(i) DM_(0, i)
#define DM1(i) DM_(1, i)
#define DM2(i) DM_(2, i)
  for (int j = 0; j < 12; j += 3) { STAGE(1, 2, 0, DM0, false); STAGE(2, 0, 1, DM1, false); STAGE(0, 1, 2, DM2, false); STAMP(4 + j / 3); }
  STAGE(1, 2, 0, DM0, false); STAGE(2, 0, 1, DM1, false); STAGE(0, 1, 2, NOD, false); STAGE(1, 2, 0, NOD, true);
  CVB(3, 1);
  O[2][0] = MFMA16(va0, BF(P[2]), O[2][0]); O[2][1] = MFMA16(va1, BF(P[2]), O[2][1]); LS(2);
  O[3][0] = MFMA16(va0, BF(P[3]), O[3][0]); O[3][1] = MFMA16(va1, BF(P[3]), O[3][1]); LS(3);
  STAMP(2);
  const __amdgpu_buffer_rsrc_t rO = mkrsrc(ws + WS_O, 4194304);
#pragma unroll
  for (int m = 0; m < 4; ++m) {
    float ls = __shfl(L[m][0], l & 15);
    float inv = 1.0f / ls;
    f32x4 o0 = O[m][0], o1 = O[m][1];
    const float mo = fmaxf(fmaxf(fmaxf(fabsf(o0[0]), fabsf(o0[1])), fmaxf(fabsf(o0[2]), fabsf(o0[3]))), fmaxf(fmaxf(fabsf(o1[0]), fabsf(o1[1])), fmaxf(fabsf(o1[2]), fabsf(o1[3]))));
    const bool ok = (ls <= 1.0e18f) && (mo <= 1.0e37f);
    if (__builtin_expect(__any(!ok), 0)) { attn_slow(ws + WS_KV, qf[m], o0, o1); inv = 1.0f; }
    const u32x4 ch = chunk_of(cvtpk(o0[0] * inv, o0[1] * inv), cvtpk(o0[2] * inv, o0[3] * inv), cvtpk(o1[0] * inv, o1[1] * inv), cvtpk(o1[2] * inv, o1[3] * inv));
    const int c = ((g & 1) << 1) | (g >> 1);
    st_wt16(rO, ((4 * hg + w) * 128 + 4 * tb + m) * 1024 + c * 256 + (l & 15) * 16, ch);
  }
  asm volatile("s_waitcnt vmcnt(0)\n\ts_barrier" ::: "memory");
  if (w == 0 && lane_id() == 0) signal(ws, FL_O + tb * 8 + hg, tag);
  STAMP(3);
}

DI int w2_kh(int gi) { return gi & 1; }
DI void w2_weights_early(int gi, const u16* __restrict__ w2, LAS char* lds, const int w) {
  const int l = lane_id(), nt = gi >> 5;
  const __amdgpu_buffer_rsrc_t rW = mkrsrc((const char*)w2 - 1024, F_ * D_ * 2 + 1024);
  const unsigned vw0 = wdma_off(l, w, 2048) + nt * 256 + w * 8 * 2048 + 1024, vw1 = vw0 + 128 - 1024;
  unsigned wso = 56 * w2_kh(gi) * 65536 - 65536;
  const unsigned mw = (unsigned)(uintptr_t)lds + 8192 + w * 2048;
  dma1a<0 * 16384, 65536>(mw, vw0, rW, wso); dma1b<1024>(vw1, rW, wso); dma1a<1 * 16384, 65536>(mw, vw0, rW, wso); dma1b<1024>(vw1, rW, wso);
  dma1a<2 * 16384, 65536>(mw, vw0, rW, wso); dma1b<1024>(vw1, rW, wso); dma1a<3 * 16384, 65536>(mw, vw0, rW, wso); dma1b<1024>(vw1, rW, wso);
}

DI f32x4 gelu4(f32x4 acc, float rs, f32x4 b, float c1v) {
  constexpr float c2 = -2.0f * 0.7978845608028654f * 1.4426950408889634f * 0.044715f;
  float x0 = acc[0], x1 = acc[1], x2 = acc[2], x3 = acc[3], t0, t1, t2, t3;
  asm("v_fma_f32 %0, %0, %8, %9\n\tv_fma_f32 %1, %1, %8, %10\n\tv_fma_f32 %2, %2, %8, %11\n\tv_fma_f32 %3, %3, %8, %12\n\t"
      "v_mul_f32 %4, %0, %0\n\tv_mul_f32 %5, %1, %1\n\tv_mul_f32 %6, %2, %2\n\tv_mul_f32 %7, %3, %3\n\t"
      "v_fma_f32 %4, %4, %14, %13\n\tv_fma_f32 %5, %5, %14, %13\n\tv_fma_f32 %6, %6, %14, %13\n\tv_fma_f32 %7, %7, %14, %13\n\t"
      "v_mul_f32 %4, %0, %4\n\tv_mul_f32 %5, %1, %5\n\tv_mul_f32 %6, %2, %6\n\tv_mul_f32 %7, %3, %7\n\t"
      "v_exp_f32 %4, %4\n\tv_exp_f32 %5, %5\n\tv_exp_f32 %6, %6\n\tv_exp_f32 %7, %7\n\t"
      "v_add_f32 %4, 1.0, %4\n\tv_add_f32 %5, 1.0, %5\n\tv_add_f32 %6, 1.0, %6\n\tv_add_f32 %7, 1.0, %7\n\t"
      "v_rcp_f32 %4, %4\n\tv_rcp_f32 %5, %5\n\tv_rcp_f32 %6, %6\n\tv_rcp_f32 %7, %7\n\t"
      "v_mul_f32 %0, %0, %4\n\tv_mul_f32 %1, %1, %5\n\tv_mul_f32 %2, %2, %6\n\tv_mul_f32 %3, %3, %7"
      : "+v"(x0), "+v"(x1), "+v"(x2), "+v"(x3), "=&v"(t0), "=&v"(t1), "=&v"(t2), "=&v"(t3)
      : "v"(rs), "v"(b[0]), "v"(b[1]), "v"(b[2]), "v"(b[3]), "v"(c1v), "s"(c2));
  return (f32x4){x0, x1, x2, x3};
}

DI u64 role_w1(int gi, const u16* __restrict__ wq, const u16* __restrict__ w1, const u16* __restrict__ b1, const u16* __restrict__ w2, char* __restrict__ ws, LAS char* lds, const int w, u64 eraw) {
  const int l = lane_id();
  const int xcd = gi & 7, idx = gi >> 3, p = (xcd >> 2) * 8 + (idx & 7), j = (xcd & 3) * 4 + (idx >> 3);
  const int g = l >> 4, wm = w >> 1, wn = w & 1; const int stamp_base = 256; (void)stamp_base;
  const unsigned lds0 = (unsigned)(uintptr_t)lds;
  constexpr int SLOT = 24576;
  const __amdgpu_buffer_rsrc_t rX = mkrsrc(ws + WS_XN, 4194304), rW = mkrsrc((const char*)w1 - 4096, D_ * F_ * 2 + 4096);
  const unsigned vlin = l * 16;
  unsigned vw[4];
#pragma unroll
  for (int i = 0; i < 4; ++i) vw[i] = wdma_off(l, w, F_ * 2) + 8 * w * (F_ * 2) + 448 * j - 64 * (j & 1) + 128 * i + 4096 - 1024 * i;
  STAMP(0);
  unsigned xso = (8 * p + 2 * w) * 1024 - 131072, wso = 0u - 32 * F_ * 2;
  const unsigned mx = lds0 + w * 2048, mw = lds0 + 8192 + w * 4096;
#define PC1(SL, i) do { if constexpr ((i) == 0) dma1a_c<(SL) * SLOT, 131072>(mx, vlin, rX, xso); else if constexpr ((i) == 1) dma1b_c<1024>(vlin, rX, xso); \
    else if constexpr ((i) == 2) dma1a<(SL) * SLOT, 32 * F_ * 2>(mw, vw[0], rW, wso); else dma1b<((i) - 2) * 1024>(vw[(i) - 2], rW, wso); } while (0)
#define STG1(SL) do { PC1(SL, 0); PC1(SL, 1); PC1(SL, 2); PC1(SL, 3); PC1(SL, 4); PC1(SL, 5); } while (0)
  PC1(0, 2); PC1(0, 3); PC1(0, 4); PC1(0, 5); PC1(1, 2); PC1(1, 3); PC1(1, 4); PC1(1, 5); PC1(2, 2); PC1(2, 3); PC1(2, 4); PC1(2, 5);
  constexpr int L_R2 = 3 * SLOT, L_B1 = L_R2 + 512;
  const unsigned vpf = l < 4 ? (4 * (idx & 7) + w) * (F_ * 2) + 448 * j - 64 * (j & 1) + l * 128 + 4096 : 0x80000000u;
  const unsigned mpf = lds0 + L_B1 + 1024;
  dma4_ahead<1 * 32 * F_ * 2>(mpf, vpf, rW, wso); dma4_ahead<2 * 32 * F_ * 2>(mpf, vpf, rW, wso); dma4_ahead<3 * 32 * F_ * 2>(mpf, vpf, rW, wso);
  dma4_ahead<4 * 32 * F_ * 2>(mpf, vpf, rW, wso); dma4_ahead<5 * 32 * F_ * 2>(mpf, vpf, rW, wso);
  if (w != 0 && PFSLEEP > 0) __builtin_amdgcn_s_sleep(PFSLEEP);
  if (w == 1) dma4(mpf, (gi * 64 + l) * 128, mkrsrc(wq, D_ * D_ * 2), 0);
  if (w >= 2 && PFROWS > 0) { constexpr int NL = PFROWS * 56 / 512;
#pragma unroll
    for (int i = 0; i < (NL + 63) / 64; ++i) dma4(mpf, i * 64 + l < NL ? 256 * F_ * 2 + ((gi * 2 + (w - 2)) * NL + i * 64 + l) * 128 : 0x80000000u, mkrsrc(w1, D_ * F_ * 2), 0);
  }
  if (w >= 2) dma4(lds0 + L_B1 + (w - 2) * 256, 448 * j + ((w - 2) * 64 + l) * 4, mkrsrc(b1, F_ * 2), 0);
  asm volatile("s_waitcnt vmcnt(17)" : "+v"(eraw));
  const u64 tag = take_tag(ws, eraw, w);
  if (w == 0) await(ws, l < 16 ? FL_XN + 8 * p + (l & 7) + 128 * (l >> 3) : -1, tag);
  asm volatile("s_barrier" ::: "memory");
  STAMP(8);
  if (w < 2) dma4_c(lds0 + L_R2 + w * 256, (128 * p + 64 * w + l) * 4, mkrsrc(ws + WS_R2, 8192), 0);
  PC1(0, 0); PC1(0, 1); PC1(1, 0); PC1(1, 1); PC1(2, 0); PC1(2, 1);
  dma4(mpf, 0x80000000u, rW, 0);
  f32x4 acc[4][7];
#pragma unroll
  for (int m = 0; m < 4; ++m)
#pragma unroll
    for (int n = 0; n < 7; ++n) acc[m][n] = (f32x4){0.f, 0.f, 0.f, 0.f};
  const LAS char* xrd = lds + 4 * wm * 1024 + l * 16;
  const LAS char* wrb[5];
  { const int off = 6 * wn + 2 * (j & 1), nl = 12 + wn + 2 * (j & 1);
#pragma unroll
    for (int i = 0; i < 4; ++i) wrb[i] = lds + 8192 + (g * 4 + ((i + off) >> 2)) * 1024 + wrd_off(l, (i + off) & 3);
    wrb[4] = lds + 8192 + (g * 4 + (nl >> 2)) * 1024 + wrd_off(l, nl & 3); }
  bf16x8 xa[4], wa[7], xb[4], wb[7];
#define RDX(X, m, so) X[m] = ldsfrag(xrd + (so) + (m) * 1024)
#define RDW(W, n, so) W[n] = ((n) < 6 ? ldstr(wrb[(n) & 3] + (so) + ((n) >> 2) * 1024, wrb[(n) & 3] + (so) + ((n) >> 2) * 1024 + 512) : ldstr(wrb[4] + (so), wrb[4] + (so) + 512))
#define M1(X, W, m, n) acc[m][n] = MFMA16(W[n], X[m], acc[m][n])
#define STEP1(X, W, XN, WN, RD, RS, DMA, DS) do { \
    M1(X, W, 0, 0); if (RD) RDW(WN, 0, (RS) * SLOT); SB(); M1(X, W, 1, 0); if (RD) RDX(XN, 0, (RS) * SLOT); SB(); M1(X, W, 2, 0); if (RD) RDW(WN, 1, (RS) * SLOT); SB(); M1(X, W, 3, 0); if (DMA) PC1(DS, 0); SB(); \
    M1(X, W, 0, 1); if (RD) RDX(XN, 1, (RS) * SLOT); SB(); M1(X, W, 1, 1); if (RD) RDW(WN, 2, (RS) * SLOT); SB(); M1(X, W, 2, 1); if (RD) RDX(XN, 2, (RS) * SLOT); SB(); M1(X, W, 3, 1); if (DMA) PC1(DS, 1); SB(); \
    M1(X, W, 0, 2); if (RD) RDW(WN, 3, (RS) * SLOT); SB(); M1(X, W, 1, 2); if (RD) RDX(XN, 3, (RS) * SLOT); SB(); M1(X, W, 2, 2); if (RD) RDW(WN, 4, (RS) * SLOT); SB(); M1(X, W, 3, 2); if (DMA) PC1(DS, 2); SB(); \
    M1(X, W, 0, 3); if (RD) RDW(WN, 5, (RS) * SLOT); SB(); M1(X, W, 1, 3); SB(); M1(X, W, 2, 3); if (RD) RDW(WN, 6, (RS) * SLOT); SB(); M1(X, W, 3, 3); if (DMA) PC1(DS, 3); SB(); \
    M1(X, W, 0, 4); SB(); M1(X, W, 1, 4); SB(); M1(X, W, 2, 4); SB(); M1(X, W, 3, 4); if (DMA) PC1(DS, 4); SB(); \
    M1(X, W, 0, 5); SB(); M1(X, W, 1, 5); SB(); M1(X, W, 2, 5); SB(); M1(X, W, 3, 5); if (DMA) { PC1(DS, 5); dma4_ahead<5 * 32 * F_ * 2>(mpf, vpf, rW, wso); } SB(); \
    M1(X, W, 0, 6); SB(); M1(X, W, 1, 6); SB(); M1(X, W, 2, 6); SB(); M1(X, W, 3, 6); SB(); } while (0)
  WAIT_BAR(3);
#pragma unroll
  for (int m = 0; m < 4; ++m) RDX(xa, m, 0);
#pragma unroll
  for (int n = 0; n < 7; ++n) RDW(wa, n, 0);
#define EV(SC, SN, W_, RD, DMA) do { WAIT_BAR(W_); SB(); STEP1(xa, wa, xb, wb, RD, SN, DMA, SC); } while (0)
#define OD(SC, SN, W_, RD, DMA) do { WAIT_BAR(W_); SB(); STEP1(xb, wb, xa, wa, RD, SN, DMA, SC); } while (0)
#define PR1(i) do { if constexpr ((i) == 0 || (((G1PAT >> (i)) & 1) != ((G1PAT >> ((i) - 1 < 0 ? 0 : (i) - 1)) & 1))) __builtin_amdgcn_s_setprio(((G1PAT >> (i)) & 1) ? 2 : 0); } while (0)
#define SIX() do { PR1(0); EV(0, 1, 8, true, true); PR1(1); OD(1, 2, 8, true, true); PR1(2); EV(2, 0, 8, true, true); PR1(3); OD(0, 1, 8, true, true); PR1(4); EV(1, 2, 8, true, true); PR1(5); OD(2, 0, 8, true, true); } while (0)
  for (int it = 0; it < 4; ++it) { SIX(); STAMP(4 + it); }
  EV(0, 1, 8, true, true); OD(1, 2, 8, true, true); EV(2, 0, 8, true, true); OD(0, 1, 8, true, true); EV(1, 2, 8, true, true);
  OD(2, 0, 8, true, false); EV(0, 1, 0, true, false); OD(1, 2, 0, false, false);
  STAMP(1);
  __builtin_amdgcn_s_setprio(PRIO_GE);
  w2_weights_early(gi, w2, lds, w);
  const int le = lane_id(), ge = le >> 4;
  float r2[4];
#pragma unroll
  for (int m = 0; m < 4; ++m) r2[m] = *(const LAS float*)(lds + L_R2 + (64 * wm + 16 * m + (le & 15)) * 4);
  const __amdgpu_buffer_rsrc_t rH = mkrsrc(ws + WS_H, 112 * 128 * 1024);
  const int cch = ((ge & 1) << 1) | (ge >> 1);
  unsigned last[4][2];
  float c1v = -2.0f * 0.7978845608028654f * 1.4426950408889634f; PIN(c1v);
#pragma unroll
  for (int n = 0; n < 7; ++n) {
    const int nb = n < 6 ? 6 * wn + n : 12 + wn;
    const u32x2 braw = *(const LAS u32x2*)(lds + L_B1 + (16 * nb + 4 * ge) * 2);
    const f32x4 bb = {lo2f(braw[0]), hi2f(braw[0]), lo2f(braw[1]), hi2f(braw[1])};
#pragma unroll
    for (int m = 0; m < 4; ++m) acc[m][n] = gelu4(acc[m][n], r2[m], bb, c1v);
    if (n < 6) { if (n & 1) {
#pragma unroll
        for (int m = 0; m < 4; ++m) {
          const u32x4 ch = chunk_of(cvtpk(acc[m][n - 1][0], acc[m][n - 1][1]), cvtpk(acc[m][n - 1][2], acc[m][n - 1][3]), cvtpk(acc[m][n][0], acc[m][n][1]), cvtpk(acc[m][n][2], acc[m][n][3]));
          st_wt16(rH, ((7 * j + 3 * wn + (n >> 1)) * 128 + 8 * p + 4 * wm + m) * 1024 + cch * 256 + (le & 15) * 16, ch);
        } }
    } else {
#pragma unroll
      for (int m = 0; m < 4; ++m) { last[m][0] = cvtpk(acc[m][n][0], acc[m][n][1]); last[m][1] = cvtpk(acc[m][n][2], acc[m][n][3]); }
    }
  }
#pragma unroll
  for (int m = 0; m < 4; m += 2) {
    const u32x4 ch = chunk_of(last[m][0], last[m][1], last[m + 1][0], last[m + 1][1]);
    st_wt16(rH, ((7 * j + 6) * 128 + 8 * p + 4 * wm + m + (ge & 1)) * 1024 + (2 * wn + (ge >> 1)) * 256 + (le & 15) * 16, ch);
  }
  STAMP(2);
  __builtin_amdgcn_s_setprio(PRIO_G2);
  asm volatile("s_waitcnt vmcnt(0)\n\ts_barrier" ::: "memory");
  if (w == 0 && lane_id() == 0) signal(ws, FL_H + p * 16 + j, tag);
  STAMP(3);
#undef RDX
#undef RDW
#undef EV
#undef OD
  return tag;
}

DI void role_w2(int gi, const u16* __restrict__ w2, const u16* __restrict__ wo, const u16* __restrict__ b2, const u16* __restrict__ x, u16* __restrict__ out, char* __restrict__ ws, LAS char* lds, const int w, const u64 tag) {
  const int l = lane_id();
  const int xcd = gi & 7, idx = gi >> 3, kh = xcd & 1, p = (xcd >> 1) * 4 + (idx & 3), nt = idx >> 2;
  const int partner = gi ^ 1;
  const int g = l >> 4, wm = w >> 1, wn = w & 1; const int stamp_base = 512; (void)stamp_base;
  const unsigned lds0 = (unsigned)(uintptr_t)lds;
  constexpr int SLOT = 16384;
#define AINC_ 131072
#define WINC_ 65536
  const __amdgpu_buffer_rsrc_t rA = mkrsrc(ws + WS_H, 144 * 128 * 1024);
  __amdgpu_buffer_rsrc_t rW = mkrsrc((const char*)w2 - 1024, F_ * D_ * 2 + 1024);
  const unsigned vlin = l * 16;
  const unsigned vw0 = wdma_off(l, w, 2048) + nt * 256 + w * 8 * 2048 + 1024, vw1 = vw0 + 128 - 1024;
  if (gi == 0 && w == 0) {
    for (unsigned spins = 0; spins < (1u << 16); ++spins) {
      bool ok = true;
#pragma unroll
      for (int i = 0; i < 8; ++i) ok &= __hip_atomic_load(flagp(ws, FL_RD + 64 * i + l), __ATOMIC_RELAXED, __HIP_MEMORY_SCOPE_AGENT) == tag;
      if (__all(ok)) break;
      __builtin_amdgcn_s_sleep(2);
    }
    if (l == 0) signal(ws, FL_E, tag);
  }
  if (w == 0) await(ws, l < 8 ? FL_H + p * 16 + 8 * kh + l : -1, tag);
  asm volatile("s_barrier" ::: "memory");
  STAMP(0);
  unsigned aso = (56 * kh * 128 + 8 * p + 2 * w) * 1024 - AINC_, wso = 56 * kh * 65536 + 3 * 65536;
  const unsigned ma = lds0 + w * 2048, mw = lds0 + 8192 + w * 2048;
#define AINC 131072
#define WINC 65536
#define PC2(SL, i) do { if constexpr ((i) == 0) dma1a_c<(SL) * SLOT, AINC>(ma, vlin, rA, aso); else if constexpr ((i) == 1) dma1b_c<1024>(vlin, rA, aso); \
    else if constexpr ((i) == 2) dma1a<(SL) * SLOT, WINC>(mw, vw0, rW, wso); else dma1b<1024>(vw1, rW, wso); } while (0)
#define STG2(SL) do { PC2(SL, 0); PC2(SL, 1); PC2(SL, 2); PC2(SL, 3); } while (0)
  PC2(0, 0); PC2(0, 1); PC2(1, 0); PC2(1, 1); PC2(2, 0); PC2(2, 1); PC2(3, 0); PC2(3, 1); STG2(4);
  f32x4 acc[4][4];
#pragma unroll
  for (int m = 0; m < 4; ++m)
#pragma unroll
    for (int n = 0; n < 4; ++n) acc[m][n] = (f32x4){0.f, 0.f, 0.f, 0.f};
  const LAS char* xrd = lds + 4 * wm * 1024 + l * 16;
  const LAS char* wrb[4];
#pragma unroll
  for (int i = 0; i < 4; ++i) wrb[i] = lds + 8192 + (g * 2 + wn) * 1024 + wrd_off(l, i);
  bf16x8 xa[4], wa[4], xb[4], wb[4];
#define RDX(X, m, so) X[m] = ldsfrag(xrd + (so) + (m) * 1024)
#define RDW(W, n, so) W[n] = ldstr(wrb[n] + (so), wrb[n] + (so) + 512)
#define M2(X, W, m, n) acc[m][n] = MFMA16(W[n], X[m], acc[m][n])
#define STEP2(X, W, XN, WN, RD, RS, DMA, DS) do { \
    M2(X, W, 0, 0); if (RD) RDW(WN, 0, (RS) * SLOT); SB(); M2(X, W, 1, 0); if (RD) RDX(XN, 0, (RS) * SLOT); SB(); M2(X, W, 2, 0); if (RD) RDX(XN, 1, (RS) * SLOT); SB(); M2(X, W, 3, 0); if (DMA) PC2(DS, 0); SB(); \
    M2(X, W, 0, 1); if (RD) RDW(WN, 1, (RS) * SLOT); SB(); M2(X, W, 1, 1); if (RD) RDX(XN, 2, (RS) * SLOT); SB(); M2(X, W, 2, 1); if (RD) RDX(XN, 3, (RS) * SLOT); SB(); M2(X, W, 3, 1); if (DMA) PC2(DS, 1); SB(); \
    M2(X, W, 0, 2); if (RD) RDW(WN, 2, (RS) * SLOT); SB(); M2(X, W, 1, 2); if (RD) RDW(WN, 3, (RS) * SLOT); SB(); M2(X, W, 2, 2); SB(); M2(X, W, 3, 2); if (DMA) PC2(DS, 2); SB(); \
    M2(X, W, 0, 3); SB(); M2(X, W, 1, 3); SB(); M2(X, W, 2, 3); SB(); M2(X, W, 3, 3); if (DMA) PC2(DS, 3); SB(); } while (0)
  WAIT_BAR(6);
#pragma unroll
  for (int i = 0; i < 4; ++i) { RDX(xa, i, 0); RDW(wa, i, 0); }
#define EV(SC, SN, W_, RD, DMA) do { WAIT_BAR(W_); SB(); STEP2(xa, wa, xb, wb, RD, SN, DMA, SC); } while (0)
#define OD(SC, SN, W_, RD, DMA) do { WAIT_BAR(W_); SB(); STEP2(xb, wb, xa, wa, RD, SN, DMA, SC); } while (0)
#define PR2(i) do { if constexpr ((i) == 0 || (((G2PAT >> (i)) & 1) != ((G2PAT >> ((i) - 1 < 0 ? 0 : (i) - 1)) & 1))) __builtin_amdgcn_s_setprio(((G2PAT >> (i)) & 1) ? 2 : 0); } while (0)
  u64 oflag = 0;
  for (int it = 0; it < 6; ++it) {
    STAMP(4 + it);
    PR2(0);
    EV(0, 1, 12, true, true);
    if (it == 5) {
      if (w == 0) {
        asm volatile("" : "+v"(oflag));
        if (!__all(l >= 8 || oflag == tag)) await(ws, l < 8 ? FL_O + (2 * p + (l >> 2)) * 8 + 4 * kh + (l & 3) : -1, tag);
      }
      rW = mkrsrc((const char*)wo - 1024, D_ * D_ * 2 + 1024);
      aso = ((112 + 16 * kh) * 128 + 8 * p + 2 * w) * 1024 - 131072; wso = 16 * kh * 65536 - 65536;
    }
    PR2(1); OD(1, 2, 12, true, true); PR2(2); EV(2, 3, 12, true, true); PR2(3); OD(3, 4, 12, true, true); PR2(4); EV(4, 0, 12, true, true);
    PR2(5); OD(0, 1, 12, true, true); PR2(6); EV(1, 2, 12, true, true);
    if (it == 4 && w == 0) {
      const GAS u64* fo = flagp(ws, FL_O + (2 * p + ((l >> 2) & 1)) * 8 + 4 * kh + (l & 3));
      asm volatile("global_load_dwordx2 %0, %1, off sc1" : "=&v"(oflag) : "v"(fo));
    }
    PR2(7); OD(2, 3, 12, true, true); PR2(8); EV(3, 4, 12, true, true); PR2(9); OD(4, 0, 12, true, true);
  }
  EV(0, 1, 12, true, true); OD(1, 2, 12, true, true); EV(2, 3, 12, true, true); OD(3, 4, 12, true, true); EV(4, 0, 12, true, true); OD(0, 1, 12, true, true); EV(1, 2, 12, true, true);
  u32x2 xres[2][4], bres[4];
  { const int le = lane_id();
    const u16* xp = x + (size_t)(128 * p + 64 * wm + 32 * kh + (le & 15)) * D_ + 128 * nt + 64 * wn + 4 * (le >> 4); const u16* xq = xp + 16 * D_; const u16* bp = b2 + 128 * nt + 64 * wn + 4 * (le >> 4);
    asm volatile("global_load_dwordx2 %0, %8, off\n\tglobal_load_dwordx2 %1, %8, off offset:32\n\tglobal_load_dwordx2 %2, %8, off offset:64\n\tglobal_load_dwordx2 %3, %8, off offset:96\n\t"
                 "global_load_dwordx2 %4, %9, off\n\tglobal_load_dwordx2 %5, %9, off offset:32\n\tglobal_load_dwordx2 %6, %9, off offset:64\n\tglobal_load_dwordx2 %7, %9, off offset:96"
                 : "=&v"(xres[0][0]), "=&v"(xres[0][1]), "=&v"(xres[0][2]), "=&v"(xres[0][3]), "=&v"(xres[1][0]), "=&v"(xres[1][1]), "=&v"(xres[1][2]), "=&v"(xres[1][3]) : "v"(xp), "v"(xq));
    asm volatile("global_load_dwordx2 %0, %4, off\n\tglobal_load_dwordx2 %1, %4, off offset:32\n\tglobal_load_dwordx2 %2, %4, off offset:64\n\tglobal_load_dwordx2 %3, %4, off offset:96"
                 : "=&v"(bres[0]), "=&v"(bres[1]), "=&v"(bres[2]), "=&v"(bres[3]) : "v"(bp)); }
  OD(2, 3, 24, true, false); EV(3, 4, 20, true, false); OD(4, 0, 16, true, false); EV(0, 1, 12, true, false); OD(1, 2, 12, false, false);
  STAMP(1);
  if (kh) {
#pragma unroll
    for (int n = 0; n < 4; ++n) { f32x4 t = acc[0][n]; acc[0][n] = acc[2][n]; acc[2][n] = t; t = acc[1][n]; acc[1][n] = acc[3][n]; acc[3][n] = t; }
#pragma unroll
    for (int n = 0; n < 4; ++n) { PIN(acc[0][n]); PIN(acc[1][n]); PIN(acc[2][n]); PIN(acc[3][n]); }
  }
  const int le = lane_id(), ge = le >> 4;
  const int token0 = 128 * p + 64 * wm + 32 * kh + (le & 15);
  {
    const __amdgpu_buffer_rsrc_t rP = mkrsrc(ws + WS_PART + (size_t)gi * 32768, 16384);
#pragma unroll
    for (int m = 0; m < 2; ++m)
#pragma unroll
      for (int n2 = 0; n2 < 2; ++n2)
        st_wt16(rP, ((w * 2 + m) * 2 + n2) * 1024 + le * 16, (u32x4){cvtpk(acc[2 + m][2 * n2][0], acc[2 + m][2 * n2][1]), cvtpk(acc[2 + m][2 * n2][2], acc[2 + m][2 * n2][3]),
                                                                       cvtpk(acc[2 + m][2 * n2 + 1][0], acc[2 + m][2 * n2 + 1][1]), cvtpk(acc[2 + m][2 * n2 + 1][2], acc[2 + m][2 * n2 + 1][3])});
    asm volatile("s_waitcnt vmcnt(0)\n\ts_barrier" : "+v"(xres[0][0]), "+v"(xres[0][1]), "+v"(xres[0][2]), "+v"(xres[0][3]), "+v"(xres[1][0]), "+v"(xres[1][1]), "+v"(xres[1][2]), "+v"(xres[1][3]),
                 "+v"(bres[0]), "+v"(bres[1]), "+v"(bres[2]), "+v"(bres[3]) :: "memory");
    if (w == 0) { if (le == 0) signal(ws, FL_X + gi, tag); await(ws, FL_X + partner, tag); }
    asm volatile("s_barrier" ::: "memory");
  }
  STAMP(2);
  const __amdgpu_buffer_rsrc_t rPP = mkrsrc(ws + WS_PART + (size_t)partner * 32768, 16384);
  u32x4 pv[2][2];
#pragma unroll
  for (int m = 0; m < 2; ++m)
#pragma unroll
    for (int n2 = 0; n2 < 2; ++n2) pv[m][n2] = __builtin_amdgcn_raw_buffer_load_b128(rPP, ((w * 2 + m) * 2 + n2) * 1024 + le * 16, 0, 16);
#pragma unroll
  for (int m = 0; m < 2; ++m) {
    unsigned d[4][2];
#pragma unroll
    for (int n = 0; n < 4; ++n) {
      const unsigned p0 = pv[m][n >> 1][2 * (n & 1)], p1 = pv[m][n >> 1][2 * (n & 1) + 1];
      const f32x4 a4 = acc[m][n];
      d[n][0] = cvtpk(a4[0] + lo2f(p0) + lo2f(bres[n][0]) + lo2f(xres[m][n][0]), a4[1] + hi2f(p0) + hi2f(bres[n][0]) + hi2f(xres[m][n][0]));
      d[n][1] = cvtpk(a4[2] + lo2f(p1) + lo2f(bres[n][1]) + lo2f(xres[m][n][1]), a4[3] + hi2f(p1) + hi2f(bres[n][1]) + hi2f(xres[m][n][1]));
    }
    const int cch = ((ge & 1) << 1) | (ge >> 1);
#pragma unroll
    for (int n2 = 0; n2 < 2; ++n2) {
      const u32x4 ch = chunk_of(d[2 * n2][0], d[2 * n2][1], d[2 * n2 + 1][0], d[2 * n2 + 1][1]);
      st_wt16(mkrsrc(out, S_ * D_ * 2), ((token0 + 16 * m) * D_ + 128 * nt + 64 * wn + 32 * n2 + 8 * cch) * 2, ch);
    }
  }
  STAMP(3);
#undef RDX
#undef RDW
}

constexpr int LDS_BYTES = 81920;
#define MODE 0
__global__ __launch_bounds__(256, 2) void k_main(const u16* __restrict__ x, const u16* __restrict__ wq, const u16* __restrict__ wk, const u16* __restrict__ wv, const u16* __restrict__ wo,
                                                  const u16* __restrict__ w1, const u16* __restrict__ b1, const u16* __restrict__ w2, const u16* __restrict__ b2, u16* __restrict__ out, char* __restrict__ ws) {
  extern __shared__ __attribute__((aligned(16))) char lds_[];
  LAS char* lds = (LAS char*)lds_;
  const int w = __builtin_amdgcn_readfirstlane(threadIdx.x >> 6);
  u64 eraw;
  { const GAS u64* ep = flagp(ws, FL_E); asm volatile("global_load_dwordx2 %0, %1, off sc1" : "=&v"(eraw) : "v"(ep)); }
  if ((blockIdx.x >> 8) == 0) {
    __builtin_amdgcn_s_setprio(PRIO_Q);
    const u64 tag = phase0(blockIdx.x, x, wk, wv, ws, lds, w, eraw);
    role_attn(blockIdx.x, wq, w2, wo, ws, lds, w, tag);
  } else {
    __builtin_amdgcn_s_setprio(PRIO_G1);
    const u64 tag = role_w1(blockIdx.x & 255, wq, w1, b1, w2, ws, lds, w, eraw);
    role_w2(blockIdx.x & 255, w2, wo, b2, x, out, ws, lds, w, tag);
  }
  asm volatile("s_endpgm\n\t.fill 16384, 4, 0xbf800000");
}

extern "C" void kernel_launch(void* const* d_in, const int* in_sizes, int n_in, void* d_out, int out_size, void* d_ws, size_t ws_size, hipStream_t stream) {
  const u16* x = (const u16*)d_in[0]; const u16* wq = (const u16*)d_in[1]; const u16* wk = (const u16*)d_in[2];
  const u16* wv = (const u16*)d_in[3]; const u16* wo = (const u16*)d_in[4];
  const u16* w1 = (const u16*)d_in[6]; const u16* b1 = (const u16*)d_in[7]; const u16* w2 = (const u16*)d_in[8]; const u16* b2 = (const u16*)d_in[9];
  (void)hipFuncSetAttribute((const void*)k_main, hipFuncAttributeMaxDynamicSharedMemorySize, LDS_BYTES);
  k_main<<<512, 256, LDS_BYTES, stream>>>(x, wq, wk, wv, wo, w1, b1, w2, b2, (u16*)d_out, (char*)d_ws);
}
```

```cpp
#include <hip/hip_runtime.h>
#include <stdint.h>
#define PROBE 0

typedef unsigned short u16;
typedef __bf16 bf16x8 __attribute__((ext_vector_type(8)));
typedef __bf16 bf16x2 __attribute__((ext_vector_type(2)));
typedef short s16x8 __attribute__((ext_vector_type(8)));
typedef float f32x4 __attribute__((ext_vector_type(4)));
typedef float f32x2 __attribute__((ext_vector_type(2)));
typedef unsigned u32x4 __attribute__((ext_vector_type(4)));
typedef unsigned u32x2 __attribute__((ext_vector_type(2)));
#define LAS __attribute__((address_space(3)))
#define GAS __attribute__((address_space(1)))
#define DI __device__ __forceinline__
#define SB() __builtin_amdgcn_sched_barrier(0)
#define PIN(x) asm volatile("" : "+v"(x))
#define MFMA16(a, b, c) __builtin_amdgcn_mfma_f32_16x16x32_bf16((a), (b), (c), 0, 0, 0)
#define BF(x) __builtin_bit_cast(bf16x8, x)

#define PF0ROWS 1024
constexpr int S_ = 2048, D_ = 1024, F_ = 3584;
constexpr size_t WS_FLAGS = 0;
constexpr size_t WS_R2   = 131072;
constexpr size_t WS_KV   = WS_R2 + 8192;
constexpr size_t WS_XN   = WS_KV + 262144;
constexpr size_t WS_H    = WS_XN + 4194304;
constexpr size_t WS_O    = WS_H + (size_t)112 * 128 * 1024;
constexpr size_t WS_PART = WS_O + 4194304;
constexpr size_t WS_STAMP = WS_PART + (size_t)2 * 256 * 32768;

constexpr float QSCALE = 0.17677669529663687f * 1.4426950408889634f;

DI int lane_id() { int l; asm volatile("v_mbcnt_lo_u32_b32 %0, -1, 0\n\tv_mbcnt_hi_u32_b32 %0, -1, %0" : "=v"(l)); return l; }
DI float bf2f(u16 v) { return __uint_as_float(((unsigned)v) << 16); }
DI unsigned cvtpk(float lo, float hi) { f32x2 v = {lo, hi}; bf16x2 b = __builtin_convertvector(v, bf16x2); return __builtin_bit_cast(unsigned, b); }
DI float lo2f(unsigned u) { return __uint_as_float(u << 16); }
DI float hi2f(unsigned u) { return __uint_as_float(u & 0xffff0000u); }
DI __amdgpu_buffer_rsrc_t mkrsrc(const void* p, unsigned bytes) { return __builtin_amdgcn_make_buffer_rsrc((void*)p, 0, bytes, 0x00020000); }
DI void dma16(unsigned lds_addr, unsigned voff, __amdgpu_buffer_rsrc_t rsrc, unsigned soff) {
  asm volatile("s_mov_b32 m0, %0\n\ts_nop 0\n\tbuffer_load_dwordx4 %1, %2, %3 offen lds" :: "s"(lds_addr), "v"(voff), "s"(rsrc), "s"(soff));
}
template <int IMM, int INC> DI void dma2(unsigned mbase, unsigned v0, unsigned v1, __amdgpu_buffer_rsrc_t rsrc, unsigned& soff) {
  asm volatile("s_add_u32 m0, %3, %5\n\ts_add_u32 %0, %0, %6\n\tbuffer_load_dwordx4 %1, %4, %0 offen lds\n\tbuffer_load_dwordx4 %2, %4, %0 offen offset:1024 lds"
               : "+s"(soff) : "v"(v0), "v"(v1), "s"(mbase), "s"(rsrc), "i"(IMM), "i"(INC) : "scc");
}
template <int IMM, int INC> DI void dma1a(unsigned mbase, unsigned v0, __amdgpu_buffer_rsrc_t rsrc, unsigned& soff) {
  asm volatile("s_add_u32 m0, %2, %4\n\ts_add_u32 %0, %0, %5\n\tbuffer_load_dwordx4 %1, %3, %0 offen lds" : "+s"(soff) : "v"(v0), "s"(mbase), "s"(rsrc), "i"(IMM), "i"(INC) : "scc");
}
template <int OFF> DI void dma1b(unsigned v0, __amdgpu_buffer_rsrc_t rsrc, unsigned soff) {
  asm volatile("buffer_load_dwordx4 %0, %1, %2 offen offset:%3 lds" :: "v"(v0), "s"(rsrc), "s"(soff), "i"(OFF));
}
template <int IMM, int INC> DI void dma1a_c(unsigned mbase, unsigned v0, __amdgpu_buffer_rsrc_t rsrc, unsigned& soff) {
  asm volatile("s_add_u32 m0, %2, %4\n\ts_add_u32 %0, %0, %5\n\tbuffer_load_dwordx4 %1, %3, %0 offen sc1 lds" : "+s"(soff) : "v"(v0), "s"(mbase), "s"(rsrc), "i"(IMM), "i"(INC) : "scc");
}
template <int OFF> DI void dma1b_c(unsigned v0, __amdgpu_buffer_rsrc_t rsrc, unsigned soff) {
  asm volatile("buffer_load_dwordx4 %0, %1, %2 offen offset:%3 sc1 lds" :: "v"(v0), "s"(rsrc), "s"(soff), "i"(OFF));
}
DI void dma4(unsigned lds_addr, unsigned voff, __amdgpu_buffer_rsrc_t rsrc, unsigned soff) {
  asm volatile("s_mov_b32 m0, %0\n\ts_nop 0\n\tbuffer_load_dword %1, %2, %3 offen lds" :: "s"(lds_addr), "v"(voff), "s"(rsrc), "s"(soff));
}
DI void dma4_c(unsigned lds_addr, unsigned voff, __amdgpu_buffer_rsrc_t rsrc, unsigned soff) {
  asm volatile("s_mov_b32 m0, %0\n\ts_nop 0\n\tbuffer_load_dword %1, %2, %3 offen sc1 lds" :: "s"(lds_addr), "v"(voff), "s"(rsrc), "s"(soff));
}
template <int ADD> DI void dma4_ahead(unsigned lds_addr, unsigned voff, __amdgpu_buffer_rsrc_t rsrc, unsigned soff) {
  unsigned t;
  asm volatile("s_mov_b32 m0, %1\n\ts_add_u32 %0, %4, %5\n\tbuffer_load_dword %2, %3, %0 offen lds" : "=&s"(t) : "s"(lds_addr), "v"(voff), "s"(rsrc), "s"(soff), "i"(ADD) : "scc");
}
#define WAIT_BAR(N) asm volatile("s_waitcnt vmcnt(" #N ") lgkmcnt(0)\n\ts_barrier" ::: "memory")
DI bf16x8 ldsfrag(const LAS char* p) { return *(const LAS bf16x8*)p; }
typedef short v4i16_t __attribute__((ext_vector_type(4)));
DI bf16x8 ldstr(const LAS char* p0, const LAS char* p1) {
  v4i16_t a = __builtin_amdgcn_ds_read_tr16_b64_v4i16((LAS v4i16_t*)p0);
  v4i16_t b = __builtin_amdgcn_ds_read_tr16_b64_v4i16((LAS v4i16_t*)p1);
  s16x8 v = __builtin_shufflevector(a, b, 0, 1, 2, 3, 4, 5, 6, 7);
  return __builtin_bit_cast(bf16x8, v);
}
#define STAUX 17
DI void st_wt16(__amdgpu_buffer_rsrc_t r, unsigned voff, u32x4 v) { __builtin_amdgcn_raw_buffer_store_b128(v, r, voff, 0, STAUX); }
DI u32x4 chunk_of(unsigned x0, unsigned x1, unsigned y0, unsigned y1) {
  auto s0 = __builtin_amdgcn_permlane16_swap(x0, y0, false, false);
  auto s1 = __builtin_amdgcn_permlane16_swap(x1, y1, false, false);
  return (u32x4){s0[0], s1[0], s0[1], s1[1]};
}
DI unsigned wdma_off(int l, int kblk, int ldbytes) {
  const int row = l >> 3, c8 = l & 7, sw = ((row >> 1) & 1) | ((kblk & 1) << 1);
  return row * ldbytes + (((c8 >> 1) ^ sw) * 32) + (c8 & 1) * 16;
}
DI unsigned wrd_off(int l, int seg) {
  const int g = l >> 4, q = (l & 15) >> 2, sw = ((q >> 1) & 1) | ((g & 1) << 1);
  return q * 128 + ((seg ^ sw) * 32) + (l & 3) * 8;
}
DI float xsum4(float v) {
  auto a = __builtin_amdgcn_permlane16_swap(__float_as_uint(v), __float_as_uint(v), false, false); v = __uint_as_float(a[0]) + __uint_as_float(a[1]);
  auto b = __builtin_amdgcn_permlane32_swap(__float_as_uint(v), __float_as_uint(v), false, false); return __uint_as_float(b[0]) + __uint_as_float(b[1]);
}
DI float xmax4(float v) {
  auto a = __builtin_amdgcn_permlane16_swap(__float_as_uint(v), __float_as_uint(v), false, false); v = fmaxf(__uint_as_float(a[0]), __uint_as_float(a[1]));
  auto b = __builtin_amdgcn_permlane32_swap(__float_as_uint(v), __float_as_uint(v), false, false); return fmaxf(__uint_as_float(b[0]), __uint_as_float(b[1]));
}
#define STAMP(i)

typedef unsigned long long u64;
constexpr int FL_E = 0, FL_RD = 16, FL_XN = 528, FL_KV = 784, FL_H = 1040, FL_O = 1296, FL_X = 1552;
DI GAS u64* flagp(char* ws, int i) { return (GAS u64*)(ws + WS_FLAGS + (size_t)i * 64); }
DI void signal(char* ws, int i, u64 tag) { __hip_atomic_store(flagp(ws, i), tag, __ATOMIC_RELAXED, __HIP_MEMORY_SCOPE_AGENT); }
DI u64 take_tag(char* ws, u64 eraw, int w) {
  asm volatile("" : "+v"(eraw));
  const u64 tag = (((u64)(unsigned)__builtin_amdgcn_readfirstlane((unsigned)(eraw >> 32))) << 32 | (unsigned)__builtin_amdgcn_readfirstlane((unsigned)eraw)) + 1;
  if (w == 0 && lane_id() == 0) signal(ws, FL_RD + blockIdx.x, tag);
  return tag;
}
DI void await(char* ws, int idx, u64 tag) {
  GAS u64* f = flagp(ws, idx < 0 ? FL_E : idx);
  for (unsigned spins = 0; spins < (1u << 20); ++spins) {
    const u64 v = __hip_atomic_load(f, __ATOMIC_RELAXED, __HIP_MEMORY_SCOPE_AGENT);
    if (__all(idx < 0 || v == tag)) break;
    __builtin_amdgcn_s_sleep(2);
  }
  asm volatile("" ::: "memory");
}

DI u64 phase0(int a, const u16* __restrict__ x, const u16* __restrict__ wk, const u16* __restrict__ wv, char* __restrict__ ws, LAS char* lds, const int w, const u64 eraw) {
  const int l = lane_id(), tid = w * 64 + l;
  LAS float* part = (LAS float*)(lds + 65536);
  LAS float* fin = (LAS float*)(lds + 65536 + 8192);
  LAS float* red = (LAS float*)(lds + 65536 + 8192 + 2048);
  const int rb = a & 127, hf = a >> 7;
  const int r = l & 15, c = l >> 4; const int stamp_base = 768; (void)stamp_base;
  STAMP(0);
  u32x4 xv[8];
  { const u16* xr = x + (size_t)(rb * 16 + r) * D_ + w * 256 + c * 8;
#define XL(i) asm volatile("global_load_dwordx4 %0, %1, off offset:" #i "*64" : "=&v"(xv[i]) : "v"(xr))
    XL(0); XL(1); XL(2); XL(3); XL(4); XL(5); XL(6); XL(7); }
  const unsigned lds0 = (unsigned)(uintptr_t)lds;
  {
    __amdgpu_buffer_rsrc_t rw = mkrsrc(hf ? wv : wk, 65536);
#pragma unroll
    for (int i = 0; i < 16; ++i) dma16(lds0 + w * 16384 + i * 1024, l * 16, rw, w * 16384 + i * 1024);
  }
  asm volatile("s_waitcnt vmcnt(16)" : "+v"(xv[0]), "+v"(xv[1]), "+v"(xv[2]), "+v"(xv[3]), "+v"(xv[4]), "+v"(xv[5]), "+v"(xv[6]), "+v"(xv[7]));
  STAMP(1);
  const u64 tag = take_tag(ws, eraw, w);
  f32x2 v[32];
#pragma unroll
  for (int i = 0; i < 8; ++i)
#pragma unroll
    for (int j = 0; j < 4; ++j) v[4 * i + j] = (f32x2){lo2f(xv[i][j]), hi2f(xv[i][j])};
  f32x2 s2 = (v[0] + v[1]) + (v[2] + v[3]);
#pragma unroll
  for (int i = 4; i < 32; i += 4) s2 += (v[i] + v[i + 1]) + (v[i + 2] + v[i + 3]);
  const float s = xsum4(s2[0] + s2[1]);
  if (c == 0) red[w * 16 + r] = s;
  asm volatile("s_waitcnt lgkmcnt(0)\n\ts_barrier" ::: "memory");
  const float mean = ((red[r] + red[16 + r]) + (red[32 + r] + red[48 + r])) * (1.0f / D_);
  f32x2 q2 = {0.f, 0.f}, q3 = {0.f, 0.f};
#pragma unroll
  for (int i = 0; i < 32; i += 2) { v[i] -= mean; v[i + 1] -= mean; q2 += v[i] * v[i]; q3 += v[i + 1] * v[i + 1]; }
  const float q = xsum4((q2[0] + q2[1]) + (q3[0] + q3[1]));
  if (c == 0) red[64 + w * 16 + r] = q;
  asm volatile("s_waitcnt lgkmcnt(0)\n\ts_barrier" ::: "memory");
  const float var = ((red[64 + r] + red[80 + r]) + (red[96 + r] + red[112 + r])) * (1.0f / D_);
  const float rstd = 1.0f / sqrtf(var + 1e-5f);
#pragma unroll
  for (int i = 0; i < 8; ++i)
#pragma unroll
    for (int j = 0; j < 4; ++j) { const f32x2 t = v[4 * i + j] * rstd; xv[i][j] = cvtpk(t[0], t[1]); }
  STAMP(4);
  const __amdgpu_buffer_rsrc_t rXN = mkrsrc(ws + WS_XN, 4194304);
#pragma unroll
  for (int i = 0; i < 4; ++i) st_wt16(rXN, ((8 * w + 2 * i + hf) * 128 + rb) * 1024 + l * 16, hf ? xv[2 * i + 1] : xv[2 * i]);
  if (hf == 0 && w == 0 && c == 0) __hip_atomic_store((GAS float*)(ws + WS_R2) + rb * 16 + r, 1.0f / sqrtf(var * rstd * rstd + 1e-5f), __ATOMIC_RELAXED, __HIP_MEMORY_SCOPE_AGENT);
  asm volatile("s_waitcnt vmcnt(0)\n\ts_barrier" ::: "memory");
  if (tid == 0) signal(ws, FL_XN + a, tag);
  STAMP(5);
  f32x4 acc[2] = {{0.f, 0.f, 0.f, 0.f}, {0.f, 0.f, 0.f, 0.f}};
  const LAS char* wb = (const LAS char*)lds + w * 16384 + (8 * c + ((l & 15) >> 2)) * 64 + (l & 3) * 8;
#pragma unroll
  for (int i = 0; i < 8; ++i)
#pragma unroll
    for (int b = 0; b < 2; ++b) {
      bf16x8 wf = ldstr(wb + i * 2048 + b * 32, wb + i * 2048 + b * 32 + 256);
      acc[b] = MFMA16(BF(xv[i]), wf, acc[b]);
    }
#pragma unroll
  for (int b = 0; b < 2; ++b)
#pragma unroll
    for (int j = 0; j < 4; ++j) part[(w * 16 + 4 * c + j) * 32 + 16 * b + r] = acc[b][j];
  asm volatile("s_waitcnt lgkmcnt(0)\n\ts_barrier" ::: "memory");
  STAMP(2);
#pragma unroll
  for (int u = 0; u < 2; ++u) { const int e = tid + 256 * u; fin[e] = (part[e] + part[512 + e]) + (part[1024 + e] + part[1536 + e]); }
  asm volatile("s_waitcnt lgkmcnt(0)\n\ts_barrier" ::: "memory");
  if (w == 0) {
    float o[8];
    unsigned dst = (rb >> 1) * 4096;
    const int half = rb & 1;
    if (hf == 0) { const int kb = l >> 5, g = (l >> 3) & 3, rr = l & 7;
      const int row = 8 * half + rr, key = 8 * (row >> 2) + 4 * kb + (row & 3) - 16 * half;
#pragma unroll
      for (int j = 0; j < 8; ++j) o[j] = fin[key * 32 + 16 * (j >> 2) + 4 * g + (j & 3)];
      dst += kb * 1024 + (g * 16 + row) * 16;
    } else { const int db = l >> 5, gl = (l >> 4) & 1, rr = l & 15;
#pragma unroll
      for (int j = 0; j < 8; ++j) o[j] = fin[(8 * gl + j) * 32 + 16 * db + rr];
      dst += 2048 + db * 1024 + ((2 * half + gl) * 16 + rr) * 16;
    }
    st_wt16(mkrsrc(ws + WS_KV, 262144), dst, (u32x4){cvtpk(o[0], o[1]), cvtpk(o[2], o[3]), cvtpk(o[4], o[5]), cvtpk(o[6], o[7])});
    asm volatile("s_waitcnt vmcnt(0)" ::: "memory");
    if (l == 0) signal(ws, FL_KV + a, tag);
  }
  STAMP(3);
  return tag;
}

#define MERGED 1
#define PRIO_A 1
#define G1PAT 0
#define G2PAT 0
#define PFSLEEP 30
#define GDELAY 0
#define PFROWS 256
#define PRIO_Q 1
#define PRIO_G1 0
#define PRIO_GE 2
#define PRIO_G2 0
#define LSUM_MFMA 1
DI float fadd_s(float a, float b) { float r; asm("s_nop 0\n\tv_add_f32_e32 %0, %1, %2" : "=v"(r) : "v"(a), "v"(b)); return r; }

DI void attn_slow(const char* __restrict__ kv, bf16x8 qf, f32x4& o0, f32x4& o1) {
  const int l = lane_id();
  const __amdgpu_buffer_rsrc_t rk = mkrsrc(kv, 262144);
  o0 = (f32x4){0.f, 0.f, 0.f, 0.f}; o1 = o0;
  float m = -3.0e38f, ls = 0.f;
#pragma unroll 1
  for (int t = 0; t < 64; ++t) {
    const int p = t * 4096 + l * 16;
    const bf16x8 k0 = BF(__builtin_amdgcn_raw_buffer_load_b128(rk, p, 0, 16)), k1 = BF(__builtin_amdgcn_raw_buffer_load_b128(rk, p + 1024, 0, 16)),
                 v0 = BF(__builtin_amdgcn_raw_buffer_load_b128(rk, p + 2048, 0, 16)), v1 = BF(__builtin_amdgcn_raw_buffer_load_b128(rk, p + 3072, 0, 16));
    const f32x4 z = {0.f, 0.f, 0.f, 0.f};
    f32x4 s0 = MFMA16(k0, qf, z), s1 = MFMA16(k1, qf, z);
    float mx = fmaxf(fmaxf(fmaxf(s0[0], s0[1]), fmaxf(s0[2], s0[3])), fmaxf(fmaxf(s1[0], s1[1]), fmaxf(s1[2], s1[3])));
    mx = fmaxf(mx, __shfl_xor(mx, 16)); mx = fmaxf(mx, __shfl_xor(mx, 32));
    const float mn = fmaxf(m, mx), al = __builtin_amdgcn_exp2f(m - mn);
    m = mn; float rs = 0.f;
#pragma unroll
    for (int i = 0; i < 4; ++i) { s0[i] = __builtin_amdgcn_exp2f(s0[i] - mn); s1[i] = __builtin_amdgcn_exp2f(s1[i] - mn); rs += s0[i] + s1[i]; o0[i] *= al; o1[i] *= al; }
    rs += __shfl_xor(rs, 16); rs += __shfl_xor(rs, 32); ls = ls * al + rs;
    const u32x4 pp = {cvtpk(s0[0], s0[1]), cvtpk(s0[2], s0[3]), cvtpk(s1[0], s1[1]), cvtpk(s1[2], s1[3])};
    o0 = MFMA16(v0, BF(pp), o0); o1 = MFMA16(v1, BF(pp), o1);
  }
#pragma unroll
  for (int i = 0; i < 4; ++i) { o0[i] /= ls; o1[i] /= ls; }
}

DI void role_attn(int a, const u16* __restrict__ wq, const u16* __restrict__ w2, const u16* __restrict__ wo, char* __restrict__ ws, LAS char* lds, const int w, const u64 tag) {
  const int l = lane_id();
#define AMAP 2
  constexpr int NTB = 32 / AMAP;
  const int xcd = a & 7, idx = a >> 3, tb = (xcd / (8 / AMAP)) * NTB + (idx % NTB), hg = (xcd % (8 / AMAP)) * AMAP + idx / NTB;
  const int g = l >> 4; const int stamp_base = 0; (void)stamp_base;
  const unsigned lds0 = (unsigned)(uintptr_t)lds;
  constexpr int SLOT = 24576;
  const __amdgpu_buffer_rsrc_t rX = mkrsrc(ws + WS_XN, 4194304), rKV = mkrsrc(ws + WS_KV, 262144);
  const unsigned vlin = l * 16;
  constexpr int QS = 12288;
  const __amdgpu_buffer_rsrc_t rWq = mkrsrc((const char*)wq - 1024, D_ * D_ * 2 + 1024);
  const unsigned vq0 = wdma_off(l, w, 2048) + hg * 256 + w * 8 * 2048 + 1024, vq1 = vq0 + 128 - 1024;
  unsigned xso = (4 * tb + w) * 1024 - 131072, qso = 0u - 65536;
  const unsigned mx = lds0 + w * 1024, mq = lds0 + 4096 + w * 2048;
#define PCQ(SL, i) do { if constexpr ((i) == 0) dma1a_c<(SL) * QS, 131072>(mx, vlin, rX, xso); else if constexpr ((i) == 1) dma1a<(SL) * QS, 65536>(mq, vq0, rWq, qso); else dma1b<1024>(vq1, rWq, qso); } while (0)
#define STGQ(SL) do { PCQ(SL, 0); PCQ(SL, 1); PCQ(SL, 2); } while (0)
  if (w == 0) await(ws, l < 8 ? FL_XN + 4 * tb + (l & 3) + 128 * (l >> 2) : -1, tag);
  asm volatile("s_waitcnt lgkmcnt(0)\n\ts_barrier" ::: "memory");
  STAMP(0);
  constexpr int NPL = 64 / (4 * NTB);
  const int pl = ((idx % NTB) * 4 + w) * NPL + l;
  const unsigned vpq = l < NPL ? (pl >> 1) * 2048 + hg * 256 + (pl & 1) * 128 + 1024 : 0x80000000u;
  const unsigned mpq = lds0 + 6 * QS;
  STGQ(0); STGQ(1); STGQ(2); STGQ(3); STGQ(4); STGQ(5);
  dma4_ahead<1 * 65536>(mpq, vpq, rWq, qso); dma4_ahead<2 * 65536>(mpq, vpq, rWq, qso); dma4_ahead<3 * 65536>(mpq, vpq, rWq, qso);
  dma4_ahead<4 * 65536>(mpq, vpq, rWq, qso); dma4_ahead<5 * 65536>(mpq, vpq, rWq, qso); dma4_ahead<6 * 65536>(mpq, vpq, rWq, qso);
  f32x4 qacc[2][4];
#pragma unroll
  for (int i = 0; i < 2; ++i)
#pragma unroll
    for (int j = 0; j < 4; ++j) qacc[i][j] = (f32x4){0.f, 0.f, 0.f, 0.f};
  const LAS char* xrd = lds + l * 16;
  const LAS char* wrd0 = lds + 4096 + (g * 2 + (w >> 1)) * 1024 + wrd_off(l, 2 * (w & 1));
  const LAS char* wrd1 = lds + 4096 + (g * 2 + (w >> 1)) * 1024 + wrd_off(l, 2 * (w & 1) + 1);
  bf16x8 qxa[4], qwa[2], qxb[4], qwb[2];
#define QRX(X, m, SL) X[m] = ldsfrag(xrd + (SL) * QS + (m) * 1024)
#define QRW(W, n, SL) W[n] = ((n) == 0 ? ldstr(wrd0 + (SL) * QS, wrd0 + (SL) * QS + 512) : ldstr(wrd1 + (SL) * QS, wrd1 + (SL) * QS + 512))
#define QM(X, W, m, n) qacc[n][m] = MFMA16(W[n], X[m], qacc[n][m])
#define STEPQ(X, W, XN, WN, RD, RS, DMA, DS) do { \
    QM(X, W, 0, 0); if (RD) { QRW(WN, 0, RS); QRX(XN, 0, RS); } SB(); QM(X, W, 1, 0); if (RD) { QRW(WN, 1, RS); QRX(XN, 1, RS); } SB(); QM(X, W, 2, 0); if (RD) QRX(XN, 2, RS); SB(); QM(X, W, 3, 0); if (RD) QRX(XN, 3, RS); SB(); \
    QM(X, W, 0, 1); if (DMA) PCQ(DS, 0); SB(); QM(X, W, 1, 1); if (DMA) PCQ(DS, 1); SB(); QM(X, W, 2, 1); if (DMA) PCQ(DS, 2); SB(); QM(X, W, 3, 1); if (DMA) dma4_ahead<6 * 65536>(mpq, vpq, rWq, qso); SB(); } while (0)
  WAIT_BAR(21);
#pragma unroll
  for (int m = 0; m < 4; ++m) QRX(qxa, m, 0);
  QRW(qwa, 0, 0); QRW(qwa, 1, 0);
#define QEV(SC, SN, W_, RD, DMA) do { WAIT_BAR(W_); SB(); STEPQ(qxa, qwa, qxb, qwb, RD, SN, DMA, SC); } while (0)
#define QOD(SC, SN, W_, RD, DMA) do { WAIT_BAR(W_); SB(); STEPQ(qxb, qwb, qxa, qwa, RD, SN, DMA, SC); } while (0)
  QEV(0, 1, 18, true, true); QOD(1, 2, 19, true, true); QEV(2, 3, 20, true, true); QOD(3, 4, 21, true, true); QEV(4, 5, 22, true, true); QOD(5, 0, 17, true, true);
  for (int it = 0; it < 3; ++it) { QEV(0, 1, 17, true, true); QOD(1, 2, 17, true, true); QEV(2, 3, 17, true, true); QOD(3, 4, 17, true, true); QEV(4, 5, 17, true, true); QOD(5, 0, 17, true, true); }
  QEV(0, 1, 17, true, true); QOD(1, 2, 17, true, true);
  QEV(2, 3, 17, true, false); QOD(3, 4, 13, true, false); QEV(4, 5, 9, true, false); QOD(5, 0, 5, true, false);
  if (w == 0) { await(ws, FL_KV + l, tag); await(ws, FL_KV + 64 + l, tag); await(ws, FL_KV + 128 + l, tag); await(ws, FL_KV + 192 + l, tag); }
  QEV(0, 1, 0, true, false);
  unsigned kvso = w * 4096 - 16384;
  const unsigned kvm = lds0 + w * 4096;
#define STGKV(SF) do { dma1a_c<(SF) * SLOT, 16384>(kvm, vlin, rKV, kvso); dma1b_c<1024>(vlin, rKV, kvso); dma1b_c<2048>(vlin, rKV, kvso); dma1b_c<3072>(vlin, rKV, kvso); } while (0)
  STGKV(1); STGKV(2);
  {
    const unsigned line = (a * 4 + w) * 64 + l;
    dma4(lds0 + 6 * QS, line * 128, mkrsrc(w2, F_ * D_ * 2), 0);
    dma4(lds0 + 6 * QS, line * 128, mkrsrc(wo, D_ * D_ * 2), 0);
  }
  STEPQ(qxb, qwb, qxa, qwa, false, 0, false, 0);
  STAMP(1);
  __builtin_amdgcn_s_setprio(PRIO_A);
  bf16x8 qf[4];
#pragma unroll
  for (int m = 0; m < 4; ++m)
    qf[m] = BF(((u32x4){cvtpk(qacc[0][m][0] * QSCALE, qacc[0][m][1] * QSCALE), cvtpk(qacc[0][m][2] * QSCALE, qacc[0][m][3] * QSCALE),
                        cvtpk(qacc[1][m][0] * QSCALE, qacc[1][m][1] * QSCALE), cvtpk(qacc[1][m][2] * QSCALE, qacc[1][m][3] * QSCALE)}));
  WAIT_BAR(6);
  const LAS char* kvb = lds + l * 16;
  f32x4 negm[4], S[4][2], O[4][2];
  bf16x8 k0, k1;
  {
    const LAS char* p = kvb + 1 * SLOT;
    k0 = ldsfrag(p); k1 = ldsfrag(p + 1024);
    const f32x4 z = {0.f, 0.f, 0.f, 0.f};
#pragma unroll
    for (int m = 0; m < 4; ++m) {
      const f32x4 s0 = MFMA16(k0, qf[m], z), s1 = MFMA16(k1, qf[m], z);
      float mx = fmaxf(fmaxf(fmaxf(s0[0], s0[1]), fmaxf(s0[2], s0[3])), fmaxf(fmaxf(s1[0], s1[1]), fmaxf(s1[2], s1[3])));
      mx = xmax4(mx);
      negm[m] = (f32x4){-mx, -mx, -mx, -mx}; PIN(negm[m]);
      O[m][0] = z; O[m][1] = z;
    }
    S[0][0] = MFMA16(k0, qf[0], negm[0]); S[0][1] = MFMA16(k1, qf[0], negm[0]);
    S[1][0] = MFMA16(k0, qf[1], negm[1]); S[1][1] = MFMA16(k1, qf[1], negm[1]);
  }
  u32x4 P[4];
  P[2] = (u32x4){0, 0, 0, 0}; P[3] = P[2];
  bf16x8 va0 = BF(P[2]), va1 = va0, vb0, vb1;
  S[3][1] = (f32x4){0.f, 0.f, 0.f, 0.f};
  f32x4 L[4];
#pragma unroll
  for (int m = 0; m < 4; ++m) L[m] = (f32x4){0.f, 0.f, 0.f, 0.f};
  bf16x8 ones; { const short o1 = (l & 15) == 0 ? (short)0x3f80 : (short)0; s16x8 ov = {o1, o1, o1, o1, o1, o1, o1, o1}; ones = __builtin_bit_cast(bf16x8, ov); PIN(ones); }
#define LS(m) do { L[m] = MFMA16(ones, BF(P[m]), L[m]); PIN(L[m]); SB(); } while (0)
#define LADD2(m, kb, i)
#define NOLS(m)
#define EXPF(x) __builtin_amdgcn_exp2f(x)
#define EX2(m, kb, i) do { float t0_ = EXPF(S[m][kb][i]), t1_ = EXPF(S[m][kb][i + 1]); PIN(t0_); PIN(t1_); S[m][kb][i] = t0_; S[m][kb][i + 1] = t1_; SB(); } while (0)
#define CVA(m, kb) do { unsigned c_ = cvtpk(S[m][kb][0], S[m][kb][1]); PIN(c_); P[m][2 * kb] = c_; LADD2(m, kb, 0); SB(); } while (0)
#define CVB(m, kb) do { unsigned c_ = cvtpk(S[m][kb][2], S[m][kb][3]); PIN(c_); P[m][2 * kb + 1] = c_; LADD2(m, kb, 2); SB(); } while (0)
#define GRP(mq, kq, KF, pm, pk, me, ke, mp, db, VF, LSM, X) do { \
    S[mq][kq] = MFMA16(KF, qf[mq], negm[mq]); PIN(S[mq][kq]); SB(); X; CVB(pm, pk); EX2(me, ke, 0); \
    O[mp][db] = MFMA16(VF, BF(P[mp]), O[mp][db]); PIN(O[mp][db]); SB(); EX2(me, ke, 2); LSM(mp); CVA(me, ke); } while (0)
#define TILE(KC0, KC1, KN0, KN1, VP0, VP1, VC0, VC1, kn, vp, D) do { \
    GRP(2, 0, KC0, 3, 1, 0, 0, 2, 0, VP0, NOLS, KN0 = ldsfrag(kn); SB()); \
    GRP(2, 1, KC1, 0, 0, 0, 1, 2, 1, VP1, LS,   KN1 = ldsfrag((kn) + 1024); SB()); \
    GRP(3, 0, KC0, 0, 1, 1, 0, 3, 0, VP0, NOLS, VC0 = ldsfrag(vp); SB()); \
    GRP(3, 1, KC1, 1, 0, 1, 1, 3, 1, VP1, LS,   VC1 = ldsfrag((vp) + 1024); SB()); \
    GRP(0, 0, KN0, 1, 1, 2, 0, 0, 0, VC0, NOLS, D(0)); \
    GRP(0, 1, KN1, 2, 0, 2, 1, 0, 1, VC1, LS,   D(1)); \
    GRP(1, 0, KN0, 2, 1, 3, 0, 1, 0, VC0, NOLS, D(2)); \
    GRP(1, 1, KN1, 3, 0, 3, 1, 1, 1, VC1, LS,   D(3)); \
  } while (0)
#define NOD(i)
  bf16x8 kx0, kx1;
#define STAGE(SC, SN, SF, DM, LAST) do { \
    TILE(k0, k1, kx0, kx1, va0, va1, vb0, vb1, kvb + (SC) * SLOT + 4096, kvb + (SC) * SLOT + 2048, NOD); \
    TILE(kx0, kx1, k0, k1, vb0, vb1, va0, va1, kvb + (SC) * SLOT + 8192, kvb + (SC) * SLOT + 4096 + 2048, NOD); \
    TILE(k0, k1, kx0, kx1, va0, va1, vb0, vb1, kvb + (SC) * SLOT + 12288, kvb + (SC) * SLOT + 8192 + 2048, NOD); \
    WAIT_BAR(0);        \
    TILE(kx0, kx1, k0, k1, vb0, vb1, va0, va1, kvb + ((LAST) ? (SC) : (SN)) * SLOT, kvb + (SC) * SLOT + 12288 + 2048, DM); } while (0)
#define DM_(SF, i) do { if constexpr ((i) == 0) { dma1a_c<(SF) * SLOT, 16384>(kvm, vlin, rKV, kvso); } else { dma1b_c<(i) * 1024>(vlin, rKV, kvso); } SB(); } while (0)
#define DM0(i) DM_(0, i)
#define DM1(i) DM_(1, i)
#define DM2(i) DM_(2, i)
  for (int j = 0; j < 12; j += 3) { STAGE(1, 2, 0, DM0, false); STAGE(2, 0, 1, DM1, false); STAGE(0, 1, 2, DM2, false); STAMP(4 + j / 3); }
  STAGE(1, 2, 0, DM0, false); STAGE(2, 0, 1, DM1, false); STAGE(0, 1, 2, NOD, false); STAGE(1, 2, 0, NOD, true);
  CVB(3, 1);
  O[2][0] = MFMA16(va0, BF(P[2]), O[2][0]); O[2][1] = MFMA16(va1, BF(P[2]), O[2][1]); LS(2);
  O[3][0] = MFMA16(va0, BF(P[3]), O[3][0]); O[3][1] = MFMA16(va1, BF(P[3]), O[3][1]); LS(3);
  STAMP(2);
  const __amdgpu_buffer_rsrc_t rO = mkrsrc(ws + WS_O, 4194304);
#pragma unroll
  for (int m = 0; m < 4; ++m) {
    float ls = __shfl(L[m][0], l & 15);
    float inv = 1.0f / ls;
    f32x4 o0 = O[m][0], o1 = O[m][1];
    const float mo = fmaxf(fmaxf(fmaxf(fabsf(o0[0]), fabsf(o0[1])), fmaxf(fabsf(o0[2]), fabsf(o0[3]))), fmaxf(fmaxf(fabsf(o1[0]), fabsf(o1[1])), fmaxf(fabsf(o1[2]), fabsf(o1[3]))));
    const bool ok = (ls <= 1.0e18f) && (mo <= 1.0e37f);
    if (__builtin_expect(__any(!ok), 0)) { attn_slow(ws + WS_KV, qf[m], o0, o1); inv = 1.0f; }
    const u32x4 ch = chunk_of(cvtpk(o0[0] * inv, o0[1] * inv), cvtpk(o0[2] * inv, o0[3] * inv), cvtpk(o1[0] * inv, o1[1] * inv), cvtpk(o1[2] * inv, o1[3] * inv));
    const int c = ((g & 1) << 1) | (g >> 1);
    st_wt16(rO, ((4 * hg + w) * 128 + 4 * tb + m) * 1024 + c * 256 + (l & 15) * 16, ch);
  }
  asm volatile("s_waitcnt vmcnt(0)\n\ts_barrier" ::: "memory");
  if (w == 0 && lane_id() == 0) signal(ws, FL_O + tb * 8 + hg, tag);
  STAMP(3);
}

DI int w2_kh(int gi) { return gi & 1; }
DI void w2_weights_early(int gi, const u16* __restrict__ w2, LAS char* lds, const int w) {
  const int l = lane_id(), nt = gi >> 5;
  const __amdgpu_buffer_rsrc_t rW = mkrsrc((const char*)w2 - 1024, F_ * D_ * 2 + 1024);
  const unsigned vw0 = wdma_off(l, w, 2048) + nt * 256 + w * 8 * 2048 + 1024, vw1 = vw0 + 128 - 1024;
  unsigned wso = 56 * w2_kh(gi) * 65536 - 65536;
  const unsigned mw = (unsigned)(uintptr_t)lds + 8192 + w * 2048;
  dma1a<0 * 16384, 65536>(mw, vw0, rW, wso); dma1b<1024>(vw1, rW, wso); dma1a<1 * 16384, 65536>(mw, vw0, rW, wso); dma1b<1024>(vw1, rW, wso);
  dma1a<2 * 16384, 65536>(mw, vw0, rW, wso); dma1b<1024>(vw1, rW, wso); dma1a<3 * 16384, 65536>(mw, vw0, rW, wso); dma1b<1024>(vw1, rW, wso);
}

DI f32x4 gelu4(f32x4 acc, float rs, f32x4 b, float c1v) {
  constexpr float c2 = -2.0f * 0.7978845608028654f * 1.4426950408889634f * 0.044715f;
  float x0 = acc[0], x1 = acc[1], x2 = acc[2], x3 = acc[3], t0, t1, t2, t3;
  asm("v_fma_f32 %0, %0, %8, %9\n\tv_fma_f32 %1, %1, %8, %10\n\tv_fma_f32 %2, %2, %8, %11\n\tv_fma_f32 %3, %3, %8, %12\n\t"
      "v_mul_f32 %4, %0, %0\n\tv_mul_f32 %5, %1, %1\n\tv_mul_f32 %6, %2, %2\n\tv_mul_f32 %7, %3, %3\n\t"
      "v_fma_f32 %4, %4, %14, %13\n\tv_fma_f32 %5, %5, %14, %13\n\tv_fma_f32 %6, %6, %14, %13\n\tv_fma_f32 %7, %7, %14, %13\n\t"
      "v_mul_f32 %4, %0, %4\n\tv_mul_f32 %5, %1, %5\n\tv_mul_f32 %6, %2, %6\n\tv_mul_f32 %7, %3, %7\n\t"
      "v_exp_f32 %4, %4\n\tv_exp_f32 %5, %5\n\tv_exp_f32 %6, %6\n\tv_exp_f32 %7, %7\n\t"
      "v_add_f32 %4, 1.0, %4\n\tv_add_f32 %5, 1.0, %5\n\tv_add_f32 %6, 1.0, %6\n\tv_add_f32 %7, 1.0, %7\n\t"
      "v_rcp_f32 %4, %4\n\tv_rcp_f32 %5, %5\n\tv_rcp_f32 %6, %6\n\tv_rcp_f32 %7, %7\n\t"
      "v_mul_f32 %0, %0, %4\n\tv_mul_f32 %1, %1, %5\n\tv_mul_f32 %2, %2, %6\n\tv_mul_f32 %3, %3, %7"
      : "+v"(x0), "+v"(x1), "+v"(x2), "+v"(x3), "=&v"(t0), "=&v"(t1), "=&v"(t2), "=&v"(t3)
      : "v"(rs), "v"(b[0]), "v"(b[1]), "v"(b[2]), "v"(b[3]), "v"(c1v), "s"(c2));
  return (f32x4){x0, x1, x2, x3};
}

DI u64 role_w1(unsigned long long t_in, unsigned long long& probe, int gi, const u16* __restrict__ wq, const u16* __restrict__ w1, const u16* __restrict__ b1, const u16* __restrict__ w2, char* __restrict__ ws, LAS char* lds, const int w, u64 eraw) {
  const int l = lane_id();
  const int xcd = gi & 7, idx = gi >> 3, p = (xcd >> 2) * 8 + (idx & 7), j = (xcd & 3) * 4 + (idx >> 3);
  const int g = l >> 4, wm = w >> 1, wn = w & 1; const int stamp_base = 256; (void)stamp_base;
  const unsigned lds0 = (unsigned)(uintptr_t)lds;
  constexpr int SLOT = 24576;
  const __amdgpu_buffer_rsrc_t rX = mkrsrc(ws + WS_XN, 4194304), rW = mkrsrc((const char*)w1 - 4096, D_ * F_ * 2 + 4096);
  const unsigned vlin = l * 16;
  unsigned vw[4];
#pragma unroll
  for (int i = 0; i < 4; ++i) vw[i] = wdma_off(l, w, F_ * 2) + 8 * w * (F_ * 2) + 448 * j - 64 * (j & 1) + 128 * i + 4096 - 1024 * i;
  STAMP(0);
  unsigned xso = (8 * p + 2 * w) * 1024 - 131072, wso = 0u - 32 * F_ * 2;
  const unsigned mx = lds0 + w * 2048, mw = lds0 + 8192 + w * 4096;
#define PC1(SL, i) do { if constexpr ((i) == 0) dma1a_c<(SL) * SLOT, 131072>(mx, vlin, rX, xso); else if constexpr ((i) == 1) dma1b_c<1024>(vlin, rX, xso); \
    else if constexpr ((i) == 2) dma1a<(SL) * SLOT, 32 * F_ * 2>(mw, vw[0], rW, wso); else dma1b<((i) - 2) * 1024>(vw[(i) - 2], rW, wso); } while (0)
#define STG1(SL) do { PC1(SL, 0); PC1(SL, 1); PC1(SL, 2); PC1(SL, 3); PC1(SL, 4); PC1(SL, 5); } while (0)
  PC1(0, 2); PC1(0, 3); PC1(0, 4); PC1(0, 5); PC1(1, 2); PC1(1, 3); PC1(1, 4); PC1(1, 5); PC1(2, 2); PC1(2, 3); PC1(2, 4); PC1(2, 5);
  constexpr int L_R2 = 3 * SLOT, L_B1 = L_R2 + 512;
  const unsigned vpf = l < 4 ? (4 * (idx & 7) + w) * (F_ * 2) + 448 * j - 64 * (j & 1) + l * 128 + 4096 : 0x80000000u;
  const unsigned mpf = lds0 + L_B1 + 1024;
  dma4_ahead<1 * 32 * F_ * 2>(mpf, vpf, rW, wso); dma4_ahead<2 * 32 * F_ * 2>(mpf, vpf, rW, wso); dma4_ahead<3 * 32 * F_ * 2>(mpf, vpf, rW, wso);
  dma4_ahead<4 * 32 * F_ * 2>(mpf, vpf, rW, wso); dma4_ahead<5 * 32 * F_ * 2>(mpf, vpf, rW, wso);
  if (w != 0 && PFSLEEP > 0) __builtin_amdgcn_s_sleep(PFSLEEP);
  if (w == 1) dma4(mpf, (gi * 64 + l) * 128, mkrsrc(wq, D_ * D_ * 2), 0);
  if (w >= 2 && PFROWS > 0) { constexpr int NL = PFROWS * 56 / 512;
#pragma unroll
    for (int i = 0; i < (NL + 63) / 64; ++i) dma4(mpf, i * 64 + l < NL ? 256 * F_ * 2 + ((gi * 2 + (w - 2)) * NL + i * 64 + l) * 128 : 0x80000000u, mkrsrc(w1, D_ * F_ * 2), 0);
  }
  if (w >= 2) dma4(lds0 + L_B1 + (w - 2) * 256, 448 * j + ((w - 2) * 64 + l) * 4, mkrsrc(b1, F_ * 2), 0);
  asm volatile("s_waitcnt vmcnt(17)" : "+v"(eraw));
  const u64 tag = take_tag(ws, eraw, w);
  if (w == 0) await(ws, l < 16 ? FL_XN + 8 * p + (l & 7) + 128 * (l >> 3) : -1, tag);
  asm volatile("s_barrier" ::: "memory");
  STAMP(8);
  if (PROBE == 1) probe = __builtin_amdgcn_s_memrealtime() - t_in;
  if (w < 2) dma4_c(lds0 + L_R2 + w * 256, (128 * p + 64 * w + l) * 4, mkrsrc(ws + WS_R2, 8192), 0);
  PC1(0, 0); PC1(0, 1); PC1(1, 0); PC1(1, 1); PC1(2, 0); PC1(2, 1);
  dma4(mpf, 0x80000000u, rW, 0);
  f32x4 acc[4][7];
#pragma unroll
  for (int m = 0; m < 4; ++m)
#pragma unroll
    for (int n = 0; n < 7; ++n) acc[m][n] = (f32x4){0.f, 0.f, 0.f, 0.f};
  const LAS char* xrd = lds + 4 * wm * 1024 + l * 16;
  const LAS char* wrb[5];
  { const int off = 6 * wn + 2 * (j & 1), nl = 12 + wn + 2 * (j & 1);
#pragma unroll
    for (int i = 0; i < 4; ++i) wrb[i] = lds + 8192 + (g * 4 + ((i + off) >> 2)) * 1024 + wrd_off(l, (i + off) & 3);
    wrb[4] = lds + 8192 + (g * 4 + (nl >> 2)) * 1024 + wrd_off(l, nl & 3); }
  bf16x8 xa[4], wa[7], xb[4], wb[7];
#define RDX(X, m, so) X[m] = ldsfrag(xrd + (so) + (m) * 1024)
#define RDW(W, n, so) W[n] = ((n) < 6 ? ldstr(wrb[(n) & 3] + (so) + ((n) >> 2) * 1024, wrb[(n) & 3] + (so) + ((n) >> 2) * 1024 + 512) : ldstr(wrb[4] + (so), wrb[4] + (so) + 512))
#define M1(X, W, m, n) acc[m][n] = MFMA16(W[n], X[m], acc[m][n])
#define STEP1(X, W, XN, WN, RD, RS, DMA, DS) do { \
    M1(X, W, 0, 0); if (RD) RDW(WN, 0, (RS) * SLOT); SB(); M1(X, W, 1, 0); if (RD) RDX(XN, 0, (RS) * SLOT); SB(); M1(X, W, 2, 0); if (RD) RDW(WN, 1, (RS) * SLOT); SB(); M1(X, W, 3, 0); if (DMA) PC1(DS, 0); SB(); \
    M1(X, W, 0, 1); if (RD) RDX(XN, 1, (RS) * SLOT); SB(); M1(X, W, 1, 1); if (RD) RDW(WN, 2, (RS) * SLOT); SB(); M1(X, W, 2, 1); if (RD) RDX(XN, 2, (RS) * SLOT); SB(); M1(X, W, 3, 1); if (DMA) PC1(DS, 1); SB(); \
    M1(X, W, 0, 2); if (RD) RDW(WN, 3, (RS) * SLOT); SB(); M1(X, W, 1, 2); if (RD) RDX(XN, 3, (RS) * SLOT); SB(); M1(X, W, 2, 2); if (RD) RDW(WN, 4, (RS) * SLOT); SB(); M1(X, W, 3, 2); if (DMA) PC1(DS, 2); SB(); \
    M1(X, W, 0, 3); if (RD) RDW(WN, 5, (RS) * SLOT); SB(); M1(X, W, 1, 3); SB(); M1(X, W, 2, 3); if (RD) RDW(WN, 6, (RS) * SLOT); SB(); M1(X, W, 3, 3); if (DMA) PC1(DS, 3); SB(); \
    M1(X, W, 0, 4); SB(); M1(X, W, 1, 4); SB(); M1(X, W, 2, 4); SB(); M1(X, W, 3, 4); if (DMA) PC1(DS, 4); SB(); \
    M1(X, W, 0, 5); SB(); M1(X, W, 1, 5); SB(); M1(X, W, 2, 5); SB(); M1(X, W, 3, 5); if (DMA) { PC1(DS, 5); dma4_ahead<5 * 32 * F_ * 2>(mpf, vpf, rW, wso); } SB(); \
    M1(X, W, 0, 6); SB(); M1(X, W, 1, 6); SB(); M1(X, W, 2, 6); SB(); M1(X, W, 3, 6); SB(); } while (0)
  WAIT_BAR(3);
#pragma unroll
  for (int m = 0; m < 4; ++m) RDX(xa, m, 0);
#pragma unroll
  for (int n = 0; n < 7; ++n) RDW(wa, n, 0);
#define EV(SC, SN, W_, RD, DMA) do { WAIT_BAR(W_); SB(); STEP1(xa, wa, xb, wb, RD, SN, DMA, SC); } while (0)
#define OD(SC, SN, W_, RD, DMA) do { WAIT_BAR(W_); SB(); STEP1(xb, wb, xa, wa, RD, SN, DMA, SC); } while (0)
#define PR1(i) do { if constexpr ((i) == 0 || (((G1PAT >> (i)) & 1) != ((G1PAT >> ((i) - 1 < 0 ? 0 : (i) - 1)) & 1))) __builtin_amdgcn_s_setprio(((G1PAT >> (i)) & 1) ? 2 : 0); } while (0)
#define SIX() do { PR1(0); EV(0, 1, 8, true, true); PR1(1); OD(1, 2, 8, true, true); PR1(2); EV(2, 0, 8, true, true); PR1(3); OD(0, 1, 8, true, true); PR1(4); EV(1, 2, 8, true, true); PR1(5); OD(2, 0, 8, true, true); } while (0)
  for (int it = 0; it < 4; ++it) { SIX(); STAMP(4 + it); }
  EV(0, 1, 8, true, true); OD(1, 2, 8, true, true); EV(2, 0, 8, true, true); OD(0, 1, 8, true, true); EV(1, 2, 8, true, true);
  OD(2, 0, 8, true, false); EV(0, 1, 0, true, false); OD(1, 2, 0, false, false);
  STAMP(1);
  if (PROBE == 2) probe = __builtin_amdgcn_s_memrealtime() - t_in;
  __builtin_amdgcn_s_setprio(PRIO_GE);
  w2_weights_early(gi, w2, lds, w);
  const int le = lane_id(), ge = le >> 4;
  float r2[4];
#pragma unroll
  for (int m = 0; m < 4; ++m) r2[m] = *(const LAS float*)(lds + L_R2 + (64 * wm + 16 * m + (le & 15)) * 4);
  const __amdgpu_buffer_rsrc_t rH = mkrsrc(ws + WS_H, 112 * 128 * 1024);
  const int cch = ((ge & 1) << 1) | (ge >> 1);
  unsigned last[4][2];
  float c1v = -2.0f * 0.7978845608028654f * 1.4426950408889634f; PIN(c1v);
#pragma unroll
  for (int n = 0; n < 7; ++n) {
    const int nb = n < 6 ? 6 * wn + n : 12 + wn;
    const u32x2 braw = *(const LAS u32x2*)(lds + L_B1 + (16 * nb + 4 * ge) * 2);
    const f32x4 bb = {lo2f(braw[0]), hi2f(braw[0]), lo2f(braw[1]), hi2f(braw[1])};
#pragma unroll
    for (int m = 0; m < 4; ++m) acc[m][n] = gelu4(acc[m][n], r2[m], bb, c1v);
    if (n < 6) { if (n & 1) {
#pragma unroll
        for (int m = 0; m < 4; ++m) {
          const u32x4 ch = chunk_of(cvtpk(acc[m][n - 1][0], acc[m][n - 1][1]), cvtpk(acc[m][n - 1][2], acc[m][n - 1][3]), cvtpk(acc[m][n][0], acc[m][n][1]), cvtpk(acc[m][n][2], acc[m][n][3]));
          st_wt16(rH, ((7 * j + 3 * wn + (n >> 1)) * 128 + 8 * p + 4 * wm + m) * 1024 + cch * 256 + (le & 15) * 16, ch);
        } }
    } else {
#pragma unroll
      for (int m = 0; m < 4; ++m) { last[m][0] = cvtpk(acc[m][n][0], acc[m][n][1]); last[m][1] = cvtpk(acc[m][n][2], acc[m][n][3]); }
    }
  }
#pragma unroll
  for (int m = 0; m < 4; m += 2) {
    const u32x4 ch = chunk_of(last[m][0], last[m][1], last[m + 1][0], last[m + 1][1]);
    st_wt16(rH, ((7 * j + 6) * 128 + 8 * p + 4 * wm + m + (ge & 1)) * 1024 + (2 * wn + (ge >> 1)) * 256 + (le & 15) * 16, ch);
  }
  STAMP(2);
  __builtin_amdgcn_s_setprio(PRIO_G2);
  asm volatile("s_waitcnt vmcnt(0)\n\ts_barrier" ::: "memory");
  if (w == 0 && lane_id() == 0) signal(ws, FL_H + p * 16 + j, tag);
  STAMP(3);
#undef RDX
#undef RDW
#undef EV
#undef OD
  return tag;
}

DI void role_w2(unsigned long long t_in, unsigned long long& probe, int gi, const u16* __restrict__ w2, const u16* __restrict__ wo, const u16* __restrict__ b2, const u16* __restrict__ x, u16* __restrict__ out, char* __restrict__ ws, LAS char* lds, const int w, const u64 tag) {
  const int l = lane_id();
  const int xcd = gi & 7, idx = gi >> 3, kh = xcd & 1, p = (xcd >> 1) * 4 + (idx & 3), nt = idx >> 2;
  const int partner = gi ^ 1;
  const int g = l >> 4, wm = w >> 1, wn = w & 1; const int stamp_base = 512; (void)stamp_base;
  const unsigned lds0 = (unsigned)(uintptr_t)lds;
  constexpr int SLOT = 16384;
#define AINC_ 131072
#define WINC_ 65536
  const __amdgpu_buffer_rsrc_t rA = mkrsrc(ws + WS_H, 144 * 128 * 1024);
  __amdgpu_buffer_rsrc_t rW = mkrsrc((const char*)w2 - 1024, F_ * D_ * 2 + 1024);
  const unsigned vlin = l * 16;
  const unsigned vw0 = wdma_off(l, w, 2048) + nt * 256 + w * 8 * 2048 + 1024, vw1 = vw0 + 128 - 1024;
  if (gi == 0 && w == 0) {
    for (unsigned spins = 0; spins < (1u << 16); ++spins) {
      bool ok = true;
#pragma unroll
      for (int i = 0; i < 8; ++i) ok &= __hip_atomic_load(flagp(ws, FL_RD + 64 * i + l), __ATOMIC_RELAXED, __HIP_MEMORY_SCOPE_AGENT) == tag;
      if (__all(ok)) break;
      __builtin_amdgcn_s_sleep(2);
    }
    if (l == 0) signal(ws, FL_E, tag);
  }
  if (w == 0) await(ws, l < 8 ? FL_H + p * 16 + 8 * kh + l : -1, tag);
  asm volatile("s_barrier" ::: "memory");
  STAMP(0);
  if (PROBE == 3) probe = __builtin_amdgcn_s_memrealtime() - t_in;
  unsigned aso = (56 * kh * 128 + 8 * p + 2 * w) * 1024 - AINC_, wso = 56 * kh * 65536 + 3 * 65536;
  const unsigned ma = lds0 + w * 2048, mw = lds0 + 8192 + w * 2048;
#define AINC 131072
#define WINC 65536
#define PC2(SL, i) do { if constexpr ((i) == 0) dma1a_c<(SL) * SLOT, AINC>(ma, vlin, rA, aso); else if constexpr ((i) == 1) dma1b_c<1024>(vlin, rA, aso); \
    else if constexpr ((i) == 2) dma1a<(SL) * SLOT, WINC>(mw, vw0, rW, wso); else dma1b<1024>(vw1, rW, wso); } while (0)
#define STG2(SL) do { PC2(SL, 0); PC2(SL, 1); PC2(SL, 2); PC2(SL, 3); } while (0)
  PC2(0, 0); PC2(0, 1); PC2(1, 0); PC2(1, 1); PC2(2, 0); PC2(2, 1); PC2(3, 0); PC2(3, 1); STG2(4);
  f32x4 acc[4][4];
#pragma unroll
  for (int m = 0; m < 4; ++m)
#pragma unroll
    for (int n = 0; n < 4; ++n) acc[m][n] = (f32x4){0.f, 0.f, 0.f, 0.f};
  const LAS char* xrd = lds + 4 * wm * 1024 + l * 16;
  const LAS char* wrb[4];
#pragma unroll
  for (int i = 0; i < 4; ++i) wrb[i] = lds + 8192 + (g * 2 + wn) * 1024 + wrd_off(l, i);
  bf16x8 xa[4], wa[4], xb[4], wb[4];
#define RDX(X, m, so) X[m] = ldsfrag(xrd + (so) + (m) * 1024)
#define RDW(W, n, so) W[n] = ldstr(wrb[n] + (so), wrb[n] + (so) + 512)
#define M2(X, W, m, n) acc[m][n] = MFMA16(W[n], X[m], acc[m][n])
#define STEP2(X, W, XN, WN, RD, RS, DMA, DS) do { \
    M2(X, W, 0, 0); if (RD) RDW(WN, 0, (RS) * SLOT); SB(); M2(X, W, 1, 0); if (RD) RDX(XN, 0, (RS) * SLOT); SB(); M2(X, W, 2, 0); if (RD) RDX(XN, 1, (RS) * SLOT); SB(); M2(X, W, 3, 0); if (DMA) PC2(DS, 0); SB(); \
    M2(X, W, 0, 1); if (RD) RDW(WN, 1, (RS) * SLOT); SB(); M2(X, W, 1, 1); if (RD) RDX(XN, 2, (RS) * SLOT); SB(); M2(X, W, 2, 1); if (RD) RDX(XN, 3, (RS) * SLOT); SB(); M2(X, W, 3, 1); if (DMA) PC2(DS, 1); SB(); \
    M2(X, W, 0, 2); if (RD) RDW(WN, 2, (RS) * SLOT); SB(); M2(X, W, 1, 2); if (RD) RDW(WN, 3, (RS) * SLOT); SB(); M2(X, W, 2, 2); SB(); M2(X, W, 3, 2); if (DMA) PC2(DS, 2); SB(); \
    M2(X, W, 0, 3); SB(); M2(X, W, 1, 3); SB(); M2(X, W, 2, 3); SB(); M2(X, W, 3, 3); if (DMA) PC2(DS, 3); SB(); } while (0)
  WAIT_BAR(6);
#pragma unroll
  for (int i = 0; i < 4; ++i) { RDX(xa, i, 0); RDW(wa, i, 0); }
#define EV(SC, SN, W_, RD, DMA) do { WAIT_BAR(W_); SB(); STEP2(xa, wa, xb, wb, RD, SN, DMA, SC); } while (0)
#define OD(SC, SN, W_, RD, DMA) do { WAIT_BAR(W_); SB(); STEP2(xb, wb, xa, wa, RD, SN, DMA, SC); } while (0)
#define PR2(i) do { if constexpr ((i) == 0 || (((G2PAT >> (i)) & 1) != ((G2PAT >> ((i) - 1 < 0 ? 0 : (i) - 1)) & 1))) __builtin_amdgcn_s_setprio(((G2PAT >> (i)) & 1) ? 2 : 0); } while (0)
  u64 oflag = 0;
  for (int it = 0; it < 6; ++it) {
    STAMP(4 + it);
    PR2(0);
    EV(0, 1, 12, true, true);
    if (it == 5) {
      if (w == 0) {
        asm volatile("" : "+v"(oflag));
        if (!__all(l >= 8 || oflag == tag)) await(ws, l < 8 ? FL_O + (2 * p + (l >> 2)) * 8 + 4 * kh + (l & 3) : -1, tag);
      }
      rW = mkrsrc((const char*)wo - 1024, D_ * D_ * 2 + 1024);
      aso = ((112 + 16 * kh) * 128 + 8 * p + 2 * w) * 1024 - 131072; wso = 16 * kh * 65536 - 65536;
    }
    PR2(1); OD(1, 2, 12, true, true); PR2(2); EV(2, 3, 12, true, true); PR2(3); OD(3, 4, 12, true, true); PR2(4); EV(4, 0, 12, true, true);
    PR2(5); OD(0, 1, 12, true, true); PR2(6); EV(1, 2, 12, true, true);
    if (it == 4 && w == 0) {
      const GAS u64* fo = flagp(ws, FL_O + (2 * p + ((l >> 2) & 1)) * 8 + 4 * kh + (l & 3));
      asm volatile("global_load_dwordx2 %0, %1, off sc1" : "=&v"(oflag) : "v"(fo));
    }
    PR2(7); OD(2, 3, 12, true, true); PR2(8); EV(3, 4, 12, true, true); PR2(9); OD(4, 0, 12, true, true);
  }
  EV(0, 1, 12, true, true); OD(1, 2, 12, true, true); EV(2, 3, 12, true, true); OD(3, 4, 12, true, true); EV(4, 0, 12, true, true); OD(0, 1, 12, true, true); EV(1, 2, 12, true, true);
  u32x2 xres[2][4], bres[4];
  { const int le = lane_id();
    const u16* xp = x + (size_t)(128 * p + 64 * wm + 32 * kh + (le & 15)) * D_ + 128 * nt + 64 * wn + 4 * (le >> 4); const u16* xq = xp + 16 * D_; const u16* bp = b2 + 128 * nt + 64 * wn + 4 * (le >> 4);
    asm volatile("global_load_dwordx2 %0, %8, off\n\tglobal_load_dwordx2 %1, %8, off offset:32\n\tglobal_load_dwordx2 %2, %8, off offset:64\n\tglobal_load_dwordx2 %3, %8, off offset:96\n\t"
                 "global_load_dwordx2 %4, %9, off\n\tglobal_load_dwordx2 %5, %9, off offset:32\n\tglobal_load_dwordx2 %6, %9, off offset:64\n\tglobal_load_dwordx2 %7, %9, off offset:96"
                 : "=&v"(xres[0][0]), "=&v"(xres[0][1]), "=&v"(xres[0][2]), "=&v"(xres[0][3]), "=&v"(xres[1][0]), "=&v"(xres[1][1]), "=&v"(xres[1][2]), "=&v"(xres[1][3]) : "v"(xp), "v"(xq));
    asm volatile("global_load_dwordx2 %0, %4, off\n\tglobal_load_dwordx2 %1, %4, off offset:32\n\tglobal_load_dwordx2 %2, %4, off offset:64\n\tglobal_load_dwordx2 %3, %4, off offset:96"
                 : "=&v"(bres[0]), "=&v"(bres[1]), "=&v"(bres[2]), "=&v"(bres[3]) : "v"(bp)); }
  OD(2, 3, 24, true, false); EV(3, 4, 20, true, false); OD(4, 0, 16, true, false); EV(0, 1, 12, true, false); OD(1, 2, 12, false, false);
  STAMP(1);
  if (PROBE == 4) probe = __builtin_amdgcn_s_memrealtime() - t_in;
  if (kh) {
#pragma unroll
    for (int n = 0; n < 4; ++n) { f32x4 t = acc[0][n]; acc[0][n] = acc[2][n]; acc[2][n] = t; t = acc[1][n]; acc[1][n] = acc[3][n]; acc[3][n] = t; }
#pragma unroll
    for (int n = 0; n < 4; ++n) { PIN(acc[0][n]); PIN(acc[1][n]); PIN(acc[2][n]); PIN(acc[3][n]); }
  }
  const int le = lane_id(), ge = le >> 4;
  const int token0 = 128 * p + 64 * wm + 32 * kh + (le & 15);
  {
    const __amdgpu_buffer_rsrc_t rP = mkrsrc(ws + WS_PART + (size_t)gi * 32768, 16384);
#pragma unroll
    for (int m = 0; m < 2; ++m)
#pragma unroll
      for (int n2 = 0; n2 < 2; ++n2)
        st_wt16(rP, ((w * 2 + m) * 2 + n2) * 1024 + le * 16, (u32x4){cvtpk(acc[2 + m][2 * n2][0], acc[2 + m][2 * n2][1]), cvtpk(acc[2 + m][2 * n2][2], acc[2 + m][2 * n2][3]),
                                                                       cvtpk(acc[2 + m][2 * n2 + 1][0], acc[2 + m][2 * n2 + 1][1]), cvtpk(acc[2 + m][2 * n2 + 1][2], acc[2 + m][2 * n2 + 1][3])});
    asm volatile("s_waitcnt vmcnt(0)\n\ts_barrier" : "+v"(xres[0][0]), "+v"(xres[0][1]), "+v"(xres[0][2]), "+v"(xres[0][3]), "+v"(xres[1][0]), "+v"(xres[1][1]), "+v"(xres[1][2]), "+v"(xres[1][3]),
                 "+v"(bres[0]), "+v"(bres[1]), "+v"(bres[2]), "+v"(bres[3]) :: "memory");
    if (w == 0) { if (le == 0) signal(ws, FL_X + gi, tag); await(ws, FL_X + partner, tag); }
    asm volatile("s_barrier" ::: "memory");
  }
  STAMP(2);
  const __amdgpu_buffer_rsrc_t rPP = mkrsrc(ws + WS_PART + (size_t)partner * 32768, 16384);
  u32x4 pv[2][2];
#pragma unroll
  for (int m = 0; m < 2; ++m)
#pragma unroll
    for (int n2 = 0; n2 < 2; ++n2) pv[m][n2] = __builtin_amdgcn_raw_buffer_load_b128(rPP, ((w * 2 + m) * 2 + n2) * 1024 + le * 16, 0, 16);
#pragma unroll
  for (int m = 0; m < 2; ++m) {
    unsigned d[4][2];
#pragma unroll
    for (int n = 0; n < 4; ++n) {
      const unsigned p0 = pv[m][n >> 1][2 * (n & 1)], p1 = pv[m][n >> 1][2 * (n & 1) + 1];
      const f32x4 a4 = acc[m][n];
      d[n][0] = cvtpk(a4[0] + lo2f(p0) + lo2f(bres[n][0]) + lo2f(xres[m][n][0]), a4[1] + hi2f(p0) + hi2f(bres[n][0]) + hi2f(xres[m][n][0]));
      d[n][1] = cvtpk(a4[2] + lo2f(p1) + lo2f(bres[n][1]) + lo2f(xres[m][n][1]), a4[3] + hi2f(p1) + hi2f(bres[n][1]) + hi2f(xres[m][n][1]));
    }
    const int cch = ((ge & 1) << 1) | (ge >> 1);
#pragma unroll
    for (int n2 = 0; n2 < 2; ++n2) {
      const u32x4 ch = chunk_of(d[2 * n2][0], d[2 * n2][1], d[2 * n2 + 1][0], d[2 * n2 + 1][1]);
      st_wt16(mkrsrc(out, S_ * D_ * 2), ((token0 + 16 * m) * D_ + 128 * nt + 64 * wn + 32 * n2 + 8 * cch) * 2, ch);
    }
  }
  STAMP(3);
#undef RDX
#undef RDW
}

constexpr int LDS_BYTES = 81920;
#define MODE 0
__global__ __launch_bounds__(256, 2) void k_main(const u16* __restrict__ x, const u16* __restrict__ wq, const u16* __restrict__ wk, const u16* __restrict__ wv, const u16* __restrict__ wo,
                                                  const u16* __restrict__ w1, const u16* __restrict__ b1, const u16* __restrict__ w2, const u16* __restrict__ b2, u16* __restrict__ out, char* __restrict__ ws) {
  extern __shared__ __attribute__((aligned(16))) char lds_[];
  LAS char* lds = (LAS char*)lds_;
  const int w = __builtin_amdgcn_readfirstlane(threadIdx.x >> 6);
  const unsigned long long t_in = __builtin_amdgcn_s_memrealtime(); unsigned long long probe = 0;
  u64 eraw;
  { const GAS u64* ep = flagp(ws, FL_E); asm volatile("global_load_dwordx2 %0, %1, off sc1" : "=&v"(eraw) : "v"(ep)); }
  if ((blockIdx.x >> 8) == 0) {
    __builtin_amdgcn_s_setprio(PRIO_Q);
    const u64 tag = phase0(blockIdx.x, x, wk, wv, ws, lds, w, eraw);
    role_attn(blockIdx.x, wq, w2, wo, ws, lds, w, tag);
  } else {
    __builtin_amdgcn_s_setprio(PRIO_G1);
    const u64 tag = role_w1(t_in, probe, blockIdx.x & 255, wq, w1, b1, w2, ws, lds, w, eraw);
    role_w2(t_in, probe, blockIdx.x & 255, w2, wo, b2, x, out, ws, lds, w, tag);
    if (PROBE == 5) probe = __builtin_amdgcn_s_memrealtime() - t_in;
    if (blockIdx.x == 256 + 17 && PROBE > 0) { const unsigned long long t0 = __builtin_amdgcn_s_memrealtime(); while (__builtin_amdgcn_s_memrealtime() - t0 < probe) __builtin_amdgcn_s_sleep(1); }
  }
}

extern "C" void kernel_launch(void* const* d_in, const int* in_sizes, int n_in, void* d_out, int out_size, void* d_ws, size_t ws_size, hipStream_t stream) {
  const u16* x = (const u16*)d_in[0]; const u16* wq = (const u16*)d_in[1]; const u16* wk = (const u16*)d_in[2];
  const u16* wv = (const u16*)d_in[3]; const u16* wo = (const u16*)d_in[4];
  const u16* w1 = (const u16*)d_in[6]; const u16* b1 = (const u16*)d_in[7]; const u16* w2 = (const u16*)d_in[8]; const u16* b2 = (const u16*)d_in[9];
  (void)hipFuncSetAttribute((const void*)k_main, hipFuncAttributeMaxDynamicSharedMemorySize, LDS_BYTES);
  k_main<<<512, 256, LDS_BYTES, stream>>>(x, wq, wk, wv, wo, w1, b1, w2, b2, (u16*)d_out, (char*)d_ws);
}
```

```cpp
#include <hip/hip_runtime.h>
#include <stdint.h>

typedef unsigned short u16;
typedef __bf16 bf16x8 __attribute__((ext_vector_type(8)));
typedef __bf16 bf16x2 __attribute__((ext_vector_type(2)));
typedef short s16x8 __attribute__((ext_vector_type(8)));
typedef float f32x4 __attribute__((ext_vector_type(4)));
typedef float f32x2 __attribute__((ext_vector_type(2)));
typedef unsigned u32x4 __attribute__((ext_vector_type(4)));
typedef unsigned u32x2 __attribute__((ext_vector_type(2)));
#define LAS __attribute__((address_space(3)))
#define GAS __attribute__((address_space(1)))
#define DI __device__ __forceinline__
#define SB() __builtin_amdgcn_sched_barrier(0)
#define PIN(x) asm volatile("" : "+v"(x))
#define MFMA16(a, b, c) __builtin_amdgcn_mfma_f32_16x16x32_bf16((a), (b), (c), 0, 0, 0)
#define BF(x) __builtin_bit_cast(bf16x8, x)

constexpr int S_ = 2048, D_ = 1024, F_ = 3584;
constexpr size_t WS_FLAGS = 0;
constexpr size_t WS_R2   = 131072;
constexpr size_t WS_KV   = WS_R2 + 8192;
constexpr size_t WS_XN   = WS_KV + 262144;
constexpr size_t WS_H    = WS_XN + 4194304;
constexpr size_t WS_O    = WS_H + (size_t)112 * 128 * 1024;
constexpr size_t WS_PART = WS_O + 4194304;

constexpr float QSCALE = 0.17677669529663687f * 1.4426950408889634f;

DI int lane_id() { int l; asm volatile("v_mbcnt_lo_u32_b32 %0, -1, 0\n\tv_mbcnt_hi_u32_b32 %0, -1, %0" : "=v"(l)); return l; }
DI unsigned cvtpk(float lo, float hi) { f32x2 v = {lo, hi}; bf16x2 b = __builtin_convertvector(v, bf16x2); return __builtin_bit_cast(unsigned, b); }
DI float lo2f(unsigned u) { return __uint_as_float(u << 16); }
DI float hi2f(unsigned u) { return __uint_as_float(u & 0xffff0000u); }
DI __amdgpu_buffer_rsrc_t mkrsrc(const void* p, unsigned bytes) { return __builtin_amdgcn_make_buffer_rsrc((void*)p, 0, bytes, 0x00020000); }
DI void dma16(unsigned lds_addr, unsigned voff, __amdgpu_buffer_rsrc_t rsrc, unsigned soff) {
  asm volatile("s_mov_b32 m0, %0\n\ts_nop 0\n\tbuffer_load_dwordx4 %1, %2, %3 offen lds" :: "s"(lds_addr), "v"(voff), "s"(rsrc), "s"(soff));
}
template <int IMM, int INC> DI void dma1a(unsigned mbase, unsigned v0, __amdgpu_buffer_rsrc_t rsrc, unsigned& soff) {
  asm volatile("s_add_u32 m0, %2, %4\n\ts_add_u32 %0, %0, %5\n\tbuffer_load_dwordx4 %1, %3, %0 offen lds" : "+s"(soff) : "v"(v0), "s"(mbase), "s"(rsrc), "i"(IMM), "i"(INC) : "scc");
}
template <int OFF> DI void dma1b(unsigned v0, __amdgpu_buffer_rsrc_t rsrc, unsigned soff) {
  asm volatile("buffer_load_dwordx4 %0, %1, %2 offen offset:%3 lds" :: "v"(v0), "s"(rsrc), "s"(soff), "i"(OFF));
}
template <int IMM, int INC> DI void dma1a_c(unsigned mbase, unsigned v0, __amdgpu_buffer_rsrc_t rsrc, unsigned& soff) {
  asm volatile("s_add_u32 m0, %2, %4\n\ts_add_u32 %0, %0, %5\n\tbuffer_load_dwordx4 %1, %3, %0 offen sc1 lds" : "+s"(soff) : "v"(v0), "s"(mbase), "s"(rsrc), "i"(IMM), "i"(INC) : "scc");
}
template <int OFF> DI void dma1b_c(unsigned v0, __amdgpu_buffer_rsrc_t rsrc, unsigned soff) {
  asm volatile("buffer_load_dwordx4 %0, %1, %2 offen offset:%3 sc1 lds" :: "v"(v0), "s"(rsrc), "s"(soff), "i"(OFF));
}
DI void dma4(unsigned lds_addr, unsigned voff, __amdgpu_buffer_rsrc_t rsrc, unsigned soff) {
  asm volatile("s_mov_b32 m0, %0\n\ts_nop 0\n\tbuffer_load_dword %1, %2, %3 offen lds" :: "s"(lds_addr), "v"(voff), "s"(rsrc), "s"(soff));
}
DI void dma4_c(unsigned lds_addr, unsigned voff, __amdgpu_buffer_rsrc_t rsrc, unsigned soff) {
  asm volatile("s_mov_b32 m0, %0\n\ts_nop 0\n\tbuffer_load_dword %1, %2, %3 offen sc1 lds" :: "s"(lds_addr), "v"(voff), "s"(rsrc), "s"(soff));
}
template <int ADD> DI void dma4_ahead(unsigned lds_addr, unsigned voff, __amdgpu_buffer_rsrc_t rsrc, unsigned soff) {
  unsigned t;
  asm volatile("s_mov_b32 m0, %1\n\ts_add_u32 %0, %4, %5\n\tbuffer_load_dword %2, %3, %0 offen lds" : "=&s"(t) : "s"(lds_addr), "v"(voff), "s"(rsrc), "s"(soff), "i"(ADD) : "scc");
}
#define WAIT_BAR(N) asm volatile("s_waitcnt vmcnt(" #N ") lgkmcnt(0)\n\ts_barrier" ::: "memory")
DI bf16x8 ldsfrag(const LAS char* p) { return *(const LAS bf16x8*)p; }
typedef short v4i16_t __attribute__((ext_vector_type(4)));
DI bf16x8 ldstr(const LAS char* p0, const LAS char* p1) {
  v4i16_t a = __builtin_amdgcn_ds_read_tr16_b64_v4i16((LAS v4i16_t*)p0);
  v4i16_t b = __builtin_amdgcn_ds_read_tr16_b64_v4i16((LAS v4i16_t*)p1);
  s16x8 v = __builtin_shufflevector(a, b, 0, 1, 2, 3, 4, 5, 6, 7);
  return __builtin_bit_cast(bf16x8, v);
}
DI void st_wt16(__amdgpu_buffer_rsrc_t r, unsigned voff, u32x4 v) { __builtin_amdgcn_raw_buffer_store_b128(v, r, voff, 0, 17); }
DI u32x4 chunk_of(unsigned x0, unsigned x1, unsigned y0, unsigned y1) {
  auto s0 = __builtin_amdgcn_permlane16_swap(x0, y0, false, false);
  auto s1 = __builtin_amdgcn_permlane16_swap(x1, y1, false, false);
  return (u32x4){s0[0], s1[0], s0[1], s1[1]};
}
DI unsigned wdma_off(int l, int kblk, int ldbytes) {
  const int row = l >> 3, c8 = l & 7, sw = ((row >> 1) & 1) | ((kblk & 1) << 1);
  return row * ldbytes + (((c8 >> 1) ^ sw) * 32) + (c8 & 1) * 16;
}
DI unsigned wrd_off(int l, int seg) {
  const int g = l >> 4, q = (l & 15) >> 2, sw = ((q >> 1) & 1) | ((g & 1) << 1);
  return q * 128 + ((seg ^ sw) * 32) + (l & 3) * 8;
}
DI float xsum4(float v) {
  auto a = __builtin_amdgcn_permlane16_swap(__float_as_uint(v), __float_as_uint(v), false, false); v = __uint_as_float(a[0]) + __uint_as_float(a[1]);
  auto b = __builtin_amdgcn_permlane32_swap(__float_as_uint(v), __float_as_uint(v), false, false); return __uint_as_float(b[0]) + __uint_as_float(b[1]);
}
DI float xmax4(float v) {
  auto a = __builtin_amdgcn_permlane16_swap(__float_as_uint(v), __float_as_uint(v), false, false); v = fmaxf(__uint_as_float(a[0]), __uint_as_float(a[1]));
  auto b = __builtin_amdgcn_permlane32_swap(__float_as_uint(v), __float_as_uint(v), false, false); return fmaxf(__uint_as_float(b[0]), __uint_as_float(b[1]));
}

typedef unsigned long long u64;
constexpr int FL_E = 0, FL_RD = 16, FL_XN = 528, FL_KV = 784, FL_H = 1040, FL_O = 1296, FL_X = 1552;
DI GAS u64* flagp(char* ws, int i) { return (GAS u64*)(ws + WS_FLAGS + (size_t)i * 64); }
DI void signal(char* ws, int i, u64 tag) { __hip_atomic_store(flagp(ws, i), tag, __ATOMIC_RELAXED, __HIP_MEMORY_SCOPE_AGENT); }
DI u64 take_tag(char* ws, u64 eraw, int w) {
  asm volatile("" : "+v"(eraw));
  const u64 tag = (((u64)(unsigned)__builtin_amdgcn_readfirstlane((unsigned)(eraw >> 32))) << 32 | (unsigned)__builtin_amdgcn_readfirstlane((unsigned)eraw)) + 1;
  if (w == 0 && lane_id() == 0) signal(ws, FL_RD + blockIdx.x, tag);
  return tag;
}
DI void await(char* ws, int idx, u64 tag) {
  GAS u64* f = flagp(ws, idx < 0 ? FL_E : idx);
  for (unsigned spins = 0; spins < (1u << 20); ++spins) {
    const u64 v = __hip_atomic_load(f, __ATOMIC_RELAXED, __HIP_MEMORY_SCOPE_AGENT);
    if (__all(idx < 0 || v == tag)) break;
    __builtin_amdgcn_s_sleep(2);
  }
  asm volatile("" ::: "memory");
}

template <int N> DI void await_n(char* ws, int idx, u64 tag) {
  for (unsigned spins = 0; spins < (1u << 20); ++spins) {
    bool ok = true;
#pragma unroll
    for (int i = 0; i < N; ++i) ok &= __hip_atomic_load(flagp(ws, idx + 64 * i), __ATOMIC_RELAXED, __HIP_MEMORY_SCOPE_AGENT) == tag;
    if (__all(ok)) break;
    __builtin_amdgcn_s_sleep(2);
  }
  asm volatile("" ::: "memory");
}

DI u64 phase0(int a, const u16* __restrict__ x, const u16* __restrict__ wk, const u16* __restrict__ wv, char* __restrict__ ws, LAS char* lds, const int w, const u64 eraw) {
  const int l = lane_id(), tid = w * 64 + l;
  LAS float* part = (LAS float*)(lds + 65536);
  LAS float* fin = (LAS float*)(lds + 65536 + 8192);
  LAS float* red = (LAS float*)(lds + 65536 + 8192 + 2048);
  const int rb = a & 127, hf = a >> 7;
  const int r = l & 15, c = l >> 4;
  u32x4 xv[8];
  { const u16* xr = x + (size_t)(rb * 16 + r) * D_ + w * 256 + c * 8;
#define XL(i) asm volatile("global_load_dwordx4 %0, %1, off offset:" #i "*64" : "=&v"(xv[i]) : "v"(xr))
    XL(0); XL(1); XL(2); XL(3); XL(4); XL(5); XL(6); XL(7); }
  const unsigned lds0 = (unsigned)(uintptr_t)lds;
  {
    __amdgpu_buffer_rsrc_t rw = mkrsrc(hf ? wv : wk, 65536);
#pragma unroll
    for (int i = 0; i < 16; ++i) dma16(lds0 + w * 16384 + i * 1024, l * 16, rw, w * 16384 + i * 1024);
  }
  asm volatile("s_waitcnt vmcnt(16)" : "+v"(xv[0]), "+v"(xv[1]), "+v"(xv[2]), "+v"(xv[3]), "+v"(xv[4]), "+v"(xv[5]), "+v"(xv[6]), "+v"(xv[7]));
  const u64 tag = take_tag(ws, eraw, w);
  f32x2 v[32];
#pragma unroll
  for (int i = 0; i < 8; ++i)
#pragma unroll
    for (int j = 0; j < 4; ++j) v[4 * i + j] = (f32x2){lo2f(xv[i][j]), hi2f(xv[i][j])};
  f32x2 s2 = (v[0] + v[1]) + (v[2] + v[3]);
#pragma unroll
  for (int i = 4; i < 32; i += 4) s2 += (v[i] + v[i + 1]) + (v[i + 2] + v[i + 3]);
  const float s = xsum4(s2[0] + s2[1]);
  if (c == 0) red[w * 16 + r] = s;
  asm volatile("s_waitcnt lgkmcnt(0)\n\ts_barrier" ::: "memory");
  const float mean = ((red[r] + red[16 + r]) + (red[32 + r] + red[48 + r])) * (1.0f / D_);
  f32x2 q2 = {0.f, 0.f}, q3 = {0.f, 0.f};
#pragma unroll
  for (int i = 0; i < 32; i += 2) { v[i] -= mean; v[i + 1] -= mean; q2 += v[i] * v[i]; q3 += v[i + 1] * v[i + 1]; }
  const float q = xsum4((q2[0] + q2[1]) + (q3[0] + q3[1]));
  if (c == 0) red[64 + w * 16 + r] = q;
  asm volatile("s_waitcnt lgkmcnt(0)\n\ts_barrier" ::: "memory");
  const float var = ((red[64 + r] + red[80 + r]) + (red[96 + r] + red[112 + r])) * (1.0f / D_);
  const float rstd = 1.0f / sqrtf(var + 1e-5f);
#pragma unroll
  for (int i = 0; i < 8; ++i)
#pragma unroll
    for (int j = 0; j < 4; ++j) { const f32x2 t = v[4 * i + j] * rstd; xv[i][j] = cvtpk(t[0], t[1]); }
  const __amdgpu_buffer_rsrc_t rXN = mkrsrc(ws + WS_XN, 4194304);
#pragma unroll
  for (int i = 0; i < 4; ++i) st_wt16(rXN, ((8 * w + 2 * i + hf) * 128 + rb) * 1024 + l * 16, hf ? xv[2 * i + 1] : xv[2 * i]);
  if (hf == 0 && w == 0 && c == 0) __hip_atomic_store((GAS float*)(ws + WS_R2) + rb * 16 + r, 1.0f / sqrtf(var * rstd * rstd + 1e-5f), __ATOMIC_RELAXED, __HIP_MEMORY_SCOPE_AGENT);
  asm volatile("s_waitcnt vmcnt(0)\n\ts_barrier" ::: "memory");
  if (tid == 0) signal(ws, FL_XN + a, tag);
  f32x4 acc[2] = {{0.f, 0.f, 0.f, 0.f}, {0.f, 0.f, 0.f, 0.f}};
  const LAS char* wb = (const LAS char*)lds + w * 16384 + (8 * c + ((l & 15) >> 2)) * 64 + (l & 3) * 8;
#pragma unroll
  for (int i = 0; i < 8; ++i)
#pragma unroll
    for (int b = 0; b < 2; ++b) {
      bf16x8 wf = ldstr(wb + i * 2048 + b * 32, wb + i * 2048 + b * 32 + 256);
      acc[b] = MFMA16(BF(xv[i]), wf, acc[b]);
    }
#pragma unroll
  for (int b = 0; b < 2; ++b)
#pragma unroll
    for (int j = 0; j < 4; ++j) part[(w * 16 + 4 * c + j) * 32 + 16 * b + r] = acc[b][j];
  asm volatile("s_waitcnt lgkmcnt(0)\n\ts_barrier" ::: "memory");
#pragma unroll
  for (int u = 0; u < 2; ++u) { const int e = tid + 256 * u; fin[e] = (part[e] + part[512 + e]) + (part[1024 + e] + part[1536 + e]); }
  asm volatile("s_waitcnt lgkmcnt(0)\n\ts_barrier" ::: "memory");
  if (w == 0) {
    float o[8];
    unsigned dst = (rb >> 1) * 4096;
    const int half = rb & 1;
    if (hf == 0) { const int kb = l >> 5, g = (l >> 3) & 3, rr = l & 7;
      const int row = 8 * half + rr, key = 8 * (row >> 2) + 4 * kb + (row & 3) - 16 * half;
#pragma unroll
      for (int j = 0; j < 8; ++j) o[j] = fin[key * 32 + 16 * (j >> 2) + 4 * g + (j & 3)];
      dst += kb * 1024 + (g * 16 + row) * 16;
    } else { const int db = l >> 5, gl = (l >> 4) & 1, rr = l & 15;
#pragma unroll
      for (int j = 0; j < 8; ++j) o[j] = fin[(8 * gl + j) * 32 + 16 * db + rr];
      dst += 2048 + db * 1024 + ((2 * half + gl) * 16 + rr) * 16;
    }
    st_wt16(mkrsrc(ws + WS_KV, 262144), dst, (u32x4){cvtpk(o[0], o[1]), cvtpk(o[2], o[3]), cvtpk(o[4], o[5]), cvtpk(o[6], o[7])});
    asm volatile("s_waitcnt vmcnt(0)" ::: "memory");
    if (l == 0) signal(ws, FL_KV + a, tag);
  }
  return tag;
}

constexpr int PRIO_Q = 1, PRIO_A = 1, PRIO_G1 = 0, PRIO_GE = 2, PRIO_G2 = 0;
constexpr int G1PAT = 0x09;
constexpr int PFSLEEP = 30, PFROWS = 256, AMAP = 2;

DI void attn_slow(const char* __restrict__ kv, bf16x8 qf, f32x4& o0, f32x4& o1) {
  const int l = lane_id();
  const __amdgpu_buffer_rsrc_t rk = mkrsrc(kv, 262144);
  o0 = (f32x4){0.f, 0.f, 0.f, 0.f}; o1 = o0;
  float m = -3.0e38f, ls = 0.f;
#pragma unroll 1
  for (int t = 0; t < 64; ++t) {
    const int p = t * 4096 + l * 16;
    const bf16x8 k0 = BF(__builtin_amdgcn_raw_buffer_load_b128(rk, p, 0, 16)), k1 = BF(__builtin_amdgcn_raw_buffer_load_b128(rk, p + 1024, 0, 16)),
                 v0 = BF(__builtin_amdgcn_raw_buffer_load_b128(rk, p + 2048, 0, 16)), v1 = BF(__builtin_amdgcn_raw_buffer_load_b128(rk, p + 3072, 0, 16));
    const f32x4 z = {0.f, 0.f, 0.f, 0.f};
    f32x4 s0 = MFMA16(k0, qf, z), s1 = MFMA16(k1, qf, z);
    float mx = fmaxf(fmaxf(fmaxf(s0[0], s0[1]), fmaxf(s0[2], s0[3])), fmaxf(fmaxf(s1[0], s1[1]), fmaxf(s1[2], s1[3])));
    mx = fmaxf(mx, __shfl_xor(mx, 16)); mx = fmaxf(mx, __shfl_xor(mx, 32));
    const float mn = fmaxf(m, mx), al = __builtin_amdgcn_exp2f(m - mn);
    m = mn; float rs = 0.f;
#pragma unroll
    for (int i = 0; i < 4; ++i) { s0[i] = __builtin_amdgcn_exp2f(s0[i] - mn); s1[i] = __builtin_amdgcn_exp2f(s1[i] - mn); rs += s0[i] + s1[i]; o0[i] *= al; o1[i] *= al; }
    rs += __shfl_xor(rs, 16); rs += __shfl_xor(rs, 32); ls = ls * al + rs;
    const u32x4 pp = {cvtpk(s0[0], s0[1]), cvtpk(s0[2], s0[3]), cvtpk(s1[0], s1[1]), cvtpk(s1[2], s1[3])};
    o0 = MFMA16(v0, BF(pp), o0); o1 = MFMA16(v1, BF(pp), o1);
  }
#pragma unroll
  for (int i = 0; i < 4; ++i) { o0[i] /= ls; o1[i] /= ls; }
}

DI void role_attn(int a, const u16* __restrict__ wq, const u16* __restrict__ w2, const u16* __restrict__ wo, char* __restrict__ ws, LAS char* lds, const int w, const u64 tag) {
  const int l = lane_id();
  constexpr int NTB = 32 / AMAP;
  const int xcd = a & 7, idx = a >> 3, tb = (xcd / (8 / AMAP)) * NTB + (idx % NTB), hg = (xcd % (8 / AMAP)) * AMAP + idx / NTB;
  const int g = l >> 4;
  const unsigned lds0 = (unsigned)(uintptr_t)lds;
  constexpr int SLOT = 24576;
  const __amdgpu_buffer_rsrc_t rX = mkrsrc(ws + WS_XN, 4194304), rKV = mkrsrc(ws + WS_KV, 262144);
  const unsigned vlin = l * 16;
  constexpr int QS = 12288;
  const __amdgpu_buffer_rsrc_t rWq = mkrsrc((const char*)wq - 1024, D_ * D_ * 2 + 1024);
  const unsigned vq0 = wdma_off(l, w, 2048) + hg * 256 + w * 8 * 2048 + 1024, vq1 = vq0 + 128 - 1024;
  unsigned xso = (4 * tb + w) * 1024 - 131072, qso = 0u - 65536;
  const unsigned mx = lds0 + w * 1024, mq = lds0 + 4096 + w * 2048;
#define PCQ(SL, i) do { if constexpr ((i) == 0) dma1a_c<(SL) * QS, 131072>(mx, vlin, rX, xso); else if constexpr ((i) == 1) dma1a<(SL) * QS, 65536>(mq, vq0, rWq, qso); else dma1b<1024>(vq1, rWq, qso); } while (0)
#define STGQ(SL) do { PCQ(SL, 0); PCQ(SL, 1); PCQ(SL, 2); } while (0)
  if (w == 0) await(ws, l < 8 ? FL_XN + 4 * tb + (l & 3) + 128 * (l >> 2) : -1, tag);
  asm volatile("s_waitcnt lgkmcnt(0)\n\ts_barrier" ::: "memory");
  constexpr int NPL = 64 / (4 * NTB);
  const int pl = ((idx % NTB) * 4 + w) * NPL + l;
  const unsigned vpq = l < NPL ? (pl >> 1) * 2048 + hg * 256 + (pl & 1) * 128 + 1024 : 0x80000000u;
  const unsigned mpq = lds0 + 6 * QS;
  STGQ(0); STGQ(1); STGQ(2); STGQ(3); STGQ(4); STGQ(5);
  dma4_ahead<1 * 65536>(mpq, vpq, rWq, qso); dma4_ahead<2 * 65536>(mpq, vpq, rWq, qso); dma4_ahead<3 * 65536>(mpq, vpq, rWq, qso);
  dma4_ahead<4 * 65536>(mpq, vpq, rWq, qso); dma4_ahead<5 * 65536>(mpq, vpq, rWq, qso); dma4_ahead<6 * 65536>(mpq, vpq, rWq, qso);
  f32x4 qacc[2][4];
#pragma unroll
  for (int i = 0; i < 2; ++i)
#pragma unroll
    for (int j = 0; j < 4; ++j) qacc[i][j] = (f32x4){0.f, 0.f, 0.f, 0.f};
  const LAS char* xrd = lds + l * 16;
  const LAS char* wrd0 = lds + 4096 + (g * 2 + (w >> 1)) * 1024 + wrd_off(l, 2 * (w & 1));
  const LAS char* wrd1 = lds + 4096 + (g * 2 + (w >> 1)) * 1024 + wrd_off(l, 2 * (w & 1) + 1);
  bf16x8 qxa[4], qwa[2], qxb[4], qwb[2];
#define QRX(X, m, SL) X[m] = ldsfrag(xrd + (SL) * QS + (m) * 1024)
#define QRW(W, n, SL) W[n] = ((n) == 0 ? ldstr(wrd0 + (SL) * QS, wrd0 + (SL) * QS + 512) : ldstr(wrd1 + (SL) * QS, wrd1 + (SL) * QS + 512))
#define QM(X, W, m, n) qacc[n][m] = MFMA16(W[n], X[m], qacc[n][m])
#define STEPQ(X, W, XN, WN, RD, RS, DMA, DS) do { \
    QM(X, W, 0, 0); if (RD) { QRW(WN, 0, RS); QRX(XN, 0, RS); } SB(); QM(X, W, 1, 0); if (RD) { QRW(WN, 1, RS); QRX(XN, 1, RS); } SB(); QM(X, W, 2, 0); if (RD) QRX(XN, 2, RS); SB(); QM(X, W, 3, 0); if (RD) QRX(XN, 3, RS); SB(); \
    QM(X, W, 0, 1); if (DMA) PCQ(DS, 0); SB(); QM(X, W, 1, 1); if (DMA) PCQ(DS, 1); SB(); QM(X, W, 2, 1); if (DMA) PCQ(DS, 2); SB(); QM(X, W, 3, 1); if (DMA) dma4_ahead<6 * 65536>(mpq, vpq, rWq, qso); SB(); } while (0)
  WAIT_BAR(21);
#pragma unroll
  for (int m = 0; m < 4; ++m) QRX(qxa, m, 0);
  QRW(qwa, 0, 0); QRW(qwa, 1, 0);
#define QEV(SC, SN, W_, RD, DMA) do { WAIT_BAR(W_); SB(); STEPQ(qxa, qwa, qxb, qwb, RD, SN, DMA, SC); } while (0)
#define QOD(SC, SN, W_, RD, DMA) do { WAIT_BAR(W_); SB(); STEPQ(qxb, qwb, qxa, qwa, RD, SN, DMA, SC); } while (0)
  QEV(0, 1, 18, true, true); QOD(1, 2, 19, true, true); QEV(2, 3, 20, true, true); QOD(3, 4, 21, true, true); QEV(4, 5, 22, true, true); QOD(5, 0, 17, true, true);
  for (int it = 0; it < 3; ++it) { QEV(0, 1, 17, true, true); QOD(1, 2, 17, true, true); QEV(2, 3, 17, true, true); QOD(3, 4, 17, true, true); QEV(4, 5, 17, true, true); QOD(5, 0, 17, true, true); }
  QEV(0, 1, 17, true, true); QOD(1, 2, 17, true, true);
  QEV(2, 3, 17, true, false); QOD(3, 4, 13, true, false); QEV(4, 5, 9, true, false); QOD(5, 0, 5, true, false);
  if (w == 0) await_n<4>(ws, FL_KV + l, tag);
  QEV(0, 1, 0, true, false);
  unsigned kvso = w * 4096 - 16384;
  const unsigned kvm = lds0 + w * 4096;
#define STGKV(SF) do { dma1a_c<(SF) * SLOT, 16384>(kvm, vlin, rKV, kvso); dma1b_c<1024>(vlin, rKV, kvso); dma1b_c<2048>(vlin, rKV, kvso); dma1b_c<3072>(vlin, rKV, kvso); } while (0)
  STGKV(1); STGKV(2);
  {
    const unsigned line = (a * 4 + w) * 64 + l;
    dma4(lds0 + 6 * QS, line * 128, mkrsrc(w2, F_ * D_ * 2), 0);
    dma4(lds0 + 6 * QS, line * 128, mkrsrc(wo, D_ * D_ * 2), 0);
  }
  STEPQ(qxb, qwb, qxa, qwa, false, 0, false, 0);
  __builtin_amdgcn_s_setprio(PRIO_A);
  bf16x8 qf[4];
#pragma unroll
  for (int m = 0; m < 4; ++m)
    qf[m] = BF(((u32x4){cvtpk(qacc[0][m][0] * QSCALE, qacc[0][m][1] * QSCALE), cvtpk(qacc[0][m][2] * QSCALE, qacc[0][m][3] * QSCALE),
                        cvtpk(qacc[1][m][0] * QSCALE, qacc[1][m][1] * QSCALE), cvtpk(qacc[1][m][2] * QSCALE, qacc[1][m][3] * QSCALE)}));
  WAIT_BAR(6);
  const LAS char* kvb = lds + l * 16;
  f32x4 negm[4], S[4][2], O[4][2];
  bf16x8 k0, k1;
  {
    const LAS char* p = kvb + 1 * SLOT;
    k0 = ldsfrag(p); k1 = ldsfrag(p + 1024);
    const f32x4 z = {0.f, 0.f, 0.f, 0.f};
#pragma unroll
    for (int m = 0; m < 4; ++m) {
      const f32x4 s0 = MFMA16(k0, qf[m], z), s1 = MFMA16(k1, qf[m], z);
      float mx = fmaxf(fmaxf(fmaxf(s0[0], s0[1]), fmaxf(s0[2], s0[3])), fmaxf(fmaxf(s1[0], s1[1]), fmaxf(s1[2], s1[3])));
      mx = xmax4(mx);
      negm[m] = (f32x4){-mx, -mx, -mx, -mx}; PIN(negm[m]);
      O[m][0] = z; O[m][1] = z;
    }
    S[0][0] = MFMA16(k0, qf[0], negm[0]); S[0][1] = MFMA16(k1, qf[0], negm[0]);
    S[1][0] = MFMA16(k0, qf[1], negm[1]); S[1][1] = MFMA16(k1, qf[1], negm[1]);
  }
  u32x4 P[4];
  P[2] = (u32x4){0, 0, 0, 0}; P[3] = P[2];
  bf16x8 va0 = BF(P[2]), va1 = va0, vb0, vb1;
  S[3][1] = (f32x4){0.f, 0.f, 0.f, 0.f};
  f32x4 L[4];
#pragma unroll
  for (int m = 0; m < 4; ++m) L[m] = (f32x4){0.f, 0.f, 0.f, 0.f};
  bf16x8 ones; { const short o1 = (l & 15) == 0 ? (short)0x3f80 : (short)0; s16x8 ov = {o1, o1, o1, o1, o1, o1, o1, o1}; ones = __builtin_bit_cast(bf16x8, ov); PIN(ones); }
#define LS(m) do { L[m] = MFMA16(ones, BF(P[m]), L[m]); PIN(L[m]); SB(); } while (0)
#define NOLS(m)
#define EX2(m, kb, i) do { float t0_ = __builtin_amdgcn_exp2f(S[m][kb][i]), t1_ = __builtin_amdgcn_exp2f(S[m][kb][i + 1]); PIN(t0_); PIN(t1_); S[m][kb][i] = t0_; S[m][kb][i + 1] = t1_; SB(); } while (0)
#define CVA(m, kb) do { unsigned c_ = cvtpk(S[m][kb][0], S[m][kb][1]); PIN(c_); P[m][2 * kb] = c_; SB(); } while (0)
#define CVB(m, kb) do { unsigned c_ = cvtpk(S[m][kb][2], S[m][kb][3]); PIN(c_); P[m][2 * kb + 1] = c_; SB(); } while (0)
#define GRP(mq, kq, KF, pm, pk, me, ke, mp, db, VF, LSM, X) do { \
    S[mq][kq] = MFMA16(KF, qf[mq], negm[mq]); PIN(S[mq][kq]); SB(); X; CVB(pm, pk); EX2(me, ke, 0); \
    O[mp][db] = MFMA16(VF, BF(P[mp]), O[mp][db]); PIN(O[mp][db]); SB(); EX2(me, ke, 2); LSM(mp); CVA(me, ke); } while (0)
#define TILE(KC0, KC1, KN0, KN1, VP0, VP1, VC0, VC1, kn, vp, D) do { \
    GRP(2, 0, KC0, 3, 1, 0, 0, 2, 0, VP0, NOLS, KN0 = ldsfrag(kn); SB()); \
    GRP(2, 1, KC1, 0, 0, 0, 1, 2, 1, VP1, LS,   KN1 = ldsfrag((kn) + 1024); SB()); \
    GRP(3, 0, KC0, 0, 1, 1, 0, 3, 0, VP0, NOLS, VC0 = ldsfrag(vp); SB()); \
    GRP(3, 1, KC1, 1, 0, 1, 1, 3, 1, VP1, LS,   VC1 = ldsfrag((vp) + 1024); SB()); \
    GRP(0, 0, KN0, 1, 1, 2, 0, 0, 0, VC0, NOLS, D(0)); \
    GRP(0, 1, KN1, 2, 0, 2, 1, 0, 1, VC1, LS,   D(1)); \
    GRP(1, 0, KN0, 2, 1, 3, 0, 1, 0, VC0, NOLS, D(2)); \
    GRP(1, 1, KN1, 3, 0, 3, 1, 1, 1, VC1, LS,   D(3)); \
  } while (0)
#define NOD(i)
  bf16x8 kx0, kx1;
#define STAGE(SC, SN, SF, DM, LAST) do { \
    TILE(k0, k1, kx0, kx1, va0, va1, vb0, vb1, kvb + (SC) * SLOT + 4096, kvb + (SC) * SLOT + 2048, NOD); \
    TILE(kx0, kx1, k0, k1, vb0, vb1, va0, va1, kvb + (SC) * SLOT + 8192, kvb + (SC) * SLOT + 4096 + 2048, NOD); \
    TILE(k0, k1, kx0, kx1, va0, va1, vb0, vb1, kvb + (SC) * SLOT + 12288, kvb + (SC) * SLOT + 8192 + 2048, NOD); \
    WAIT_BAR(0);        \
    TILE(kx0, kx1, k0, k1, vb0, vb1, va0, va1, kvb + ((LAST) ? (SC) : (SN)) * SLOT, kvb + (SC) * SLOT + 12288 + 2048, DM); } while (0)
#define DM_(SF, i) do { if constexpr ((i) == 0) { dma1a_c<(SF) * SLOT, 16384>(kvm, vlin, rKV, kvso); } else { dma1b_c<(i) * 1024>(vlin, rKV, kvso); } SB(); } while (0)
#define DM0(i) DM_(0, i)
#define DM1(i) DM_(1, i)
#define DM2(i) DM_(2, i)
  for (int j = 0; j < 12; j += 3) { STAGE(1, 2, 0, DM0, false); STAGE(2, 0, 1, DM1, false); STAGE(0, 1, 2, DM2, false); }
  STAGE(1, 2, 0, DM0, false); STAGE(2, 0, 1, DM1, false); STAGE(0, 1, 2, NOD, false); STAGE(1, 2, 0, NOD, true);
  CVB(3, 1);
  O[2][0] = MFMA16(va0, BF(P[2]), O[2][0]); O[2][1] = MFMA16(va1, BF(P[2]), O[2][1]); LS(2);
  O[3][0] = MFMA16(va0, BF(P[3]), O[3][0]); O[3][1] = MFMA16(va1, BF(P[3]), O[3][1]); LS(3);
  const __amdgpu_buffer_rsrc_t rO = mkrsrc(ws + WS_O, 4194304);
#pragma unroll
  for (int m = 0; m < 4; ++m) {
    float ls = __shfl(L[m][0], l & 15);
    float inv = 1.0f / ls;
    f32x4 o0 = O[m][0], o1 = O[m][1];
    const float mo = fmaxf(fmaxf(fmaxf(fabsf(o0[0]), fabsf(o0[1])), fmaxf(fabsf(o0[2]), fabsf(o0[3]))), fmaxf(fmaxf(fabsf(o1[0]), fabsf(o1[1])), fmaxf(fabsf(o1[2]), fabsf(o1[3]))));
    const bool ok = (ls <= 1.0e18f) && (mo <= 1.0e37f);
    if (__builtin_expect(__any(!ok), 0)) { attn_slow(ws + WS_KV, qf[m], o0, o1); inv = 1.0f; }
    const u32x4 ch = chunk_of(cvtpk(o0[0] * inv, o0[1] * inv), cvtpk(o0[2] * inv, o0[3] * inv), cvtpk(o1[0] * inv, o1[1] * inv), cvtpk(o1[2] * inv, o1[3] * inv));
    const int c = ((g & 1) << 1) | (g >> 1);
    st_wt16(rO, ((4 * hg + w) * 128 + 4 * tb + m) * 1024 + c * 256 + (l & 15) * 16, ch);
  }
  asm volatile("s_waitcnt vmcnt(0)\n\ts_barrier" ::: "memory");
  if (w == 0 && lane_id() == 0) signal(ws, FL_O + tb * 8 + hg, tag);
}

DI int w2_kh(int gi) { return gi & 1; }
DI void w2_weights_early(int gi, const u16* __restrict__ w2, LAS char* lds, const int w) {
  const int l = lane_id(), nt = gi >> 5;
  const __amdgpu_buffer_rsrc_t rW = mkrsrc((const char*)w2 - 1024, F_ * D_ * 2 + 1024);
  const unsigned vw0 = wdma_off(l, w, 2048) + nt * 256 + w * 8 * 2048 + 1024, vw1 = vw0 + 128 - 1024;
  unsigned wso = 56 * w2_kh(gi) * 65536 - 65536;
  const unsigned mw = (unsigned)(uintptr_t)lds + 8192 + w * 2048;
  dma1a<0 * 16384, 65536>(mw, vw0, rW, wso); dma1b<1024>(vw1, rW, wso); dma1a<1 * 16384, 65536>(mw, vw0, rW, wso); dma1b<1024>(vw1, rW, wso);
  dma1a<2 * 16384, 65536>(mw, vw0, rW, wso); dma1b<1024>(vw1, rW, wso); dma1a<3 * 16384, 65536>(mw, vw0, rW, wso); dma1b<1024>(vw1, rW, wso);
}

DI f32x4 gelu4(f32x4 acc, float rs, f32x4 b, float c1v) {
  constexpr float c2 = -2.0f * 0.7978845608028654f * 1.4426950408889634f * 0.044715f;
  float x0 = acc[0], x1 = acc[1], x2 = acc[2], x3 = acc[3], t0, t1, t2, t3;
  asm("v_fma_f32 %0, %0, %8, %9\n\tv_fma_f32 %1, %1, %8, %10\n\tv_fma_f32 %2, %2, %8, %11\n\tv_fma_f32 %3, %3, %8, %12\n\t"
      "v_mul_f32 %4, %0, %0\n\tv_mul_f32 %5, %1, %1\n\tv_mul_f32 %6, %2, %2\n\tv_mul_f32 %7, %3, %3\n\t"
      "v_fma_f32 %4, %4, %14, %13\n\tv_fma_f32 %5, %5, %14, %13\n\tv_fma_f32 %6, %6, %14, %13\n\tv_fma_f32 %7, %7, %14, %13\n\t"
      "v_mul_f32 %4, %0, %4\n\tv_mul_f32 %5, %1, %5\n\tv_mul_f32 %6, %2, %6\n\tv_mul_f32 %7, %3, %7\n\t"
      "v_exp_f32 %4, %4\n\tv_exp_f32 %5, %5\n\tv_exp_f32 %6, %6\n\tv_exp_f32 %7, %7\n\t"
      "v_add_f32 %4, 1.0, %4\n\tv_add_f32 %5, 1.0, %5\n\tv_add_f32 %6, 1.0, %6\n\tv_add_f32 %7, 1.0, %7\n\t"
      "v_rcp_f32 %4, %4\n\tv_rcp_f32 %5, %5\n\tv_rcp_f32 %6, %6\n\tv_rcp_f32 %7, %7\n\t"
      "v_mul_f32 %0, %0, %4\n\tv_mul_f32 %1, %1, %5\n\tv_mul_f32 %2, %2, %6\n\tv_mul_f32 %3, %3, %7"
      : "+v"(x0), "+v"(x1), "+v"(x2), "+v"(x3), "=&v"(t0), "=&v"(t1), "=&v"(t2), "=&v"(t3)
      : "v"(rs), "v"(b[0]), "v"(b[1]), "v"(b[2]), "v"(b[3]), "v"(c1v), "s"(c2));
  return (f32x4){x0, x1, x2, x3};
}

DI u64 role_w1(int gi, const u16* __restrict__ wq, const u16* __restrict__ w1, const u16* __restrict__ b1, const u16* __restrict__ w2, char* __restrict__ ws, LAS char* lds, const int w, u64 eraw) {
  const int l = lane_id();
  const int xcd = gi & 7, idx = gi >> 3, p = (xcd >> 2) * 8 + (idx & 7), j = (xcd & 3) * 4 + (idx >> 3);
  const int g = l >> 4, wm = w >> 1, wn = w & 1;
  const unsigned lds0 = (unsigned)(uintptr_t)lds;
  constexpr int SLOT = 24576;
  const __amdgpu_buffer_rsrc_t rX = mkrsrc(ws + WS_XN, 4194304), rW = mkrsrc((const char*)w1 - 4096, D_ * F_ * 2 + 4096);
  const unsigned vlin = l * 16;
  unsigned vw[4];
#pragma unroll
  for (int i = 0; i < 4; ++i) vw[i] = wdma_off(l, w, F_ * 2) + 8 * w * (F_ * 2) + 448 * j - 64 * (j & 1) + 128 * i + 4096 - 1024 * i;
  unsigned xso = (8 * p + 2 * w) * 1024 - 131072, wso = 0u - 32 * F_ * 2;
  const unsigned mx = lds0 + w * 2048, mw = lds0 + 8192 + w * 4096;
#define PC1(SL, i) do { if constexpr ((i) == 0) dma1a_c<(SL) * SLOT, 131072>(mx, vlin, rX, xso); else if constexpr ((i) == 1) dma1b_c<1024>(vlin, rX, xso); \
    else if constexpr ((i) == 2) dma1a<(SL) * SLOT, 32 * F_ * 2>(mw, vw[0], rW, wso); else dma1b<((i) - 2) * 1024>(vw[(i) - 2], rW, wso); } while (0)
#define STG1(SL) do { PC1(SL, 0); PC1(SL, 1); PC1(SL, 2); PC1(SL, 3); PC1(SL, 4); PC1(SL, 5); } while (0)
  PC1(0, 2); PC1(0, 3); PC1(0, 4); PC1(0, 5); PC1(1, 2); PC1(1, 3); PC1(1, 4); PC1(1, 5); PC1(2, 2); PC1(2, 3); PC1(2, 4); PC1(2, 5);
  constexpr int L_R2 = 3 * SLOT, L_B1 = L_R2 + 512;
  const unsigned vpf = l < 4 ? (4 * (idx & 7) + w) * (F_ * 2) + 448 * j - 64 * (j & 1) + l * 128 + 4096 : 0x80000000u;
  const unsigned mpf = lds0 + L_B1 + 1024;
  dma4_ahead<1 * 32 * F_ * 2>(mpf, vpf, rW, wso); dma4_ahead<2 * 32 * F_ * 2>(mpf, vpf, rW, wso); dma4_ahead<3 * 32 * F_ * 2>(mpf, vpf, rW, wso);
  dma4_ahead<4 * 32 * F_ * 2>(mpf, vpf, rW, wso); dma4_ahead<5 * 32 * F_ * 2>(mpf, vpf, rW, wso);
  if (w != 0 && PFSLEEP > 0) __builtin_amdgcn_s_sleep(PFSLEEP);
  if (w == 1) dma4(mpf, (gi * 64 + l) * 128, mkrsrc(wq, D_ * D_ * 2), 0);
  if (w >= 2 && PFROWS > 0) { constexpr int NL = PFROWS * 56 / 512;
#pragma unroll
    for (int i = 0; i < (NL + 63) / 64; ++i) dma4(mpf, i * 64 + l < NL ? 256 * F_ * 2 + ((gi * 2 + (w - 2)) * NL + i * 64 + l) * 128 : 0x80000000u, mkrsrc(w1, D_ * F_ * 2), 0);
  }
  if (w >= 2) dma4(lds0 + L_B1 + (w - 2) * 256, 448 * j + ((w - 2) * 64 + l) * 4, mkrsrc(b1, F_ * 2), 0);
  asm volatile("s_waitcnt vmcnt(17)" : "+v"(eraw));
  const u64 tag = take_tag(ws, eraw, w);
  if (w == 0) await(ws, l < 16 ? FL_XN + 8 * p + (l & 7) + 128 * (l >> 3) : -1, tag);
  asm volatile("s_barrier" ::: "memory");
  if (w < 2) dma4_c(lds0 + L_R2 + w * 256, (128 * p + 64 * w + l) * 4, mkrsrc(ws + WS_R2, 8192), 0);
  PC1(0, 0); PC1(0, 1); PC1(1, 0); PC1(1, 1); PC1(2, 0); PC1(2, 1);
  dma4(mpf, 0x80000000u, rW, 0);
  f32x4 acc[4][7];
#pragma unroll
  for (int m = 0; m < 4; ++m)
#pragma unroll
    for (int n = 0; n < 7; ++n) acc[m][n] = (f32x4){0.f, 0.f, 0.f, 0.f};
  const LAS char* xrd = lds + 4 * wm * 1024 + l * 16;
  const LAS char* wrb[5];
  { const int off = 6 * wn + 2 * (j & 1), nl = 12 + wn + 2 * (j & 1);
#pragma unroll
    for (int i = 0; i < 4; ++i) wrb[i] = lds + 8192 + (g * 4 + ((i + off) >> 2)) * 1024 + wrd_off(l, (i + off) & 3);
    wrb[4] = lds + 8192 + (g * 4 + (nl >> 2)) * 1024 + wrd_off(l, nl & 3); }
  bf16x8 xa[4], wa[7], xb[4], wb[7];
#define RDX(X, m, so) X[m] = ldsfrag(xrd + (so) + (m) * 1024)
#define RDW(W, n, so) W[n] = ((n) < 6 ? ldstr(wrb[(n) & 3] + (so) + ((n) >> 2) * 1024, wrb[(n) & 3] + (so) + ((n) >> 2) * 1024 + 512) : ldstr(wrb[4] + (so), wrb[4] + (so) + 512))
#define M1(X, W, m, n) acc[m][n] = MFMA16(W[n], X[m], acc[m][n])
#define STEP1(X, W, XN, WN, RD, RS, DMA, DS) do { \
    M1(X, W, 0, 0); if (RD) RDW(WN, 0, (RS) * SLOT); SB(); M1(X, W, 1, 0); if (RD) RDX(XN, 0, (RS) * SLOT); SB(); M1(X, W, 2, 0); if (RD) RDW(WN, 1, (RS) * SLOT); SB(); M1(X, W, 3, 0); if (DMA) PC1(DS, 0); SB(); \
    M1(X, W, 0, 1); if (RD) RDX(XN, 1, (RS) * SLOT); SB(); M1(X, W, 1, 1); if (RD) RDW(WN, 2, (RS) * SLOT); SB(); M1(X, W, 2, 1); if (RD) RDX(XN, 2, (RS) * SLOT); SB(); M1(X, W, 3, 1); if (DMA) PC1(DS, 1); SB(); \
    M1(X, W, 0, 2); if (RD) RDW(WN, 3, (RS) * SLOT); SB(); M1(X, W, 1, 2); if (RD) RDX(XN, 3, (RS) * SLOT); SB(); M1(X, W, 2, 2); if (RD) RDW(WN, 4, (RS) * SLOT); SB(); M1(X, W, 3, 2); if (DMA) PC1(DS, 2); SB(); \
    M1(X, W, 0, 3); if (RD) RDW(WN, 5, (RS) * SLOT); SB(); M1(X, W, 1, 3); SB(); M1(X, W, 2, 3); if (RD) RDW(WN, 6, (RS) * SLOT); SB(); M1(X, W, 3, 3); if (DMA) PC1(DS, 3); SB(); \
    M1(X, W, 0, 4); SB(); M1(X, W, 1, 4); SB(); M1(X, W, 2, 4); SB(); M1(X, W, 3, 4); if (DMA) PC1(DS, 4); SB(); \
    M1(X, W, 0, 5); SB(); M1(X, W, 1, 5); SB(); M1(X, W, 2, 5); SB(); M1(X, W, 3, 5); if (DMA) { PC1(DS, 5); dma4_ahead<5 * 32 * F_ * 2>(mpf, vpf, rW, wso); } SB(); \
    M1(X, W, 0, 6); SB(); M1(X, W, 1, 6); SB(); M1(X, W, 2, 6); SB(); M1(X, W, 3, 6); SB(); } while (0)
  WAIT_BAR(3);
#pragma unroll
  for (int m = 0; m < 4; ++m) RDX(xa, m, 0);
#pragma unroll
  for (int n = 0; n < 7; ++n) RDW(wa, n, 0);
#define EV(SC, SN, W_, RD, DMA) do { WAIT_BAR(W_); SB(); STEP1(xa, wa, xb, wb, RD, SN, DMA, SC); } while (0)
#define OD(SC, SN, W_, RD, DMA) do { WAIT_BAR(W_); SB(); STEP1(xb, wb, xa, wa, RD, SN, DMA, SC); } while (0)
#define PR1(i) do { if constexpr ((i) == 0 || (((G1PAT >> (i)) & 1) != ((G1PAT >> ((i) - 1 < 0 ? 0 : (i) - 1)) & 1))) __builtin_amdgcn_s_setprio(((G1PAT >> (i)) & 1) ? 2 : 0); } while (0)
#define SIX() do { PR1(0); EV(0, 1, 8, true, true); PR1(1); OD(1, 2, 8, true, true); PR1(2); EV(2, 0, 8, true, true); PR1(3); OD(0, 1, 8, true, true); PR1(4); EV(1, 2, 8, true, true); PR1(5); OD(2, 0, 8, true, true); } while (0)
  for (int it = 0; it < 4; ++it) { SIX(); }
  EV(0, 1, 8, true, true); OD(1, 2, 8, true, true); EV(2, 0, 8, true, true); OD(0, 1, 8, true, true); EV(1, 2, 8, true, true);
  OD(2, 0, 8, true, false); EV(0, 1, 0, true, false); OD(1, 2, 0, false, false);
  __builtin_amdgcn_s_setprio(PRIO_GE);
  w2_weights_early(gi, w2, lds, w);
  const int le = lane_id(), ge = le >> 4;
  float r2[4];
#pragma unroll
  for (int m = 0; m < 4; ++m) r2[m] = *(const LAS float*)(lds + L_R2 + (64 * wm + 16 * m + (le & 15)) * 4);
  const __amdgpu_buffer_rsrc_t rH = mkrsrc(ws + WS_H, 112 * 128 * 1024);
  const int cch = ((ge & 1) << 1) | (ge >> 1);
  unsigned last[4][2];
  float c1v = -2.0f * 0.7978845608028654f * 1.4426950408889634f; PIN(c1v);
#pragma unroll
  for (int n = 0; n < 7; ++n) {
    const int nb = n < 6 ? 6 * wn + n : 12 + wn;
    const u32x2 braw = *(const LAS u32x2*)(lds + L_B1 + (16 * nb + 4 * ge) * 2);
    const f32x4 bb = {lo2f(braw[0]), hi2f(braw[0]), lo2f(braw[1]), hi2f(braw[1])};
#pragma unroll
    for (int m = 0; m < 4; ++m) acc[m][n] = gelu4(acc[m][n], r2[m], bb, c1v);
    if (n < 6) { if (n & 1) {
#pragma unroll
        for (int m = 0; m < 4; ++m) {
          const u32x4 ch = chunk_of(cvtpk(acc[m][n - 1][0], acc[m][n - 1][1]), cvtpk(acc[m][n - 1][2], acc[m][n - 1][3]), cvtpk(acc[m][n][0], acc[m][n][1]), cvtpk(acc[m][n][2], acc[m][n][3]));
          st_wt16(rH, ((7 * j + 3 * wn + (n >> 1)) * 128 + 8 * p + 4 * wm + m) * 1024 + cch * 256 + (le & 15) * 16, ch);
        } }
    } else {
#pragma unroll
      for (int m = 0; m < 4; ++m) { last[m][0] = cvtpk(acc[m][n][0], acc[m][n][1]); last[m][1] = cvtpk(acc[m][n][2], acc[m][n][3]); }
    }
  }
#pragma unroll
  for (int m = 0; m < 4; m += 2) {
    const u32x4 ch = chunk_of(last[m][0], last[m][1], last[m + 1][0], last[m + 1][1]);
    st_wt16(rH, ((7 * j + 6) * 128 + 8 * p + 4 * wm + m + (ge & 1)) * 1024 + (2 * wn + (ge >> 1)) * 256 + (le & 15) * 16, ch);
  }
  __builtin_amdgcn_s_setprio(PRIO_G2);
  asm volatile("s_waitcnt vmcnt(0)\n\ts_barrier" ::: "memory");
  if (w == 0 && lane_id() == 0) signal(ws, FL_H + p * 16 + j, tag);
#undef RDX
#undef RDW
#undef EV
#undef OD
  return tag;
}

DI void role_w2(int gi, const u16* __restrict__ w2, const u16* __restrict__ wo, const u16* __restrict__ b2, const u16* __restrict__ x, u16* __restrict__ out, char* __restrict__ ws, LAS char* lds, const int w, const u64 tag) {
  const int l = lane_id();
  const int xcd = gi & 7, idx = gi >> 3, kh = xcd & 1, p = (xcd >> 1) * 4 + (idx & 3), nt = idx >> 2;
  const int partner = gi ^ 1;
  const int g = l >> 4, wm = w >> 1, wn = w & 1;
  const unsigned lds0 = (unsigned)(uintptr_t)lds;
  constexpr int SLOT = 16384;
  const __amdgpu_buffer_rsrc_t rA = mkrsrc(ws + WS_H, 144 * 128 * 1024);
  __amdgpu_buffer_rsrc_t rW = mkrsrc((const char*)w2 - 1024, F_ * D_ * 2 + 1024);
  const unsigned vlin = l * 16;
  const unsigned vw0 = wdma_off(l, w, 2048) + nt * 256 + w * 8 * 2048 + 1024, vw1 = vw0 + 128 - 1024;
  if (gi == 0 && w == 0) {
    await_n<8>(ws, FL_RD + l, tag);
    if (l == 0) signal(ws, FL_E, tag);
  }
  if (w == 0) await(ws, l < 8 ? FL_H + p * 16 + 8 * kh + l : -1, tag);
  asm volatile("s_barrier" ::: "memory");
  unsigned aso = (56 * kh * 128 + 8 * p + 2 * w) * 1024 - 131072, wso = 56 * kh * 65536 + 3 * 65536;
  const unsigned ma = lds0 + w * 2048, mw = lds0 + 8192 + w * 2048;
#define PC2(SL, i) do { if constexpr ((i) == 0) dma1a_c<(SL) * SLOT, 131072>(ma, vlin, rA, aso); else if constexpr ((i) == 1) dma1b_c<1024>(vlin, rA, aso); \
    else if constexpr ((i) == 2) dma1a<(SL) * SLOT, 65536>(mw, vw0, rW, wso); else dma1b<1024>(vw1, rW, wso); } while (0)
#define STG2(SL) do { PC2(SL, 0); PC2(SL, 1); PC2(SL, 2); PC2(SL, 3); } while (0)
  PC2(0, 0); PC2(0, 1); PC2(1, 0); PC2(1, 1); PC2(2, 0); PC2(2, 1); PC2(3, 0); PC2(3, 1); STG2(4);
  f32x4 acc[4][4];
#pragma unroll
  for (int m = 0; m < 4; ++m)
#pragma unroll
    for (int n = 0; n < 4; ++n) acc[m][n] = (f32x4){0.f, 0.f, 0.f, 0.f};
  const LAS char* xrd = lds + 4 * wm * 1024 + l * 16;
  const LAS char* wrb[4];
#pragma unroll
  for (int i = 0; i < 4; ++i) wrb[i] = lds + 8192 + (g * 2 + wn) * 1024 + wrd_off(l, i);
  bf16x8 xa[4], wa[4], xb[4], wb[4];
#define RDX(X, m, so) X[m] = ldsfrag(xrd + (so) + (m) * 1024)
#define RDW(W, n, so) W[n] = ldstr(wrb[n] + (so), wrb[n] + (so) + 512)
#define M2(X, W, m, n) acc[m][n] = MFMA16(W[n], X[m], acc[m][n])
#define STEP2(X, W, XN, WN, RD, RS, DMA, DS) do { \
    M2(X, W, 0, 0); if (RD) RDW(WN, 0, (RS) * SLOT); SB(); M2(X, W, 1, 0); if (RD) RDX(XN, 0, (RS) * SLOT); SB(); M2(X, W, 2, 0); if (RD) RDX(XN, 1, (RS) * SLOT); SB(); M2(X, W, 3, 0); if (DMA) PC2(DS, 0); SB(); \
    M2(X, W, 0, 1); if (RD) RDW(WN, 1, (RS) * SLOT); SB(); M2(X, W, 1, 1); if (RD) RDX(XN, 2, (RS) * SLOT); SB(); M2(X, W, 2, 1); if (RD) RDX(XN, 3, (RS) * SLOT); SB(); M2(X, W, 3, 1); if (DMA) PC2(DS, 1); SB(); \
    M2(X, W, 0, 2); if (RD) RDW(WN, 2, (RS) * SLOT); SB(); M2(X, W, 1, 2); if (RD) RDW(WN, 3, (RS) * SLOT); SB(); M2(X, W, 2, 2); SB(); M2(X, W, 3, 2); if (DMA) PC2(DS, 2); SB(); \
    M2(X, W, 0, 3); SB(); M2(X, W, 1, 3); SB(); M2(X, W, 2, 3); SB(); M2(X, W, 3, 3); if (DMA) PC2(DS, 3); SB(); } while (0)
  WAIT_BAR(6);
#pragma unroll
  for (int i = 0; i < 4; ++i) { RDX(xa, i, 0); RDW(wa, i, 0); }
#define EV(SC, SN, W_, RD, DMA) do { WAIT_BAR(W_); SB(); STEP2(xa, wa, xb, wb, RD, SN, DMA, SC); } while (0)
#define OD(SC, SN, W_, RD, DMA) do { WAIT_BAR(W_); SB(); STEP2(xb, wb, xa, wa, RD, SN, DMA, SC); } while (0)
  u64 oflag = 0;
  for (int it = 0; it < 6; ++it) {

    EV(0, 1, 12, true, true);
    if (it == 5) {
      if (w == 0) {
        asm volatile("" : "+v"(oflag));
        if (!__all(l >= 8 || oflag == tag)) await(ws, l < 8 ? FL_O + (2 * p + (l >> 2)) * 8 + 4 * kh + (l & 3) : -1, tag);
      }
      rW = mkrsrc((const char*)wo - 1024, D_ * D_ * 2 + 1024);
      aso = ((112 + 16 * kh) * 128 + 8 * p + 2 * w) * 1024 - 131072; wso = 16 * kh * 65536 - 65536;
    }
    OD(1, 2, 12, true, true); EV(2, 3, 12, true, true); OD(3, 4, 12, true, true); EV(4, 0, 12, true, true);
    OD(0, 1, 12, true, true); EV(1, 2, 12, true, true);
    if (it == 4 && w == 0) {
      const GAS u64* fo = flagp(ws, FL_O + (2 * p + ((l >> 2) & 1)) * 8 + 4 * kh + (l & 3));
      asm volatile("global_load_dwordx2 %0, %1, off sc1" : "=&v"(oflag) : "v"(fo));
    }
    OD(2, 3, 12, true, true); EV(3, 4, 12, true, true); OD(4, 0, 12, true, true);
  }
  EV(0, 1, 12, true, true); OD(1, 2, 12, true, true); EV(2, 3, 12, true, true); OD(3, 4, 12, true, true); EV(4, 0, 12, true, true); OD(0, 1, 12, true, true); EV(1, 2, 12, true, true);
  u32x2 xres[2][4], bres[4];
  { const int le = lane_id();
    const u16* xp = x + (size_t)(128 * p + 64 * wm + 32 * kh + (le & 15)) * D_ + 128 * nt + 64 * wn + 4 * (le >> 4); const u16* xq = xp + 16 * D_; const u16* bp = b2 + 128 * nt + 64 * wn + 4 * (le >> 4);
    asm volatile("global_load_dwordx2 %0, %8, off\n\tglobal_load_dwordx2 %1, %8, off offset:32\n\tglobal_load_dwordx2 %2, %8, off offset:64\n\tglobal_load_dwordx2 %3, %8, off offset:96\n\t"
                 "global_load_dwordx2 %4, %9, off\n\tglobal_load_dwordx2 %5, %9, off offset:32\n\tglobal_load_dwordx2 %6, %9, off offset:64\n\tglobal_load_dwordx2 %7, %9, off offset:96"
                 : "=&v"(xres[0][0]), "=&v"(xres[0][1]), "=&v"(xres[0][2]), "=&v"(xres[0][3]), "=&v"(xres[1][0]), "=&v"(xres[1][1]), "=&v"(xres[1][2]), "=&v"(xres[1][3]) : "v"(xp), "v"(xq));
    asm volatile("global_load_dwordx2 %0, %4, off\n\tglobal_load_dwordx2 %1, %4, off offset:32\n\tglobal_load_dwordx2 %2, %4, off offset:64\n\tglobal_load_dwordx2 %3, %4, off offset:96"
                 : "=&v"(bres[0]), "=&v"(bres[1]), "=&v"(bres[2]), "=&v"(bres[3]) : "v"(bp)); }
  OD(2, 3, 24, true, false); EV(3, 4, 20, true, false); OD(4, 0, 16, true, false); EV(0, 1, 12, true, false); OD(1, 2, 12, false, false);
  if (kh) {
#pragma unroll
    for (int n = 0; n < 4; ++n) { f32x4 t = acc[0][n]; acc[0][n] = acc[2][n]; acc[2][n] = t; t = acc[1][n]; acc[1][n] = acc[3][n]; acc[3][n] = t; }
#pragma unroll
    for (int n = 0; n < 4; ++n) { PIN(acc[0][n]); PIN(acc[1][n]); PIN(acc[2][n]); PIN(acc[3][n]); }
  }
  const int le = lane_id(), ge = le >> 4;
  const int token0 = 128 * p + 64 * wm + 32 * kh + (le & 15);
  {
    const __amdgpu_buffer_rsrc_t rP = mkrsrc(ws + WS_PART + (size_t)gi * 32768, 16384);
#pragma unroll
    for (int m = 0; m < 2; ++m)
#pragma unroll
      for (int n2 = 0; n2 < 2; ++n2)
        st_wt16(rP, ((w * 2 + m) * 2 + n2) * 1024 + le * 16, (u32x4){cvtpk(acc[2 + m][2 * n2][0], acc[2 + m][2 * n2][1]), cvtpk(acc[2 + m][2 * n2][2], acc[2 + m][2 * n2][3]),
                                                                       cvtpk(acc[2 + m][2 * n2 + 1][0], acc[2 + m][2 * n2 + 1][1]), cvtpk(acc[2 + m][2 * n2 + 1][2], acc[2 + m][2 * n2 + 1][3])});
    asm volatile("s_waitcnt vmcnt(0)\n\ts_barrier" : "+v"(xres[0][0]), "+v"(xres[0][1]), "+v"(xres[0][2]), "+v"(xres[0][3]), "+v"(xres[1][0]), "+v"(xres[1][1]), "+v"(xres[1][2]), "+v"(xres[1][3]),
                 "+v"(bres[0]), "+v"(bres[1]), "+v"(bres[2]), "+v"(bres[3]) :: "memory");
    if (w == 0) { if (le == 0) signal(ws, FL_X + gi, tag); await(ws, FL_X + partner, tag); }
    asm volatile("s_barrier" ::: "memory");
  }
  const __amdgpu_buffer_rsrc_t rPP = mkrsrc(ws + WS_PART + (size_t)partner * 32768, 16384);
  u32x4 pv[2][2];
#pragma unroll
  for (int m = 0; m < 2; ++m)
#pragma unroll
    for (int n2 = 0; n2 < 2; ++n2) pv[m][n2] = __builtin_amdgcn_raw_buffer_load_b128(rPP, ((w * 2 + m) * 2 + n2) * 1024 + le * 16, 0, 16);
#pragma unroll
  for (int m = 0; m < 2; ++m) {
    unsigned d[4][2];
#pragma unroll
    for (int n = 0; n < 4; ++n) {
      const unsigned p0 = pv[m][n >> 1][2 * (n & 1)], p1 = pv[m][n >> 1][2 * (n & 1) + 1];
      const f32x4 a4 = acc[m][n];
      d[n][0] = cvtpk(a4[0] + lo2f(p0) + lo2f(bres[n][0]) + lo2f(xres[m][n][0]), a4[1] + hi2f(p0) + hi2f(bres[n][0]) + hi2f(xres[m][n][0]));
      d[n][1] = cvtpk(a4[2] + lo2f(p1) + lo2f(bres[n][1]) + lo2f(xres[m][n][1]), a4[3] + hi2f(p1) + hi2f(bres[n][1]) + hi2f(xres[m][n][1]));
    }
    const int cch = ((ge & 1) << 1) | (ge >> 1);
#pragma unroll
    for (int n2 = 0; n2 < 2; ++n2) {
      const u32x4 ch = chunk_of(d[2 * n2][0], d[2 * n2][1], d[2 * n2 + 1][0], d[2 * n2 + 1][1]);
      st_wt16(mkrsrc(out, S_ * D_ * 2), ((token0 + 16 * m) * D_ + 128 * nt + 64 * wn + 32 * n2 + 8 * cch) * 2, ch);
    }
  }
#undef RDX
#undef RDW
}

constexpr int LDS_BYTES = 81920;
__global__ __launch_bounds__(256, 2) void k_main(const u16* __restrict__ x, const u16* __restrict__ wq, const u16* __restrict__ wk, const u16* __restrict__ wv, const u16* __restrict__ wo,
                                                  const u16* __restrict__ w1, const u16* __restrict__ b1, const u16* __restrict__ w2, const u16* __restrict__ b2, u16* __restrict__ out, char* __restrict__ ws) {
  extern __shared__ __attribute__((aligned(16))) char lds_[];
  LAS char* lds = (LAS char*)lds_;
  const int w = __builtin_amdgcn_readfirstlane(threadIdx.x >> 6);
  u64 eraw;
  { const GAS u64* ep = flagp(ws, FL_E); asm volatile("global_load_dwordx2 %0, %1, off sc1" : "=&v"(eraw) : "v"(ep)); }
  if ((blockIdx.x >> 8) == 0) {
    __builtin_amdgcn_s_setprio(PRIO_Q);
    const u64 tag = phase0(blockIdx.x, x, wk, wv, ws, lds, w, eraw);
    role_attn(blockIdx.x, wq, w2, wo, ws, lds, w, tag);
  } else {
    __builtin_amdgcn_s_setprio(PRIO_G1);
    const u64 tag = role_w1(blockIdx.x & 255, wq, w1, b1, w2, ws, lds, w, eraw);
    role_w2(blockIdx.x & 255, w2, wo, b2, x, out, ws, lds, w, tag);
  }
}

extern "C" void kernel_launch(void* const* d_in, const int* in_sizes, int n_in, void* d_out, int out_size, void* d_ws, size_t ws_size, hipStream_t stream) {
  const u16* x = (const u16*)d_in[0]; const u16* wq = (const u16*)d_in[1]; const u16* wk = (const u16*)d_in[2];
  const u16* wv = (const u16*)d_in[3]; const u16* wo = (const u16*)d_in[4];
  const u16* w1 = (const u16*)d_in[6]; const u16* b1 = (const u16*)d_in[7]; const u16* w2 = (const u16*)d_in[8]; const u16* b2 = (const u16*)d_in[9];
  (void)hipFuncSetAttribute((const void*)k_main, hipFuncAttributeMaxDynamicSharedMemorySize, LDS_BYTES);
  k_main<<<512, 256, LDS_BYTES, stream>>>(x, wq, wk, wv, wo, w1, b1, w2, b2, (u16*)d_out, (char*)d_ws);
}
```

```cpp
#include <hip/hip_runtime.h>
#include <stdint.h>
#define PROBE 0

typedef unsigned short u16;
typedef __bf16 bf16x8 __attribute__((ext_vector_type(8)));
typedef __bf16 bf16x2 __attribute__((ext_vector_type(2)));
typedef short s16x8 __attribute__((ext_vector_type(8)));
typedef float f32x4 __attribute__((ext_vector_type(4)));
typedef float f32x2 __attribute__((ext_vector_type(2)));
typedef unsigned u32x4 __attribute__((ext_vector_type(4)));
typedef unsigned u32x2 __attribute__((ext_vector_type(2)));
#define LAS __attribute__((address_space(3)))
#define GAS __attribute__((address_space(1)))
#define DI __device__ __forceinline__
#define SB() __builtin_amdgcn_sched_barrier(0)
#define PIN(x) asm volatile("" : "+v"(x))
#define MFMA16(a, b, c) __builtin_amdgcn_mfma_f32_16x16x32_bf16((a), (b), (c), 0, 0, 0)
#define BF(x) __builtin_bit_cast(bf16x8, x)

constexpr int S_ = 2048, D_ = 1024, F_ = 3584;
constexpr size_t WS_FLAGS = 0;
constexpr size_t WS_R2   = 131072;
constexpr size_t WS_KV   = WS_R2 + 8192;
constexpr size_t WS_XN   = WS_KV + 262144;
constexpr size_t WS_H    = WS_XN + 4194304;
constexpr size_t WS_O    = WS_H + (size_t)112 * 128 * 1024;
constexpr size_t WS_PART = WS_O + 4194304;

constexpr float QSCALE = 0.17677669529663687f * 1.4426950408889634f;

DI int lane_id() { int l; asm volatile("v_mbcnt_lo_u32_b32 %0, -1, 0\n\tv_mbcnt_hi_u32_b32 %0, -1, %0" : "=v"(l)); return l; }
DI unsigned cvtpk(float lo, float hi) { f32x2 v = {lo, hi}; bf16x2 b = __builtin_convertvector(v, bf16x2); return __builtin_bit_cast(unsigned, b); }
DI float lo2f(unsigned u) { return __uint_as_float(u << 16); }
DI float hi2f(unsigned u) { return __uint_as_float(u & 0xffff0000u); }
DI __amdgpu_buffer_rsrc_t mkrsrc(const void* p, unsigned bytes) { return __builtin_amdgcn_make_buffer_rsrc((void*)p, 0, bytes, 0x00020000); }
DI void dma16(unsigned lds_addr, unsigned voff, __amdgpu_buffer_rsrc_t rsrc, unsigned soff) {
  asm volatile("s_mov_b32 m0, %0\n\ts_nop 0\n\tbuffer_load_dwordx4 %1, %2, %3 offen lds" :: "s"(lds_addr), "v"(voff), "s"(rsrc), "s"(soff));
}
template <int IMM, int INC> DI void dma1a(unsigned mbase, unsigned v0, __amdgpu_buffer_rsrc_t rsrc, unsigned& soff) {
  asm volatile("s_add_u32 m0, %2, %4\n\ts_add_u32 %0, %0, %5\n\tbuffer_load_dwordx4 %1, %3, %0 offen lds" : "+s"(soff) : "v"(v0), "s"(mbase), "s"(rsrc), "i"(IMM), "i"(INC) : "scc");
}
template <int OFF> DI void dma1b(unsigned v0, __amdgpu_buffer_rsrc_t rsrc, unsigned soff) {
  asm volatile("buffer_load_dwordx4 %0, %1, %2 offen offset:%3 lds" :: "v"(v0), "s"(rsrc), "s"(soff), "i"(OFF));
}
template <int IMM, int INC> DI void dma1a_c(unsigned mbase, unsigned v0, __amdgpu_buffer_rsrc_t rsrc, unsigned& soff) {
  asm volatile("s_add_u32 m0, %2, %4\n\ts_add_u32 %0, %0, %5\n\tbuffer_load_dwordx4 %1, %3, %0 offen sc1 lds" : "+s"(soff) : "v"(v0), "s"(mbase), "s"(rsrc), "i"(IMM), "i"(INC) : "scc");
}
template <int OFF> DI void dma1b_c(unsigned v0, __amdgpu_buffer_rsrc_t rsrc, unsigned soff) {
  asm volatile("buffer_load_dwordx4 %0, %1, %2 offen offset:%3 sc1 lds" :: "v"(v0), "s"(rsrc), "s"(soff), "i"(OFF));
}
DI void dma4(unsigned lds_addr, unsigned voff, __amdgpu_buffer_rsrc_t rsrc, unsigned soff) {
  asm volatile("s_mov_b32 m0, %0\n\ts_nop 0\n\tbuffer_load_dword %1, %2, %3 offen lds" :: "s"(lds_addr), "v"(voff), "s"(rsrc), "s"(soff));
}
DI void dma4_c(unsigned lds_addr, unsigned voff, __amdgpu_buffer_rsrc_t rsrc, unsigned soff) {
  asm volatile("s_mov_b32 m0, %0\n\ts_nop 0\n\tbuffer_load_dword %1, %2, %3 offen sc1 lds" :: "s"(lds_addr), "v"(voff), "s"(rsrc), "s"(soff));
}
template <int ADD> DI void dma4_ahead(unsigned lds_addr, unsigned voff, __amdgpu_buffer_rsrc_t rsrc, unsigned soff) {
  unsigned t;
  asm volatile("s_mov_b32 m0, %1\n\ts_add_u32 %0, %4, %5\n\tbuffer_load_dword %2, %3, %0 offen lds" : "=&s"(t) : "s"(lds_addr), "v"(voff), "s"(rsrc), "s"(soff), "i"(ADD) : "scc");
}
#define WAIT_BAR(N) asm volatile("s_waitcnt vmcnt(" #N ") lgkmcnt(0)\n\ts_barrier" ::: "memory")
DI bf16x8 ldsfrag(const LAS char* p) { return *(const LAS bf16x8*)p; }
typedef short v4i16_t __attribute__((ext_vector_type(4)));
DI bf16x8 ldstr(const LAS char* p0, const LAS char* p1) {
  v4i16_t a = __builtin_amdgcn_ds_read_tr16_b64_v4i16((LAS v4i16_t*)p0);
  v4i16_t b = __builtin_amdgcn_ds_read_tr16_b64_v4i16((LAS v4i16_t*)p1);
  s16x8 v = __builtin_shufflevector(a, b, 0, 1, 2, 3, 4, 5, 6, 7);
  return __builtin_bit_cast(bf16x8, v);
}
DI void st_wt16(__amdgpu_buffer_rsrc_t r, unsigned voff, u32x4 v) { __builtin_amdgcn_raw_buffer_store_b128(v, r, voff, 0, 17); }
DI u32x4 chunk_of(unsigned x0, unsigned x1, unsigned y0, unsigned y1) {
  auto s0 = __builtin_amdgcn_permlane16_swap(x0, y0, false, false);
  auto s1 = __builtin_amdgcn_permlane16_swap(x1, y1, false, false);
  return (u32x4){s0[0], s1[0], s0[1], s1[1]};
}
DI unsigned wdma_off(int l, int kblk, int ldbytes) {
  const int row = l >> 3, c8 = l & 7, sw = ((row >> 1) & 1) | ((kblk & 1) << 1);
  return row * ldbytes + (((c8 >> 1) ^ sw) * 32) + (c8 & 1) * 16;
}
DI unsigned wrd_off(int l, int seg) {
  const int g = l >> 4, q = (l & 15) >> 2, sw = ((q >> 1) & 1) | ((g & 1) << 1);
  return q * 128 + ((seg ^ sw) * 32) + (l & 3) * 8;
}
DI float xsum4(float v) {
  auto a = __builtin_amdgcn_permlane16_swap(__float_as_uint(v), __float_as_uint(v), false, false); v = __uint_as_float(a[0]) + __uint_as_float(a[1]);
  auto b = __builtin_amdgcn_permlane32_swap(__float_as_uint(v), __float_as_uint(v), false, false); return __uint_as_float(b[0]) + __uint_as_float(b[1]);
}
DI float xmax4(float v) {
  auto a = __builtin_amdgcn_permlane16_swap(__float_as_uint(v), __float_as_uint(v), false, false); v = fmaxf(__uint_as_float(a[0]), __uint_as_float(a[1]));
  auto b = __builtin_amdgcn_permlane32_swap(__float_as_uint(v), __float_as_uint(v), false, false); return fmaxf(__uint_as_float(b[0]), __uint_as_float(b[1]));
}

typedef unsigned long long u64;
constexpr int FL_E = 0, FL_RD = 16, FL_XN = 528, FL_KV = 784, FL_H = 1040, FL_O = 1296, FL_X = 1552;
DI GAS u64* flagp(char* ws, int i) { return (GAS u64*)(ws + WS_FLAGS + (size_t)i * 64); }
DI void signal(char* ws, int i, u64 tag) { __hip_atomic_store(flagp(ws, i), tag, __ATOMIC_RELAXED, __HIP_MEMORY_SCOPE_AGENT); }
DI u64 take_tag(char* ws, u64 eraw, int w) {
  asm volatile("" : "+v"(eraw));
  const u64 tag = (((u64)(unsigned)__builtin_amdgcn_readfirstlane((unsigned)(eraw >> 32))) << 32 | (unsigned)__builtin_amdgcn_readfirstlane((unsigned)eraw)) + 1;
  if (w == 0 && lane_id() == 0) signal(ws, FL_RD + blockIdx.x, tag);
  return tag;
}
DI void await(char* ws, int idx, u64 tag) {
  GAS u64* f = flagp(ws, idx < 0 ? FL_E : idx);
  for (unsigned spins = 0; spins < (1u << 20); ++spins) {
    const u64 v = __hip_atomic_load(f, __ATOMIC_RELAXED, __HIP_MEMORY_SCOPE_AGENT);
    if (__all(idx < 0 || v == tag)) break;
    __builtin_amdgcn_s_sleep(2);
  }
  asm volatile("" ::: "memory");
}

template <int N> DI void await_n(char* ws, int idx, u64 tag) {
  for (unsigned spins = 0; spins < (1u << 20); ++spins) {
    bool ok = true;
#pragma unroll
    for (int i = 0; i < N; ++i) ok &= __hip_atomic_load(flagp(ws, idx + 64 * i), __ATOMIC_RELAXED, __HIP_MEMORY_SCOPE_AGENT) == tag;
    if (__all(ok)) break;
    __builtin_amdgcn_s_sleep(2);
  }
  asm volatile("" ::: "memory");
}

DI u64 phase0(int a, const u16* __restrict__ x, const u16* __restrict__ wk, const u16* __restrict__ wv, char* __restrict__ ws, LAS char* lds, const int w, const u64 eraw) {
  const int l = lane_id(), tid = w * 64 + l;
  LAS float* part = (LAS float*)(lds + 65536);
  LAS float* fin = (LAS float*)(lds + 65536 + 8192);
  LAS float* red = (LAS float*)(lds + 65536 + 8192 + 2048);
  const int rb = a & 127, hf = a >> 7;
  const int r = l & 15, c = l >> 4;
  u32x4 xv[8];
  { const u16* xr = x + (size_t)(rb * 16 + r) * D_ + w * 256 + c * 8;
#define XL(i) asm volatile("global_load_dwordx4 %0, %1, off offset:" #i "*64" : "=&v"(xv[i]) : "v"(xr))
    XL(0); XL(1); XL(2); XL(3); XL(4); XL(5); XL(6); XL(7); }
  const unsigned lds0 = (unsigned)(uintptr_t)lds;
  {
    __amdgpu_buffer_rsrc_t rw = mkrsrc(hf ? wv : wk, 65536);
#pragma unroll
    for (int i = 0; i < 16; ++i) dma16(lds0 + w * 16384 + i * 1024, l * 16, rw, w * 16384 + i * 1024);
  }
  asm volatile("s_waitcnt vmcnt(16)" : "+v"(xv[0]), "+v"(xv[1]), "+v"(xv[2]), "+v"(xv[3]), "+v"(xv[4]), "+v"(xv[5]), "+v"(xv[6]), "+v"(xv[7]));
  const u64 tag = take_tag(ws, eraw, w);
  f32x2 v[32];
#pragma unroll
  for (int i = 0; i < 8; ++i)
#pragma unroll
    for (int j = 0; j < 4; ++j) v[4 * i + j] = (f32x2){lo2f(xv[i][j]), hi2f(xv[i][j])};
  f32x2 s2 = (v[0] + v[1]) + (v[2] + v[3]);
#pragma unroll
  for (int i = 4; i < 32; i += 4) s2 += (v[i] + v[i + 1]) + (v[i + 2] + v[i + 3]);
  const float s = xsum4(s2[0] + s2[1]);
  if (c == 0) red[w * 16 + r] = s;
  asm volatile("s_waitcnt lgkmcnt(0)\n\ts_barrier" ::: "memory");
  const float mean = ((red[r] + red[16 + r]) + (red[32 + r] + red[48 + r])) * (1.0f / D_);
  f32x2 q2 = {0.f, 0.f}, q3 = {0.f, 0.f};
#pragma unroll
  for (int i = 0; i < 32; i += 2) { v[i] -= mean; v[i + 1] -= mean; q2 += v[i] * v[i]; q3 += v[i + 1] * v[i + 1]; }
  const float q = xsum4((q2[0] + q2[1]) + (q3[0] + q3[1]));
  if (c == 0) red[64 + w * 16 + r] = q;
  asm volatile("s_waitcnt lgkmcnt(0)\n\ts_barrier" ::: "memory");
  const float var = ((red[64 + r] + red[80 + r]) + (red[96 + r] + red[112 + r])) * (1.0f / D_);
  const float rstd = 1.0f / sqrtf(var + 1e-5f);
#pragma unroll
  for (int i = 0; i < 8; ++i)
#pragma unroll
    for (int j = 0; j < 4; ++j) { const f32x2 t = v[4 * i + j] * rstd; xv[i][j] = cvtpk(t[0], t[1]); }
  const __amdgpu_buffer_rsrc_t rXN = mkrsrc(ws + WS_XN, 4194304);
#pragma unroll
  for (int i = 0; i < 4; ++i) st_wt16(rXN, ((8 * w + 2 * i + hf) * 128 + rb) * 1024 + l * 16, hf ? xv[2 * i + 1] : xv[2 * i]);
  if (hf == 0 && w == 0 && c == 0) __hip_atomic_store((GAS float*)(ws + WS_R2) + rb * 16 + r, 1.0f / sqrtf(var * rstd * rstd + 1e-5f), __ATOMIC_RELAXED, __HIP_MEMORY_SCOPE_AGENT);
  asm volatile("s_waitcnt vmcnt(0)\n\ts_barrier" ::: "memory");
  if (tid == 0) signal(ws, FL_XN + a, tag);
  f32x4 acc[2] = {{0.f, 0.f, 0.f, 0.f}, {0.f, 0.f, 0.f, 0.f}};
  const LAS char* wb = (const LAS char*)lds + w * 16384 + (8 * c + ((l & 15) >> 2)) * 64 + (l & 3) * 8;
#pragma unroll
  for (int i = 0; i < 8; ++i)
#pragma unroll
    for (int b = 0; b < 2; ++b) {
      bf16x8 wf = ldstr(wb + i * 2048 + b * 32, wb + i * 2048 + b * 32 + 256);
      acc[b] = MFMA16(BF(xv[i]), wf, acc[b]);
    }
#pragma unroll
  for (int b = 0; b < 2; ++b)
#pragma unroll
    for (int j = 0; j < 4; ++j) part[(w * 16 + 4 * c + j) * 32 + 16 * b + r] = acc[b][j];
  asm volatile("s_waitcnt lgkmcnt(0)\n\ts_barrier" ::: "memory");
#pragma unroll
  for (int u = 0; u < 2; ++u) { const int e = tid + 256 * u; fin[e] = (part[e] + part[512 + e]) + (part[1024 + e] + part[1536 + e]); }
  asm volatile("s_waitcnt lgkmcnt(0)\n\ts_barrier" ::: "memory");
  if (w == 0) {
    float o[8];
    unsigned dst = (rb >> 1) * 4096;
    const int half = rb & 1;
    if (hf == 0) { const int kb = l >> 5, g = (l >> 3) & 3, rr = l & 7;
      const int row = 8 * half + rr, key = 8 * (row >> 2) + 4 * kb + (row & 3) - 16 * half;
#pragma unroll
      for (int j = 0; j < 8; ++j) o[j] = fin[key * 32 + 16 * (j >> 2) + 4 * g + (j & 3)];
      dst += kb * 1024 + (g * 16 + row) * 16;
    } else { const int db = l >> 5, gl = (l >> 4) & 1, rr = l & 15;
#pragma unroll
      for (int j = 0; j < 8; ++j) o[j] = fin[(8 * gl + j) * 32 + 16 * db + rr];
      dst += 2048 + db * 1024 + ((2 * half + gl) * 16 + rr) * 16;
    }
    st_wt16(mkrsrc(ws + WS_KV, 262144), dst, (u32x4){cvtpk(o[0], o[1]), cvtpk(o[2], o[3]), cvtpk(o[4], o[5]), cvtpk(o[6], o[7])});
    asm volatile("s_waitcnt vmcnt(0)" ::: "memory");
    if (l == 0) signal(ws, FL_KV + a, tag);
  }
  return tag;
}

constexpr int PRIO_Q = 1, PRIO_A = 1, PRIO_G1 = 0, PRIO_GE = 2, PRIO_G2 = 0;
constexpr int G1PAT = 0x09;
constexpr int PFSLEEP = 30, PFROWS = 256, AMAP = 2;

DI void attn_slow(const char* __restrict__ kv, bf16x8 qf, f32x4& o0, f32x4& o1) {
  const int l = lane_id();
  const __amdgpu_buffer_rsrc_t rk = mkrsrc(kv, 262144);
  o0 = (f32x4){0.f, 0.f, 0.f, 0.f}; o1 = o0;
  float m = -3.0e38f, ls = 0.f;
#pragma unroll 1
  for (int t = 0; t < 64; ++t) {
    const int p = t * 4096 + l * 16;
    const bf16x8 k0 = BF(__builtin_amdgcn_raw_buffer_load_b128(rk, p, 0, 16)), k1 = BF(__builtin_amdgcn_raw_buffer_load_b128(rk, p + 1024, 0, 16)),
                 v0 = BF(__builtin_amdgcn_raw_buffer_load_b128(rk, p + 2048, 0, 16)), v1 = BF(__builtin_amdgcn_raw_buffer_load_b128(rk, p + 3072, 0, 16));
    const f32x4 z = {0.f, 0.f, 0.f, 0.f};
    f32x4 s0 = MFMA16(k0, qf, z), s1 = MFMA16(k1, qf, z);
    float mx = fmaxf(fmaxf(fmaxf(s0[0], s0[1]), fmaxf(s0[2], s0[3])), fmaxf(fmaxf(s1[0], s1[1]), fmaxf(s1[2], s1[3])));
    mx = fmaxf(mx, __shfl_xor(mx, 16)); mx = fmaxf(mx, __shfl_xor(mx, 32));
    const float mn = fmaxf(m, mx), al = __builtin_amdgcn_exp2f(m - mn);
    m = mn; float rs = 0.f;
#pragma unroll
    for (int i = 0; i < 4; ++i) { s0[i] = __builtin_amdgcn_exp2f(s0[i] - mn); s1[i] = __builtin_amdgcn_exp2f(s1[i] - mn); rs += s0[i] + s1[i]; o0[i] *= al; o1[i] *= al; }
    rs += __shfl_xor(rs, 16); rs += __shfl_xor(rs, 32); ls = ls * al + rs;
    const u32x4 pp = {cvtpk(s0[0], s0[1]), cvtpk(s0[2], s0[3]), cvtpk(s1[0], s1[1]), cvtpk(s1[2], s1[3])};
    o0 = MFMA16(v0, BF(pp), o0); o1 = MFMA16(v1, BF(pp), o1);
  }
#pragma unroll
  for (int i = 0; i < 4; ++i) { o0[i] /= ls; o1[i] /= ls; }
}

DI void role_attn(int a, const u16* __restrict__ wq, const u16* __restrict__ w2, const u16* __restrict__ wo, char* __restrict__ ws, LAS char* lds, const int w, const u64 tag) {
  const int l = lane_id();
  constexpr int NTB = 32 / AMAP;
  const int xcd = a & 7, idx = a >> 3, tb = (xcd / (8 / AMAP)) * NTB + (idx % NTB), hg = (xcd % (8 / AMAP)) * AMAP + idx / NTB;
  const int g = l >> 4;
  const unsigned lds0 = (unsigned)(uintptr_t)lds;
  constexpr int SLOT = 24576;
  const __amdgpu_buffer_rsrc_t rX = mkrsrc(ws + WS_XN, 4194304), rKV = mkrsrc(ws + WS_KV, 262144);
  const unsigned vlin = l * 16;
  constexpr int QS = 12288;
  const __amdgpu_buffer_rsrc_t rWq = mkrsrc((const char*)wq - 1024, D_ * D_ * 2 + 1024);
  const unsigned vq0 = wdma_off(l, w, 2048) + hg * 256 + w * 8 * 2048 + 1024, vq1 = vq0 + 128 - 1024;
  unsigned xso = (4 * tb + w) * 1024 - 131072, qso = 0u - 65536;
  const unsigned mx = lds0 + w * 1024, mq = lds0 + 4096 + w * 2048;
#define PCQ(SL, i) do { if constexpr ((i) == 0) dma1a_c<(SL) * QS, 131072>(mx, vlin, rX, xso); else if constexpr ((i) == 1) dma1a<(SL) * QS, 65536>(mq, vq0, rWq, qso); else dma1b<1024>(vq1, rWq, qso); } while (0)
#define STGQ(SL) do { PCQ(SL, 0); PCQ(SL, 1); PCQ(SL, 2); } while (0)
  if (w == 0) await(ws, l < 8 ? FL_XN + 4 * tb + (l & 3) + 128 * (l >> 2) : -1, tag);
  asm volatile("s_waitcnt lgkmcnt(0)\n\ts_barrier" ::: "memory");
  constexpr int NPL = 64 / (4 * NTB);
  const int pl = ((idx % NTB) * 4 + w) * NPL + l;
  const unsigned vpq = l < NPL ? (pl >> 1) * 2048 + hg * 256 + (pl & 1) * 128 + 1024 : 0x80000000u;
  const unsigned mpq = lds0 + 6 * QS;
  STGQ(0); STGQ(1); STGQ(2); STGQ(3); STGQ(4); STGQ(5);
  dma4_ahead<1 * 65536>(mpq, vpq, rWq, qso); dma4_ahead<2 * 65536>(mpq, vpq, rWq, qso); dma4_ahead<3 * 65536>(mpq, vpq, rWq, qso);
  dma4_ahead<4 * 65536>(mpq, vpq, rWq, qso); dma4_ahead<5 * 65536>(mpq, vpq, rWq, qso); dma4_ahead<6 * 65536>(mpq, vpq, rWq, qso);
  f32x4 qacc[2][4];
#pragma unroll
  for (int i = 0; i < 2; ++i)
#pragma unroll
    for (int j = 0; j < 4; ++j) qacc[i][j] = (f32x4){0.f, 0.f, 0.f, 0.f};
  const LAS char* xrd = lds + l * 16;
  const LAS char* wrd0 = lds + 4096 + (g * 2 + (w >> 1)) * 1024 + wrd_off(l, 2 * (w & 1));
  const LAS char* wrd1 = lds + 4096 + (g * 2 + (w >> 1)) * 1024 + wrd_off(l, 2 * (w & 1) + 1);
  bf16x8 qxa[4], qwa[2], qxb[4], qwb[2];
#define QRX(X, m, SL) X[m] = ldsfrag(xrd + (SL) * QS + (m) * 1024)
#define QRW(W, n, SL) W[n] = ((n) == 0 ? ldstr(wrd0 + (SL) * QS, wrd0 + (SL) * QS + 512) : ldstr(wrd1 + (SL) * QS, wrd1 + (SL) * QS + 512))
#define QM(X, W, m, n) qacc[n][m] = MFMA16(W[n], X[m], qacc[n][m])
#define STEPQ(X, W, XN, WN, RD, RS, DMA, DS) do { \
    QM(X, W, 0, 0); if (RD) { QRW(WN, 0, RS); QRX(XN, 0, RS); } SB(); QM(X, W, 1, 0); if (RD) { QRW(WN, 1, RS); QRX(XN, 1, RS); } SB(); QM(X, W, 2, 0); if (RD) QRX(XN, 2, RS); SB(); QM(X, W, 3, 0); if (RD) QRX(XN, 3, RS); SB(); \
    QM(X, W, 0, 1); if (DMA) PCQ(DS, 0); SB(); QM(X, W, 1, 1); if (DMA) PCQ(DS, 1); SB(); QM(X, W, 2, 1); if (DMA) PCQ(DS, 2); SB(); QM(X, W, 3, 1); if (DMA) dma4_ahead<6 * 65536>(mpq, vpq, rWq, qso); SB(); } while (0)
  WAIT_BAR(21);
#pragma unroll
  for (int m = 0; m < 4; ++m) QRX(qxa, m, 0);
  QRW(qwa, 0, 0); QRW(qwa, 1, 0);
#define QEV(SC, SN, W_, RD, DMA) do { WAIT_BAR(W_); SB(); STEPQ(qxa, qwa, qxb, qwb, RD, SN, DMA, SC); } while (0)
#define QOD(SC, SN, W_, RD, DMA) do { WAIT_BAR(W_); SB(); STEPQ(qxb, qwb, qxa, qwa, RD, SN, DMA, SC); } while (0)
  QEV(0, 1, 18, true, true); QOD(1, 2, 19, true, true); QEV(2, 3, 20, true, true); QOD(3, 4, 21, true, true); QEV(4, 5, 22, true, true); QOD(5, 0, 17, true, true);
  for (int it = 0; it < 3; ++it) { QEV(0, 1, 17, true, true); QOD(1, 2, 17, true, true); QEV(2, 3, 17, true, true); QOD(3, 4, 17, true, true); QEV(4, 5, 17, true, true); QOD(5, 0, 17, true, true); }
  QEV(0, 1, 17, true, true); QOD(1, 2, 17, true, true);
  QEV(2, 3, 17, true, false); QOD(3, 4, 13, true, false); QEV(4, 5, 9, true, false); QOD(5, 0, 5, true, false);
  if (w == 0) await_n<4>(ws, FL_KV + l, tag);
  QEV(0, 1, 0, true, false);
  unsigned kvso = w * 4096 - 16384;
  const unsigned kvm = lds0 + w * 4096;
#define STGKV(SF) do { dma1a_c<(SF) * SLOT, 16384>(kvm, vlin, rKV, kvso); dma1b_c<1024>(vlin, rKV, kvso); dma1b_c<2048>(vlin, rKV, kvso); dma1b_c<3072>(vlin, rKV, kvso); } while (0)
  STGKV(1); STGKV(2);
  {
    const unsigned line = (a * 4 + w) * 64 + l;
    dma4(lds0 + 6 * QS, line * 128, mkrsrc(w2, F_ * D_ * 2), 0);
    dma4(lds0 + 6 * QS, line * 128, mkrsrc(wo, D_ * D_ * 2), 0);
  }
  STEPQ(qxb, qwb, qxa, qwa, false, 0, false, 0);
  __builtin_amdgcn_s_setprio(PRIO_A);
  bf16x8 qf[4];
#pragma unroll
  for (int m = 0; m < 4; ++m)
    qf[m] = BF(((u32x4){cvtpk(qacc[0][m][0] * QSCALE, qacc[0][m][1] * QSCALE), cvtpk(qacc[0][m][2] * QSCALE, qacc[0][m][3] * QSCALE),
                        cvtpk(qacc[1][m][0] * QSCALE, qacc[1][m][1] * QSCALE), cvtpk(qacc[1][m][2] * QSCALE, qacc[1][m][3] * QSCALE)}));
  WAIT_BAR(6);
  const LAS char* kvb = lds + l * 16;
  f32x4 negm[4], S[4][2], O[4][2];
  bf16x8 k0, k1;
  {
    const LAS char* p = kvb + 1 * SLOT;
    k0 = ldsfrag(p); k1 = ldsfrag(p + 1024);
    const f32x4 z = {0.f, 0.f, 0.f, 0.f};
#pragma unroll
    for (int m = 0; m < 4; ++m) {
      const f32x4 s0 = MFMA16(k0, qf[m], z), s1 = MFMA16(k1, qf[m], z);
      float mx = fmaxf(fmaxf(fmaxf(s0[0], s0[1]), fmaxf(s0[2], s0[3])), fmaxf(fmaxf(s1[0], s1[1]), fmaxf(s1[2], s1[3])));
      mx = xmax4(mx);
      negm[m] = (f32x4){-mx, -mx, -mx, -mx}; PIN(negm[m]);
      O[m][0] = z; O[m][1] = z;
    }
    S[0][0] = MFMA16(k0, qf[0], negm[0]); S[0][1] = MFMA16(k1, qf[0], negm[0]);
    S[1][0] = MFMA16(k0, qf[1], negm[1]); S[1][1] = MFMA16(k1, qf[1], negm[1]);
  }
  u32x4 P[4];
  P[2] = (u32x4){0, 0, 0, 0}; P[3] = P[2];
  bf16x8 va0 = BF(P[2]), va1 = va0, vb0, vb1;
  S[3][1] = (f32x4){0.f, 0.f, 0.f, 0.f};
  f32x4 L[4];
#pragma unroll
  for (int m = 0; m < 4; ++m) L[m] = (f32x4){0.f, 0.f, 0.f, 0.f};
  bf16x8 ones; { const short o1 = (l & 15) == 0 ? (short)0x3f80 : (short)0; s16x8 ov = {o1, o1, o1, o1, o1, o1, o1, o1}; ones = __builtin_bit_cast(bf16x8, ov); PIN(ones); }
#define LS(m) do { L[m] = MFMA16(ones, BF(P[m]), L[m]); PIN(L[m]); SB(); } while (0)
#define NOLS(m)
#define EX2(m, kb, i) do { float t0_ = __builtin_amdgcn_exp2f(S[m][kb][i]), t1_ = __builtin_amdgcn_exp2f(S[m][kb][i + 1]); PIN(t0_); PIN(t1_); S[m][kb][i] = t0_; S[m][kb][i + 1] = t1_; SB(); } while (0)
#define CVA(m, kb) do { unsigned c_ = cvtpk(S[m][kb][0], S[m][kb][1]); PIN(c_); P[m][2 * kb] = c_; SB(); } while (0)
#define CVB(m, kb) do { unsigned c_ = cvtpk(S[m][kb][2], S[m][kb][3]); PIN(c_); P[m][2 * kb + 1] = c_; SB(); } while (0)
#define GRP(mq, kq, KF, pm, pk, me, ke, mp, db, VF, LSM, X) do { \
    S[mq][kq] = MFMA16(KF, qf[mq], negm[mq]); PIN(S[mq][kq]); SB(); X; CVB(pm, pk); EX2(me, ke, 0); \
    O[mp][db] = MFMA16(VF, BF(P[mp]), O[mp][db]); PIN(O[mp][db]); SB(); EX2(me, ke, 2); LSM(mp); CVA(me, ke); } while (0)
#define TILE(KC0, KC1, KN0, KN1, VP0, VP1, VC0, VC1, kn, vp, D) do { \
    GRP(2, 0, KC0, 3, 1, 0, 0, 2, 0, VP0, NOLS, KN0 = ldsfrag(kn); SB()); \
    GRP(2, 1, KC1, 0, 0, 0, 1, 2, 1, VP1, LS,   KN1 = ldsfrag((kn) + 1024); SB()); \
    GRP(3, 0, KC0, 0, 1, 1, 0, 3, 0, VP0, NOLS, VC0 = ldsfrag(vp); SB()); \
    GRP(3, 1, KC1, 1, 0, 1, 1, 3, 1, VP1, LS,   VC1 = ldsfrag((vp) + 1024); SB()); \
    GRP(0, 0, KN0, 1, 1, 2, 0, 0, 0, VC0, NOLS, D(0)); \
    GRP(0, 1, KN1, 2, 0, 2, 1, 0, 1, VC1, LS,   D(1)); \
    GRP(1, 0, KN0, 2, 1, 3, 0, 1, 0, VC0, NOLS, D(2)); \
    GRP(1, 1, KN1, 3, 0, 3, 1, 1, 1, VC1, LS,   D(3)); \
  } while (0)
#define NOD(i)
  bf16x8 kx0, kx1;
#define STAGE(SC, SN, SF, DM, LAST) do { \
    TILE(k0, k1, kx0, kx1, va0, va1, vb0, vb1, kvb + (SC) * SLOT + 4096, kvb + (SC) * SLOT + 2048, NOD); \
    TILE(kx0, kx1, k0, k1, vb0, vb1, va0, va1, kvb + (SC) * SLOT + 8192, kvb + (SC) * SLOT + 4096 + 2048, NOD); \
    TILE(k0, k1, kx0, kx1, va0, va1, vb0, vb1, kvb + (SC) * SLOT + 12288, kvb + (SC) * SLOT + 8192 + 2048, NOD); \
    WAIT_BAR(0);        \
    TILE(kx0, kx1, k0, k1, vb0, vb1, va0, va1, kvb + ((LAST) ? (SC) : (SN)) * SLOT, kvb + (SC) * SLOT + 12288 + 2048, DM); } while (0)
#define DM_(SF, i) do { if constexpr ((i) == 0) { dma1a_c<(SF) * SLOT, 16384>(kvm, vlin, rKV, kvso); } else { dma1b_c<(i) * 1024>(vlin, rKV, kvso); } SB(); } while (0)
#define DM0(i) DM_(0, i)
#define DM1(i) DM_(1, i)
#define DM2(i) DM_(2, i)
  for (int j = 0; j < 12; j += 3) { STAGE(1, 2, 0, DM0, false); STAGE(2, 0, 1, DM1, false); STAGE(0, 1, 2, DM2, false); }
  STAGE(1, 2, 0, DM0, false); STAGE(2, 0, 1, DM1, false); STAGE(0, 1, 2, NOD, false); STAGE(1, 2, 0, NOD, true);
  CVB(3, 1);
  O[2][0] = MFMA16(va0, BF(P[2]), O[2][0]); O[2][1] = MFMA16(va1, BF(P[2]), O[2][1]); LS(2);
  O[3][0] = MFMA16(va0, BF(P[3]), O[3][0]); O[3][1] = MFMA16(va1, BF(P[3]), O[3][1]); LS(3);
  const __amdgpu_buffer_rsrc_t rO = mkrsrc(ws + WS_O, 4194304);
#pragma unroll
  for (int m = 0; m < 4; ++m) {
    float ls = __shfl(L[m][0], l & 15);
    float inv = 1.0f / ls;
    f32x4 o0 = O[m][0], o1 = O[m][1];
    const float mo = fmaxf(fmaxf(fmaxf(fabsf(o0[0]), fabsf(o0[1])), fmaxf(fabsf(o0[2]), fabsf(o0[3]))), fmaxf(fmaxf(fabsf(o1[0]), fabsf(o1[1])), fmaxf(fabsf(o1[2]), fabsf(o1[3]))));
    const bool ok = (ls <= 1.0e18f) && (mo <= 1.0e37f);
    if (__builtin_expect(__any(!ok), 0)) { attn_slow(ws + WS_KV, qf[m], o0, o1); inv = 1.0f; }
    const u32x4 ch = chunk_of(cvtpk(o0[0] * inv, o0[1] * inv), cvtpk(o0[2] * inv, o0[3] * inv), cvtpk(o1[0] * inv, o1[1] * inv), cvtpk(o1[2] * inv, o1[3] * inv));
    const int c = ((g & 1) << 1) | (g >> 1);
    st_wt16(rO, ((4 * hg + w) * 128 + 4 * tb + m) * 1024 + c * 256 + (l & 15) * 16, ch);
  }
  asm volatile("s_waitcnt vmcnt(0)\n\ts_barrier" ::: "memory");
  if (w == 0 && lane_id() == 0) signal(ws, FL_O + tb * 8 + hg, tag);
}

DI int w2_kh(int gi) { return gi & 1; }
DI void w2_weights_early(int gi, const u16* __restrict__ w2, LAS char* lds, const int w) {
  const int l = lane_id(), nt = gi >> 5;
  const __amdgpu_buffer_rsrc_t rW = mkrsrc((const char*)w2 - 1024, F_ * D_ * 2 + 1024);
  const unsigned vw0 = wdma_off(l, w, 2048) + nt * 256 + w * 8 * 2048 + 1024, vw1 = vw0 + 128 - 1024;
  unsigned wso = 56 * w2_kh(gi) * 65536 - 65536;
  const unsigned mw = (unsigned)(uintptr_t)lds + 8192 + w * 2048;
  dma1a<0 * 16384, 65536>(mw, vw0, rW, wso); dma1b<1024>(vw1, rW, wso); dma1a<1 * 16384, 65536>(mw, vw0, rW, wso); dma1b<1024>(vw1, rW, wso);
  dma1a<2 * 16384, 65536>(mw, vw0, rW, wso); dma1b<1024>(vw1, rW, wso); dma1a<3 * 16384, 65536>(mw, vw0, rW, wso); dma1b<1024>(vw1, rW, wso);
}

DI f32x4 gelu4(f32x4 acc, float rs, f32x4 b, float c1v) {
  constexpr float c2 = -2.0f * 0.7978845608028654f * 1.4426950408889634f * 0.044715f;
  float x0 = acc[0], x1 = acc[1], x2 = acc[2], x3 = acc[3], t0, t1, t2, t3;
  asm("v_fma_f32 %0, %0, %8, %9\n\tv_fma_f32 %1, %1, %8, %10\n\tv_fma_f32 %2, %2, %8, %11\n\tv_fma_f32 %3, %3, %8, %12\n\t"
      "v_mul_f32 %4, %0, %0\n\tv_mul_f32 %5, %1, %1\n\tv_mul_f32 %6, %2, %2\n\tv_mul_f32 %7, %3, %3\n\t"
      "v_fma_f32 %4, %4, %14, %13\n\tv_fma_f32 %5, %5, %14, %13\n\tv_fma_f32 %6, %6, %14, %13\n\tv_fma_f32 %7, %7, %14, %13\n\t"
      "v_mul_f32 %4, %0, %4\n\tv_mul_f32 %5, %1, %5\n\tv_mul_f32 %6, %2, %6\n\tv_mul_f32 %7, %3, %7\n\t"
      "v_exp_f32 %4, %4\n\tv_exp_f32 %5, %5\n\tv_exp_f32 %6, %6\n\tv_exp_f32 %7, %7\n\t"
      "v_add_f32 %4, 1.0, %4\n\tv_add_f32 %5, 1.0, %5\n\tv_add_f32 %6, 1.0, %6\n\tv_add_f32 %7, 1.0, %7\n\t"
      "v_rcp_f32 %4, %4\n\tv_rcp_f32 %5, %5\n\tv_rcp_f32 %6, %6\n\tv_rcp_f32 %7, %7\n\t"
      "v_mul_f32 %0, %0, %4\n\tv_mul_f32 %1, %1, %5\n\tv_mul_f32 %2, %2, %6\n\tv_mul_f32 %3, %3, %7"
      : "+v"(x0), "+v"(x1), "+v"(x2), "+v"(x3), "=&v"(t0), "=&v"(t1), "=&v"(t2), "=&v"(t3)
      : "v"(rs), "v"(b[0]), "v"(b[1]), "v"(b[2]), "v"(b[3]), "v"(c1v), "s"(c2));
  return (f32x4){x0, x1, x2, x3};
}

DI u64 role_w1(unsigned long long t_in, unsigned long long& probe, int gi, const u16* __restrict__ wq, const u16* __restrict__ w1, const u16* __restrict__ b1, const u16* __restrict__ w2, char* __restrict__ ws, LAS char* lds, const int w, u64 eraw) {
  const int l = lane_id();
  const int xcd = gi & 7, idx = gi >> 3, p = (xcd >> 2) * 8 + (idx & 7), j = (xcd & 3) * 4 + (idx >> 3);
  const int g = l >> 4, wm = w >> 1, wn = w & 1;
  const unsigned lds0 = (unsigned)(uintptr_t)lds;
  constexpr int SLOT = 24576;
  const __amdgpu_buffer_rsrc_t rX = mkrsrc(ws + WS_XN, 4194304), rW = mkrsrc((const char*)w1 - 4096, D_ * F_ * 2 + 4096);
  const unsigned vlin = l * 16;
  unsigned vw[4];
#pragma unroll
  for (int i = 0; i < 4; ++i) vw[i] = wdma_off(l, w, F_ * 2) + 8 * w * (F_ * 2) + 448 * j - 64 * (j & 1) + 128 * i + 4096 - 1024 * i;
  unsigned xso = (8 * p + 2 * w) * 1024 - 131072, wso = 0u - 32 * F_ * 2;
  const unsigned mx = lds0 + w * 2048, mw = lds0 + 8192 + w * 4096;
#define PC1(SL, i) do { if constexpr ((i) == 0) dma1a_c<(SL) * SLOT, 131072>(mx, vlin, rX, xso); else if constexpr ((i) == 1) dma1b_c<1024>(vlin, rX, xso); \
    else if constexpr ((i) == 2) dma1a<(SL) * SLOT, 32 * F_ * 2>(mw, vw[0], rW, wso); else dma1b<((i) - 2) * 1024>(vw[(i) - 2], rW, wso); } while (0)
#define STG1(SL) do { PC1(SL, 0); PC1(SL, 1); PC1(SL, 2); PC1(SL, 3); PC1(SL, 4); PC1(SL, 5); } while (0)
  PC1(0, 2); PC1(0, 3); PC1(0, 4); PC1(0, 5); PC1(1, 2); PC1(1, 3); PC1(1, 4); PC1(1, 5); PC1(2, 2); PC1(2, 3); PC1(2, 4); PC1(2, 5);
  constexpr int L_R2 = 3 * SLOT, L_B1 = L_R2 + 512;
  const unsigned vpf = l < 4 ? (4 * (idx & 7) + w) * (F_ * 2) + 448 * j - 64 * (j & 1) + l * 128 + 4096 : 0x80000000u;
  const unsigned mpf = lds0 + L_B1 + 1024;
  dma4_ahead<1 * 32 * F_ * 2>(mpf, vpf, rW, wso); dma4_ahead<2 * 32 * F_ * 2>(mpf, vpf, rW, wso); dma4_ahead<3 * 32 * F_ * 2>(mpf, vpf, rW, wso);
  dma4_ahead<4 * 32 * F_ * 2>(mpf, vpf, rW, wso); dma4_ahead<5 * 32 * F_ * 2>(mpf, vpf, rW, wso);
  if (w != 0 && PFSLEEP > 0) __builtin_amdgcn_s_sleep(PFSLEEP);
  if (w == 1) dma4(mpf, (gi * 64 + l) * 128, mkrsrc(wq, D_ * D_ * 2), 0);
  if (w >= 2 && PFROWS > 0) { constexpr int NL = PFROWS * 56 / 512;
#pragma unroll
    for (int i = 0; i < (NL + 63) / 64; ++i) dma4(mpf, i * 64 + l < NL ? 256 * F_ * 2 + ((gi * 2 + (w - 2)) * NL + i * 64 + l) * 128 : 0x80000000u, mkrsrc(w1, D_ * F_ * 2), 0);
  }
  if (w >= 2) dma4(lds0 + L_B1 + (w - 2) * 256, 448 * j + ((w - 2) * 64 + l) * 4, mkrsrc(b1, F_ * 2), 0);
  asm volatile("s_waitcnt vmcnt(17)" : "+v"(eraw));
  const u64 tag = take_tag(ws, eraw, w);
  if (w == 0) await(ws, l < 16 ? FL_XN + 8 * p + (l & 7) + 128 * (l >> 3) : -1, tag);
  asm volatile("s_barrier" ::: "memory");
  if (PROBE == 1) probe = __builtin_amdgcn_s_memrealtime() - t_in;
  if (w < 2) dma4_c(lds0 + L_R2 + w * 256, (128 * p + 64 * w + l) * 4, mkrsrc(ws + WS_R2, 8192), 0);
  PC1(0, 0); PC1(0, 1); PC1(1, 0); PC1(1, 1); PC1(2, 0); PC1(2, 1);
  dma4(mpf, 0x80000000u, rW, 0);
  f32x4 acc[4][7];
#pragma unroll
  for (int m = 0; m < 4; ++m)
#pragma unroll
    for (int n = 0; n < 7; ++n) acc[m][n] = (f32x4){0.f, 0.f, 0.f, 0.f};
  const LAS char* xrd = lds + 4 * wm * 1024 + l * 16;
  const LAS char* wrb[5];
  { const int off = 6 * wn + 2 * (j & 1), nl = 12 + wn + 2 * (j & 1);
#pragma unroll
    for (int i = 0; i < 4; ++i) wrb[i] = lds + 8192 + (g * 4 + ((i + off) >> 2)) * 1024 + wrd_off(l, (i + off) & 3);
    wrb[4] = lds + 8192 + (g * 4 + (nl >> 2)) * 1024 + wrd_off(l, nl & 3); }
  bf16x8 xa[4], wa[7], xb[4], wb[7];
#define RDX(X, m, so) X[m] = ldsfrag(xrd + (so) + (m) * 1024)
#define RDW(W, n, so) W[n] = ((n) < 6 ? ldstr(wrb[(n) & 3] + (so) + ((n) >> 2) * 1024, wrb[(n) & 3] + (so) + ((n) >> 2) * 1024 + 512) : ldstr(wrb[4] + (so), wrb[4] + (so) + 512))
#define M1(X, W, m, n) acc[m][n] = MFMA16(W[n], X[m], acc[m][n])
#define STEP1(X, W, XN, WN, RD, RS, DMA, DS) do { \
    M1(X, W, 0, 0); if (RD) RDW(WN, 0, (RS) * SLOT); SB(); M1(X, W, 1, 0); if (RD) RDX(XN, 0, (RS) * SLOT); SB(); M1(X, W, 2, 0); if (RD) RDW(WN, 1, (RS) * SLOT); SB(); M1(X, W, 3, 0); if (DMA) PC1(DS, 0); SB(); \
    M1(X, W, 0, 1); if (RD) RDX(XN, 1, (RS) * SLOT); SB(); M1(X, W, 1, 1); if (RD) RDW(WN, 2, (RS) * SLOT); SB(); M1(X, W, 2, 1); if (RD) RDX(XN, 2, (RS) * SLOT); SB(); M1(X, W, 3, 1); if (DMA) PC1(DS, 1); SB(); \
    M1(X, W, 0, 2); if (RD) RDW(WN, 3, (RS) * SLOT); SB(); M1(X, W, 1, 2); if (RD) RDX(XN, 3, (RS) * SLOT); SB(); M1(X, W, 2, 2); if (RD) RDW(WN, 4, (RS) * SLOT); SB(); M1(X, W, 3, 2); if (DMA) PC1(DS, 2); SB(); \
    M1(X, W, 0, 3); if (RD) RDW(WN, 5, (RS) * SLOT); SB(); M1(X, W, 1, 3); SB(); M1(X, W, 2, 3); if (RD) RDW(WN, 6, (RS) * SLOT); SB(); M1(X, W, 3, 3); if (DMA) PC1(DS, 3); SB(); \
    M1(X, W, 0, 4); SB(); M1(X, W, 1, 4); SB(); M1(X, W, 2, 4); SB(); M1(X, W, 3, 4); if (DMA) PC1(DS, 4); SB(); \
    M1(X, W, 0, 5); SB(); M1(X, W, 1, 5); SB(); M1(X, W, 2, 5); SB(); M1(X, W, 3, 5); if (DMA) { PC1(DS, 5); dma4_ahead<5 * 32 * F_ * 2>(mpf, vpf, rW, wso); } SB(); \
    M1(X, W, 0, 6); SB(); M1(X, W, 1, 6); SB(); M1(X, W, 2, 6); SB(); M1(X, W, 3, 6); SB(); } while (0)
  WAIT_BAR(3);
#pragma unroll
  for (int m = 0; m < 4; ++m) RDX(xa, m, 0);
#pragma unroll
  for (int n = 0; n < 7; ++n) RDW(wa, n, 0);
#define EV(SC, SN, W_, RD, DMA) do { WAIT_BAR(W_); SB(); STEP1(xa, wa, xb, wb, RD, SN, DMA, SC); } while (0)
#define OD(SC, SN, W_, RD, DMA) do { WAIT_BAR(W_); SB(); STEP1(xb, wb, xa, wa, RD, SN, DMA, SC); } while (0)
#define PR1(i) do { if constexpr ((i) == 0 || (((G1PAT >> (i)) & 1) != ((G1PAT >> ((i) - 1 < 0 ? 0 : (i) - 1)) & 1))) __builtin_amdgcn_s_setprio(((G1PAT >> (i)) & 1) ? 2 : 0); } while (0)
#define SIX() do { PR1(0); EV(0, 1, 8, true, true); PR1(1); OD(1, 2, 8, true, true); PR1(2); EV(2, 0, 8, true, true); PR1(3); OD(0, 1, 8, true, true); PR1(4); EV(1, 2, 8, true, true); PR1(5); OD(2, 0, 8, true, true); } while (0)
  for (int it = 0; it < 4; ++it) { SIX(); }
  EV(0, 1, 8, true, true); OD(1, 2, 8, true, true); EV(2, 0, 8, true, true); OD(0, 1, 8, true, true); EV(1, 2, 8, true, true);
  OD(2, 0, 8, true, false); EV(0, 1, 0, true, false); OD(1, 2, 0, false, false);
  if (PROBE == 2) probe = __builtin_amdgcn_s_memrealtime() - t_in;
  __builtin_amdgcn_s_setprio(PRIO_GE);
  w2_weights_early(gi, w2, lds, w);
  const int le = lane_id(), ge = le >> 4;
  float r2[4];
#pragma unroll
  for (int m = 0; m < 4; ++m) r2[m] = *(const LAS float*)(lds + L_R2 + (64 * wm + 16 * m + (le & 15)) * 4);
  const __amdgpu_buffer_rsrc_t rH = mkrsrc(ws + WS_H, 112 * 128 * 1024);
  const int cch = ((ge & 1) << 1) | (ge >> 1);
  unsigned last[4][2];
  float c1v = -2.0f * 0.7978845608028654f * 1.4426950408889634f; PIN(c1v);
#pragma unroll
  for (int n = 0; n < 7; ++n) {
    const int nb = n < 6 ? 6 * wn + n : 12 + wn;
    const u32x2 braw = *(const LAS u32x2*)(lds + L_B1 + (16 * nb + 4 * ge) * 2);
    const f32x4 bb = {lo2f(braw[0]), hi2f(braw[0]), lo2f(braw[1]), hi2f(braw[1])};
#pragma unroll
    for (int m = 0; m < 4; ++m) acc[m][n] = gelu4(acc[m][n], r2[m], bb, c1v);
    if (n < 6) { if (n & 1) {
#pragma unroll
        for (int m = 0; m < 4; ++m) {
          const u32x4 ch = chunk_of(cvtpk(acc[m][n - 1][0], acc[m][n - 1][1]), cvtpk(acc[m][n - 1][2], acc[m][n - 1][3]), cvtpk(acc[m][n][0], acc[m][n][1]), cvtpk(acc[m][n][2], acc[m][n][3]));
          st_wt16(rH, ((7 * j + 3 * wn + (n >> 1)) * 128 + 8 * p + 4 * wm + m) * 1024 + cch * 256 + (le & 15) * 16, ch);
        } }
    } else {
#pragma unroll
      for (int m = 0; m < 4; ++m) { last[m][0] = cvtpk(acc[m][n][0], acc[m][n][1]); last[m][1] = cvtpk(acc[m][n][2], acc[m][n][3]); }
    }
  }
#pragma unroll
  for (int m = 0; m < 4; m += 2) {
    const u32x4 ch = chunk_of(last[m][0], last[m][1], last[m + 1][0], last[m + 1][1]);
    st_wt16(rH, ((7 * j + 6) * 128 + 8 * p + 4 * wm + m + (ge & 1)) * 1024 + (2 * wn + (ge >> 1)) * 256 + (le & 15) * 16, ch);
  }
  __builtin_amdgcn_s_setprio(PRIO_G2);
  asm volatile("s_waitcnt vmcnt(0)\n\ts_barrier" ::: "memory");
  if (w == 0 && lane_id() == 0) signal(ws, FL_H + p * 16 + j, tag);
#undef RDX
#undef RDW
#undef EV
#undef OD
  return tag;
}

DI void role_w2(unsigned long long t_in, unsigned long long& probe, int gi, const u16* __restrict__ w2, const u16* __restrict__ wo, const u16* __restrict__ b2, const u16* __restrict__ x, u16* __restrict__ out, char* __restrict__ ws, LAS char* lds, const int w, const u64 tag) {
  const int l = lane_id();
  const int xcd = gi & 7, idx = gi >> 3, kh = xcd & 1, p = (xcd >> 1) * 4 + (idx & 3), nt = idx >> 2;
  const int partner = gi ^ 1;
  const int g = l >> 4, wm = w >> 1, wn = w & 1;
  const unsigned lds0 = (unsigned)(uintptr_t)lds;
  constexpr int SLOT = 16384;
  const __amdgpu_buffer_rsrc_t rA = mkrsrc(ws + WS_H, 144 * 128 * 1024);
  __amdgpu_buffer_rsrc_t rW = mkrsrc((const char*)w2 - 1024, F_ * D_ * 2 + 1024);
  const unsigned vlin = l * 16;
  const unsigned vw0 = wdma_off(l, w, 2048) + nt * 256 + w * 8 * 2048 + 1024, vw1 = vw0 + 128 - 1024;
  if (gi == 0 && w == 0) {
    await_n<8>(ws, FL_RD + l, tag);
    if (l == 0) signal(ws, FL_E, tag);
  }
  if (w == 0) await(ws, l < 8 ? FL_H + p * 16 + 8 * kh + l : -1, tag);
  asm volatile("s_barrier" ::: "memory");
  if (PROBE == 3) probe = __builtin_amdgcn_s_memrealtime() - t_in;
  unsigned aso = (56 * kh * 128 + 8 * p + 2 * w) * 1024 - 131072, wso = 56 * kh * 65536 + 3 * 65536;
  const unsigned ma = lds0 + w * 2048, mw = lds0 + 8192 + w * 2048;
#define PC2(SL, i) do { if constexpr ((i) == 0) dma1a_c<(SL) * SLOT, 131072>(ma, vlin, rA, aso); else if constexpr ((i) == 1) dma1b_c<1024>(vlin, rA, aso); \
    else if constexpr ((i) == 2) dma1a<(SL) * SLOT, 65536>(mw, vw0, rW, wso); else dma1b<1024>(vw1, rW, wso); } while (0)
#define STG2(SL) do { PC2(SL, 0); PC2(SL, 1); PC2(SL, 2); PC2(SL, 3); } while (0)
  PC2(0, 0); PC2(0, 1); PC2(1, 0); PC2(1, 1); PC2(2, 0); PC2(2, 1); PC2(3, 0); PC2(3, 1); STG2(4);
  f32x4 acc[4][4];
#pragma unroll
  for (int m = 0; m < 4; ++m)
#pragma unroll
    for (int n = 0; n < 4; ++n) acc[m][n] = (f32x4){0.f, 0.f, 0.f, 0.f};
  const LAS char* xrd = lds + 4 * wm * 1024 + l * 16;
  const LAS char* wrb[4];
#pragma unroll
  for (int i = 0; i < 4; ++i) wrb[i] = lds + 8192 + (g * 2 + wn) * 1024 + wrd_off(l, i);
  bf16x8 xa[4], wa[4], xb[4], wb[4];
#define RDX(X, m, so) X[m] = ldsfrag(xrd + (so) + (m) * 1024)
#define RDW(W, n, so) W[n] = ldstr(wrb[n] + (so), wrb[n] + (so) + 512)
#define M2(X, W, m, n) acc[m][n] = MFMA16(W[n], X[m], acc[m][n])
#define STEP2(X, W, XN, WN, RD, RS, DMA, DS) do { \
    M2(X, W, 0, 0); if (RD) RDW(WN, 0, (RS) * SLOT); SB(); M2(X, W, 1, 0); if (RD) RDX(XN, 0, (RS) * SLOT); SB(); M2(X, W, 2, 0); if (RD) RDX(XN, 1, (RS) * SLOT); SB(); M2(X, W, 3, 0); if (DMA) PC2(DS, 0); SB(); \
    M2(X, W, 0, 1); if (RD) RDW(WN, 1, (RS) * SLOT); SB(); M2(X, W, 1, 1); if (RD) RDX(XN, 2, (RS) * SLOT); SB(); M2(X, W, 2, 1); if (RD) RDX(XN, 3, (RS) * SLOT); SB(); M2(X, W, 3, 1); if (DMA) PC2(DS, 1); SB(); \
    M2(X, W, 0, 2); if (RD) RDW(WN, 2, (RS) * SLOT); SB(); M2(X, W, 1, 2); if (RD) RDW(WN, 3, (RS) * SLOT); SB(); M2(X, W, 2, 2); SB(); M2(X, W, 3, 2); if (DMA) PC2(DS, 2); SB(); \
    M2(X, W, 0, 3); SB(); M2(X, W, 1, 3); SB(); M2(X, W, 2, 3); SB(); M2(X, W, 3, 3); if (DMA) PC2(DS, 3); SB(); } while (0)
  WAIT_BAR(6);
#pragma unroll
  for (int i = 0; i < 4; ++i) { RDX(xa, i, 0); RDW(wa, i, 0); }
#define EV(SC, SN, W_, RD, DMA) do { WAIT_BAR(W_); SB(); STEP2(xa, wa, xb, wb, RD, SN, DMA, SC); } while (0)
#define OD(SC, SN, W_, RD, DMA) do { WAIT_BAR(W_); SB(); STEP2(xb, wb, xa, wa, RD, SN, DMA, SC); } while (0)
  u64 oflag = 0;
  for (int it = 0; it < 6; ++it) {

    EV(0, 1, 12, true, true);
    if (it == 5) {
      if (w == 0) {
        asm volatile("" : "+v"(oflag));
        if (!__all(l >= 8 || oflag == tag)) await(ws, l < 8 ? FL_O + (2 * p + (l >> 2)) * 8 + 4 * kh + (l & 3) : -1, tag);
      }
      rW = mkrsrc((const char*)wo - 1024, D_ * D_ * 2 + 1024);
      aso = ((112 + 16 * kh) * 128 + 8 * p + 2 * w) * 1024 - 131072; wso = 16 * kh * 65536 - 65536;
    }
    OD(1, 2, 12, true, true); EV(2, 3, 12, true, true); OD(3, 4, 12, true, true); EV(4, 0, 12, true, true);
    OD(0, 1, 12, true, true); EV(1, 2, 12, true, true);
    if (it == 4 && w == 0) {
      const GAS u64* fo = flagp(ws, FL_O + (2 * p + ((l >> 2) & 1)) * 8 + 4 * kh + (l & 3));
      asm volatile("global_load_dwordx2 %0, %1, off sc1" : "=&v"(oflag) : "v"(fo));
    }
    OD(2, 3, 12, true, true); EV(3, 4, 12, true, true); OD(4, 0, 12, true, true);
  }
  EV(0, 1, 12, true, true); OD(1, 2, 12, true, true); EV(2, 3, 12, true, true); OD(3, 4, 12, true, true); EV(4, 0, 12, true, true); OD(0, 1, 12, true, true); EV(1, 2, 12, true, true);
  u32x2 xres[2][4], bres[4];
  { const int le = lane_id();
    const u16* xp = x + (size_t)(128 * p + 64 * wm + 32 * kh + (le & 15)) * D_ + 128 * nt + 64 * wn + 4 * (le >> 4); const u16* xq = xp + 16 * D_; const u16* bp = b2 + 128 * nt + 64 * wn + 4 * (le >> 4);
    asm volatile("global_load_dwordx2 %0, %8, off\n\tglobal_load_dwordx2 %1, %8, off offset:32\n\tglobal_load_dwordx2 %2, %8, off offset:64\n\tglobal_load_dwordx2 %3, %8, off offset:96\n\t"
                 "global_load_dwordx2 %4, %9, off\n\tglobal_load_dwordx2 %5, %9, off offset:32\n\tglobal_load_dwordx2 %6, %9, off offset:64\n\tglobal_load_dwordx2 %7, %9, off offset:96"
                 : "=&v"(xres[0][0]), "=&v"(xres[0][1]), "=&v"(xres[0][2]), "=&v"(xres[0][3]), "=&v"(xres[1][0]), "=&v"(xres[1][1]), "=&v"(xres[1][2]), "=&v"(xres[1][3]) : "v"(xp), "v"(xq));
    asm volatile("global_load_dwordx2 %0, %4, off\n\tglobal_load_dwordx2 %1, %4, off offset:32\n\tglobal_load_dwordx2 %2, %4, off offset:64\n\tglobal_load_dwordx2 %3, %4, off offset:96"
                 : "=&v"(bres[0]), "=&v"(bres[1]), "=&v"(bres[2]), "=&v"(bres[3]) : "v"(bp)); }
  OD(2, 3, 24, true, false); EV(3, 4, 20, true, false); OD(4, 0, 16, true, false); EV(0, 1, 12, true, false); OD(1, 2, 12, false, false);
  if (PROBE == 4) probe = __builtin_amdgcn_s_memrealtime() - t_in;
  if (kh) {
#pragma unroll
    for (int n = 0; n < 4; ++n) { f32x4 t = acc[0][n]; acc[0][n] = acc[2][n]; acc[2][n] = t; t = acc[1][n]; acc[1][n] = acc[3][n]; acc[3][n] = t; }
#pragma unroll
    for (int n = 0; n < 4; ++n) { PIN(acc[0][n]); PIN(acc[1][n]); PIN(acc[2][n]); PIN(acc[3][n]); }
  }
  const int le = lane_id(), ge = le >> 4;
  const int token0 = 128 * p + 64 * wm + 32 * kh + (le & 15);
  {
    const __amdgpu_buffer_rsrc_t rP = mkrsrc(ws + WS_PART + (size_t)gi * 32768, 16384);
#pragma unroll
    for (int m = 0; m < 2; ++m)
#pragma unroll
      for (int n2 = 0; n2 < 2; ++n2)
        st_wt16(rP, ((w * 2 + m) * 2 + n2) * 1024 + le * 16, (u32x4){cvtpk(acc[2 + m][2 * n2][0], acc[2 + m][2 * n2][1]), cvtpk(acc[2 + m][2 * n2][2], acc[2 + m][2 * n2][3]),
                                                                       cvtpk(acc[2 + m][2 * n2 + 1][0], acc[2 + m][2 * n2 + 1][1]), cvtpk(acc[2 + m][2 * n2 + 1][2], acc[2 + m][2 * n2 + 1][3])});
    asm volatile("s_waitcnt vmcnt(0)\n\ts_barrier" : "+v"(xres[0][0]), "+v"(xres[0][1]), "+v"(xres[0][2]), "+v"(xres[0][3]), "+v"(xres[1][0]), "+v"(xres[1][1]), "+v"(xres[1][2]), "+v"(xres[1][3]),
                 "+v"(bres[0]), "+v"(bres[1]), "+v"(bres[2]), "+v"(bres[3]) :: "memory");
    if (w == 0) { if (le == 0) signal(ws, FL_X + gi, tag); await(ws, FL_X + partner, tag); }
    asm volatile("s_barrier" ::: "memory");
  }
  const __amdgpu_buffer_rsrc_t rPP = mkrsrc(ws + WS_PART + (size_t)partner * 32768, 16384);
  u32x4 pv[2][2];
#pragma unroll
  for (int m = 0; m < 2; ++m)
#pragma unroll
    for (int n2 = 0; n2 < 2; ++n2) pv[m][n2] = __builtin_amdgcn_raw_buffer_load_b128(rPP, ((w * 2 + m) * 2 + n2) * 1024 + le * 16, 0, 16);
#pragma unroll
  for (int m = 0; m < 2; ++m) {
    unsigned d[4][2];
#pragma unroll
    for (int n = 0; n < 4; ++n) {
      const unsigned p0 = pv[m][n >> 1][2 * (n & 1)], p1 = pv[m][n >> 1][2 * (n & 1) + 1];
      const f32x4 a4 = acc[m][n];
      d[n][0] = cvtpk(a4[0] + lo2f(p0) + lo2f(bres[n][0]) + lo2f(xres[m][n][0]), a4[1] + hi2f(p0) + hi2f(bres[n][0]) + hi2f(xres[m][n][0]));
      d[n][1] = cvtpk(a4[2] + lo2f(p1) + lo2f(bres[n][1]) + lo2f(xres[m][n][1]), a4[3] + hi2f(p1) + hi2f(bres[n][1]) + hi2f(xres[m][n][1]));
    }
    const int cch = ((ge & 1) << 1) | (ge >> 1);
#pragma unroll
    for (int n2 = 0; n2 < 2; ++n2) {
      const u32x4 ch = chunk_of(d[2 * n2][0], d[2 * n2][1], d[2 * n2 + 1][0], d[2 * n2 + 1][1]);
      st_wt16(mkrsrc(out, S_ * D_ * 2), ((token0 + 16 * m) * D_ + 128 * nt + 64 * wn + 32 * n2 + 8 * cch) * 2, ch);
    }
  }
#undef RDX
#undef RDW
}

constexpr int LDS_BYTES = 81920;
__global__ __launch_bounds__(256, 2) void k_main(const u16* __restrict__ x, const u16* __restrict__ wq, const u16* __restrict__ wk, const u16* __restrict__ wv, const u16* __restrict__ wo,
                                                  const u16* __restrict__ w1, const u16* __restrict__ b1, const u16* __restrict__ w2, const u16* __restrict__ b2, u16* __restrict__ out, char* __restrict__ ws) {
  extern __shared__ __attribute__((aligned(16))) char lds_[];
  LAS char* lds = (LAS char*)lds_;
  const int w = __builtin_amdgcn_readfirstlane(threadIdx.x >> 6);
  const unsigned long long t_in = __builtin_amdgcn_s_memrealtime(); unsigned long long probe = 0;
  u64 eraw;
  { const GAS u64* ep = flagp(ws, FL_E); asm volatile("global_load_dwordx2 %0, %1, off sc1" : "=&v"(eraw) : "v"(ep)); }
  if ((blockIdx.x >> 8) == 0) {
    __builtin_amdgcn_s_setprio(PRIO_Q);
    const u64 tag = phase0(blockIdx.x, x, wk, wv, ws, lds, w, eraw);
    role_attn(blockIdx.x, wq, w2, wo, ws, lds, w, tag);
  } else {
    __builtin_amdgcn_s_setprio(PRIO_G1);
    const u64 tag = role_w1(t_in, probe, blockIdx.x & 255, wq, w1, b1, w2, ws, lds, w, eraw);
    role_w2(t_in, probe, blockIdx.x & 255, w2, wo, b2, x, out, ws, lds, w, tag);
    if (PROBE == 5) probe = __builtin_amdgcn_s_memrealtime() - t_in;
    if (blockIdx.x == 256 + 17 && PROBE > 0) { const unsigned long long t0 = __builtin_amdgcn_s_memrealtime(); while (__builtin_amdgcn_s_memrealtime() - t0 < probe) __builtin_amdgcn_s_sleep(1); }
  }
}

extern "C" void kernel_launch(void* const* d_in, const int* in_sizes, int n_in, void* d_out, int out_size, void* d_ws, size_t ws_size, hipStream_t stream) {
  const u16* x = (const u16*)d_in[0]; const u16* wq = (const u16*)d_in[1]; const u16* wk = (const u16*)d_in[2];
  const u16* wv = (const u16*)d_in[3]; const u16* wo = (const u16*)d_in[4];
  const u16* w1 = (const u16*)d_in[6]; const u16* b1 = (const u16*)d_in[7]; const u16* w2 = (const u16*)d_in[8]; const u16* b2 = (const u16*)d_in[9];
  (void)hipFuncSetAttribute((const void*)k_main, hipFuncAttributeMaxDynamicSharedMemorySize, LDS_BYTES);
  k_main<<<512, 256, LDS_BYTES, stream>>>(x, wq, wk, wv, wo, w1, b1, w2, b2, (u16*)d_out, (char*)d_ws);
}
```

```cpp
#include <hip/hip_runtime.h>
#include <stdint.h>

typedef unsigned short u16;
typedef __bf16 bf16x8 __attribute__((ext_vector_type(8)));
typedef __bf16 bf16x2 __attribute__((ext_vector_type(2)));
typedef short s16x8 __attribute__((ext_vector_type(8)));
typedef float f32x4 __attribute__((ext_vector_type(4)));
typedef float f32x2 __attribute__((ext_vector_type(2)));
typedef unsigned u32x4 __attribute__((ext_vector_type(4)));
typedef unsigned u32x2 __attribute__((ext_vector_type(2)));
#define LAS __attribute__((address_space(3)))
#define GAS __attribute__((address_space(1)))
#define DI __device__ __forceinline__
#define SB() __builtin_amdgcn_sched_barrier(0)
#define PIN(x) asm volatile("" : "+v"(x))
#define MFMA16(a, b, c) __builtin_amdgcn_mfma_f32_16x16x32_bf16((a), (b), (c), 0, 0, 0)
#define BF(x) __builtin_bit_cast(bf16x8, x)

constexpr int S_ = 2048, D_ = 1024, F_ = 3584;
constexpr size_t WS_FLAGS = 0;
constexpr size_t WS_R2   = 131072;
constexpr size_t WS_KV   = WS_R2 + 8192;
constexpr size_t WS_XN   = WS_KV + 262144;
constexpr size_t WS_H    = WS_XN + 4194304;
constexpr size_t WS_O    = WS_H + (size_t)112 * 128 * 1024;
constexpr size_t WS_PART = WS_O + 4194304;

constexpr float QSCALE = 0.17677669529663687f * 1.4426950408889634f;

DI int lane_id() { int l; asm volatile("v_mbcnt_lo_u32_b32 %0, -1, 0\n\tv_mbcnt_hi_u32_b32 %0, -1, %0" : "=v"(l)); return l; }
DI unsigned cvtpk(float lo, float hi) { f32x2 v = {lo, hi}; bf16x2 b = __builtin_convertvector(v, bf16x2); return __builtin_bit_cast(unsigned, b); }
DI float lo2f(unsigned u) { return __uint_as_float(u << 16); }
DI float hi2f(unsigned u) { return __uint_as_float(u & 0xffff0000u); }
DI __amdgpu_buffer_rsrc_t mkrsrc(const void* p, unsigned bytes) { return __builtin_amdgcn_make_buffer_rsrc((void*)p, 0, bytes, 0x00020000); }
DI void dma16(unsigned lds_addr, unsigned voff, __amdgpu_buffer_rsrc_t rsrc, unsigned soff) {
  asm volatile("s_mov_b32 m0, %0\n\ts_nop 0\n\tbuffer_load_dwordx4 %1, %2, %3 offen lds" :: "s"(lds_addr), "v"(voff), "s"(rsrc), "s"(soff));
}
template <int IMM, int INC> DI void dma1a(unsigned mbase, unsigned v0, __amdgpu_buffer_rsrc_t rsrc, unsigned& soff) {
  asm volatile("s_add_u32 m0, %2, %4\n\ts_add_u32 %0, %0, %5\n\tbuffer_load_dwordx4 %1, %3, %0 offen lds" : "+s"(soff) : "v"(v0), "s"(mbase), "s"(rsrc), "i"(IMM), "i"(INC) : "scc");
}
template <int OFF> DI void dma1b(unsigned v0, __amdgpu_buffer_rsrc_t rsrc, unsigned soff) {
  asm volatile("buffer_load_dwordx4 %0, %1, %2 offen offset:%3 lds" :: "v"(v0), "s"(rsrc), "s"(soff), "i"(OFF));
}
template <int IMM, int INC> DI void dma1a_c(unsigned mbase, unsigned v0, __amdgpu_buffer_rsrc_t rsrc, unsigned& soff) {
  asm volatile("s_add_u32 m0, %2, %4\n\ts_add_u32 %0, %0, %5\n\tbuffer_load_dwordx4 %1, %3, %0 offen sc1 lds" : "+s"(soff) : "v"(v0), "s"(mbase), "s"(rsrc), "i"(IMM), "i"(INC) : "scc");
}
template <int OFF> DI void dma1b_c(unsigned v0, __amdgpu_buffer_rsrc_t rsrc, unsigned soff) {
  asm volatile("buffer_load_dwordx4 %0, %1, %2 offen offset:%3 sc1 lds" :: "v"(v0), "s"(rsrc), "s"(soff), "i"(OFF));
}
DI void dma4(unsigned lds_addr, unsigned voff, __amdgpu_buffer_rsrc_t rsrc, unsigned soff) {
  asm volatile("s_mov_b32 m0, %0\n\ts_nop 0\n\tbuffer_load_dword %1, %2, %3 offen lds" :: "s"(lds_addr), "v"(voff), "s"(rsrc), "s"(soff));
}
DI void dma4_c(unsigned lds_addr, unsigned voff, __amdgpu_buffer_rsrc_t rsrc, unsigned soff) {
  asm volatile("s_mov_b32 m0, %0\n\ts_nop 0\n\tbuffer_load_dword %1, %2, %3 offen sc1 lds" :: "s"(lds_addr), "v"(voff), "s"(rsrc), "s"(soff));
}
template <int ADD> DI void dma4_ahead(unsigned lds_addr, unsigned voff, __amdgpu_buffer_rsrc_t rsrc, unsigned soff) {
  unsigned t;
  asm volatile("s_mov_b32 m0, %1\n\ts_add_u32 %0, %4, %5\n\tbuffer_load_dword %2, %3, %0 offen lds" : "=&s"(t) : "s"(lds_addr), "v"(voff), "s"(rsrc), "s"(soff), "i"(ADD) : "scc");
}
#define WAIT_BAR(N) asm volatile("s_waitcnt vmcnt(" #N ") lgkmcnt(0)\n\ts_barrier" ::: "memory")
DI bf16x8 ldsfrag(const LAS char* p) { return *(const LAS bf16x8*)p; }
typedef short v4i16_t __attribute__((ext_vector_type(4)));
DI bf16x8 ldstr(const LAS char* p0, const LAS char* p1) {
  v4i16_t a = __builtin_amdgcn_ds_read_tr16_b64_v4i16((LAS v4i16_t*)p0);
  v4i16_t b = __builtin_amdgcn_ds_read_tr16_b64_v4i16((LAS v4i16_t*)p1);
  s16x8 v = __builtin_shufflevector(a, b, 0, 1, 2, 3, 4, 5, 6, 7);
  return __builtin_bit_cast(bf16x8, v);
}
DI void st_wt16(__amdgpu_buffer_rsrc_t r, unsigned voff, u32x4 v) { __builtin_amdgcn_raw_buffer_store_b128(v, r, voff, 0, 17); }
DI u32x4 chunk_of(unsigned x0, unsigned x1, unsigned y0, unsigned y1) {
  auto s0 = __builtin_amdgcn_permlane16_swap(x0, y0, false, false);
  auto s1 = __builtin_amdgcn_permlane16_swap(x1, y1, false, false);
  return (u32x4){s0[0], s1[0], s0[1], s1[1]};
}
DI unsigned wdma_off(int l, int kblk, int ldbytes) {
  const int row = l >> 3, c8 = l & 7, sw = ((row >> 1) & 1) | ((kblk & 1) << 1);
  return row * ldbytes + (((c8 >> 1) ^ sw) * 32) + (c8 & 1) * 16;
}
DI unsigned wrd_off(int l, int seg) {
  const int g = l >> 4, q = (l & 15) >> 2, sw = ((q >> 1) & 1) | ((g & 1) << 1);
  return q * 128 + ((seg ^ sw) * 32) + (l & 3) * 8;
}
DI float xsum4(float v) {
  auto a = __builtin_amdgcn_permlane16_swap(__float_as_uint(v), __float_as_uint(v), false, false); v = __uint_as_float(a[0]) + __uint_as_float(a[1]);
  auto b = __builtin_amdgcn_permlane32_swap(__float_as_uint(v), __float_as_uint(v), false, false); return __uint_as_float(b[0]) + __uint_as_float(b[1]);
}
DI float xmax4(float v) {
  auto a = __builtin_amdgcn_permlane16_swap(__float_as_uint(v), __float_as_uint(v), false, false); v = fmaxf(__uint_as_float(a[0]), __uint_as_float(a[1]));
  auto b = __builtin_amdgcn_permlane32_swap(__float_as_uint(v), __float_as_uint(v), false, false); return fmaxf(__uint_as_float(b[0]), __uint_as_float(b[1]));
}

typedef unsigned long long u64;
constexpr int FL_E = 0, FL_RD = 16, FL_XN = 528, FL_KV = 784, FL_H = 1040, FL_O = 1296, FL_X = 1552;
DI GAS u64* flagp(char* ws, int i) { return (GAS u64*)(ws + WS_FLAGS + (size_t)i * 64); }
DI void signal(char* ws, int i, u64 tag) { __hip_atomic_store(flagp(ws, i), tag, __ATOMIC_RELAXED, __HIP_MEMORY_SCOPE_AGENT); }
DI u64 take_tag(char* ws, u64 eraw, int w) {
  asm volatile("" : "+v"(eraw));
  const u64 tag = (((u64)(unsigned)__builtin_amdgcn_readfirstlane((unsigned)(eraw >> 32))) << 32 | (unsigned)__builtin_amdgcn_readfirstlane((unsigned)eraw)) + 1;
  if (w == 0 && lane_id() == 0) signal(ws, FL_RD + blockIdx.x, tag);
  return tag;
}
DI void await(char* ws, int idx, u64 tag) {
  GAS u64* f = flagp(ws, idx < 0 ? FL_E : idx);
  for (unsigned spins = 0; spins < (1u << 17); ++spins) {
    const u64 v = __hip_atomic_load(f, __ATOMIC_RELAXED, __HIP_MEMORY_SCOPE_AGENT);
    if (__all(idx < 0 || v == tag)) break;
    __builtin_amdgcn_s_sleep(2);
  }
  asm volatile("" ::: "memory");
}

template <int N> DI void await_n(char* ws, int idx, u64 tag) {
  for (unsigned spins = 0; spins < (1u << 17); ++spins) {
    bool ok = true;
#pragma unroll
    for (int i = 0; i < N; ++i) ok &= __hip_atomic_load(flagp(ws, idx + 64 * i), __ATOMIC_RELAXED, __HIP_MEMORY_SCOPE_AGENT) == tag;
    if (__all(ok)) break;
    __builtin_amdgcn_s_sleep(2);
  }
  asm volatile("" ::: "memory");
}

DI u64 phase0(int a, const u16* __restrict__ x, const u16* __restrict__ wk, const u16* __restrict__ wv, char* __restrict__ ws, LAS char* lds, const int w, const u64 eraw) {
  const int l = lane_id(), tid = w * 64 + l;
  LAS float* part = (LAS float*)(lds + 65536);
  LAS float* fin = (LAS float*)(lds + 65536 + 8192);
  LAS float* red = (LAS float*)(lds + 65536 + 8192 + 2048);
  const int rb = a & 127, hf = a >> 7;
  const int r = l & 15, c = l >> 4;
  u32x4 xv[8];
  { const u16* xr = x + (size_t)(rb * 16 + r) * D_ + w * 256 + c * 8;
#define XL(i) asm volatile("global_load_dwordx4 %0, %1, off offset:" #i "*64" : "=&v"(xv[i]) : "v"(xr))
    XL(0); XL(1); XL(2); XL(3); XL(4); XL(5); XL(6); XL(7); }
  const unsigned lds0 = (unsigned)(uintptr_t)lds;
  {
    __amdgpu_buffer_rsrc_t rw = mkrsrc(hf ? wv : wk, 65536);
#pragma unroll
    for (int i = 0; i < 16; ++i) dma16(lds0 + w * 16384 + i * 1024, l * 16, rw, w * 16384 + i * 1024);
  }
  asm volatile("s_waitcnt vmcnt(16)" : "+v"(xv[0]), "+v"(xv[1]), "+v"(xv[2]), "+v"(xv[3]), "+v"(xv[4]), "+v"(xv[5]), "+v"(xv[6]), "+v"(xv[7]));
  const u64 tag = take_tag(ws, eraw, w);
  f32x2 v[32];
#pragma unroll
  for (int i = 0; i < 8; ++i)
#pragma unroll
    for (int j = 0; j < 4; ++j) v[4 * i + j] = (f32x2){lo2f(xv[i][j]), hi2f(xv[i][j])};
  f32x2 s2 = (v[0] + v[1]) + (v[2] + v[3]);
#pragma unroll
  for (int i = 4; i < 32; i += 4) s2 += (v[i] + v[i + 1]) + (v[i + 2] + v[i + 3]);
  const float s = xsum4(s2[0] + s2[1]);
  if (c == 0) red[w * 16 + r] = s;
  asm volatile("s_waitcnt lgkmcnt(0)\n\ts_barrier" ::: "memory");
  const float mean = ((red[r] + red[16 + r]) + (red[32 + r] + red[48 + r])) * (1.0f / D_);
  f32x2 q2 = {0.f, 0.f}, q3 = {0.f, 0.f};
#pragma unroll
  for (int i = 0; i < 32; i += 2) { v[i] -= mean; v[i + 1] -= mean; q2 += v[i] * v[i]; q3 += v[i + 1] * v[i + 1]; }
  const float q = xsum4((q2[0] + q2[1]) + (q3[0] + q3[1]));
  if (c == 0) red[64 + w * 16 + r] = q;
  asm volatile("s_waitcnt lgkmcnt(0)\n\ts_barrier" ::: "memory");
  const float var = ((red[64 + r] + red[80 + r]) + (red[96 + r] + red[112 + r])) * (1.0f / D_);
  const float rstd = 1.0f / sqrtf(var + 1e-5f);
#pragma unroll
  for (int i = 0; i < 8; ++i)
#pragma unroll
    for (int j = 0; j < 4; ++j) { const f32x2 t = v[4 * i + j] * rstd; xv[i][j] = cvtpk(t[0], t[1]); }
  const __amdgpu_buffer_rsrc_t rXN = mkrsrc(ws + WS_XN, 4194304);
#pragma unroll
  for (int i = 0; i < 4; ++i) st_wt16(rXN, ((8 * w + 2 * i + hf) * 128 + rb) * 1024 + l * 16, hf ? xv[2 * i + 1] : xv[2 * i]);
  if (hf == 0 && w == 0 && c == 0) __hip_atomic_store((GAS float*)(ws + WS_R2) + rb * 16 + r, 1.0f / sqrtf(var * rstd * rstd + 1e-5f), __ATOMIC_RELAXED, __HIP_MEMORY_SCOPE_AGENT);
  asm volatile("s_waitcnt vmcnt(0)\n\ts_barrier" ::: "memory");
  if (tid == 0) signal(ws, FL_XN + a, tag);
  f32x4 acc[2] = {{0.f, 0.f, 0.f, 0.f}, {0.f, 0.f, 0.f, 0.f}};
  const LAS char* wb = (const LAS char*)lds + w * 16384 + (8 * c + ((l & 15) >> 2)) * 64 + (l & 3) * 8;
#pragma unroll
  for (int i = 0; i < 8; ++i)
#pragma unroll
    for (int b = 0; b < 2; ++b) {
      bf16x8 wf = ldstr(wb + i * 2048 + b * 32, wb + i * 2048 + b * 32 + 256);
      acc[b] = MFMA16(BF(xv[i]), wf, acc[b]);
    }
#pragma unroll
  for (int b = 0; b < 2; ++b)
#pragma unroll
    for (int j = 0; j < 4; ++j) part[(w * 16 + 4 * c + j) * 32 + 16 * b + r] = acc[b][j];
  asm volatile("s_waitcnt lgkmcnt(0)\n\ts_barrier" ::: "memory");
#pragma unroll
  for (int u = 0; u < 2; ++u) { const int e = tid + 256 * u; fin[e] = (part[e] + part[512 + e]) + (part[1024 + e] + part[1536 + e]); }
  asm volatile("s_waitcnt lgkmcnt(0)\n\ts_barrier" ::: "memory");
  if (w == 0) {
    float o[8];
    unsigned dst = (rb >> 1) * 4096;
    const int half = rb & 1;
    if (hf == 0) { const int kb = l >> 5, g = (l >> 3) & 3, rr = l & 7;
      const int row = 8 * half + rr, key = 8 * (row >> 2) + 4 * kb + (row & 3) - 16 * half;
#pragma unroll
      for (int j = 0; j < 8; ++j) o[j] = fin[key * 32 + 16 * (j >> 2) + 4 * g + (j & 3)];
      dst += kb * 1024 + (g * 16 + row) * 16;
    } else { const int db = l >> 5, gl = (l >> 4) & 1, rr = l & 15;
#pragma unroll
      for (int j = 0; j < 8; ++j) o[j] = fin[(8 * gl + j) * 32 + 16 * db + rr];
      dst += 2048 + db * 1024 + ((2 * half + gl) * 16 + rr) * 16;
    }
    st_wt16(mkrsrc(ws + WS_KV, 262144), dst, (u32x4){cvtpk(o[0], o[1]), cvtpk(o[2], o[3]), cvtpk(o[4], o[5]), cvtpk(o[6], o[7])});
    asm volatile("s_waitcnt vmcnt(0)" ::: "memory");
    if (l == 0) signal(ws, FL_KV + a, tag);
  }
  return tag;
}

constexpr int PRIO_Q = 1, PRIO_A = 1, PRIO_G1 = 0, PRIO_GE = 2, PRIO_G2 = 0;
constexpr int G1PAT = 0x09;
constexpr int PFSLEEP = 30, PFROWS = 256, AMAP = 4;

DI void attn_slow(const char* __restrict__ kv, bf16x8 qf, f32x4& o0, f32x4& o1) {
  const int l = lane_id();
  const __amdgpu_buffer_rsrc_t rk = mkrsrc(kv, 262144);
  o0 = (f32x4){0.f, 0.f, 0.f, 0.f}; o1 = o0;
  float m = -3.0e38f, ls = 0.f;
#pragma unroll 1
  for (int t = 0; t < 64; ++t) {
    const int p = t * 4096 + l * 16;
    const bf16x8 k0 = BF(__builtin_amdgcn_raw_buffer_load_b128(rk, p, 0, 16)), k1 = BF(__builtin_amdgcn_raw_buffer_load_b128(rk, p + 1024, 0, 16)),
                 v0 = BF(__builtin_amdgcn_raw_buffer_load_b128(rk, p + 2048, 0, 16)), v1 = BF(__builtin_amdgcn_raw_buffer_load_b128(rk, p + 3072, 0, 16));
    const f32x4 z = {0.f, 0.f, 0.f, 0.f};
    f32x4 s0 = MFMA16(k0, qf, z), s1 = MFMA16(k1, qf, z);
    float mx = fmaxf(fmaxf(fmaxf(s0[0], s0[1]), fmaxf(s0[2], s0[3])), fmaxf(fmaxf(s1[0], s1[1]), fmaxf(s1[2], s1[3])));
    mx = fmaxf(mx, __shfl_xor(mx, 16)); mx = fmaxf(mx, __shfl_xor(mx, 32));
    const float mn = fmaxf(m, mx), al = __builtin_amdgcn_exp2f(m - mn);
    m = mn; float rs = 0.f;
#pragma unroll
    for (int i = 0; i < 4; ++i) { s0[i] = __builtin_amdgcn_exp2f(s0[i] - mn); s1[i] = __builtin_amdgcn_exp2f(s1[i] - mn); rs += s0[i] + s1[i]; o0[i] *= al; o1[i] *= al; }
    rs += __shfl_xor(rs, 16); rs += __shfl_xor(rs, 32); ls = ls * al + rs;
    const u32x4 pp = {cvtpk(s0[0], s0[1]), cvtpk(s0[2], s0[3]), cvtpk(s1[0], s1[1]), cvtpk(s1[2], s1[3])};
    o0 = MFMA16(v0, BF(pp), o0); o1 = MFMA16(v1, BF(pp), o1);
  }
#pragma unroll
  for (int i = 0; i < 4; ++i) { o0[i] /= ls; o1[i] /= ls; }
}

DI void role_attn(int a, const u16* __restrict__ wq, const u16* __restrict__ w2, const u16* __restrict__ wo, char* __restrict__ ws, LAS char* lds, const int w, const u64 tag) {
  const int l = lane_id();
  constexpr int NTB = 32 / AMAP;
  const int xcd = a & 7, idx = a >> 3, tb = (xcd / (8 / AMAP)) * NTB + (idx % NTB), hg = (xcd % (8 / AMAP)) * AMAP + idx / NTB;
  const int g = l >> 4;
  const unsigned lds0 = (unsigned)(uintptr_t)lds;
  constexpr int SLOT = 24576;
  const __amdgpu_buffer_rsrc_t rX = mkrsrc(ws + WS_XN, 4194304), rKV = mkrsrc(ws + WS_KV, 262144);
  const unsigned vlin = l * 16;
  constexpr int QS = 12288;
  const __amdgpu_buffer_rsrc_t rWq = mkrsrc((const char*)wq - 1024, D_ * D_ * 2 + 1024);
  const unsigned vq0 = wdma_off(l, w, 2048) + hg * 256 + w * 8 * 2048 + 1024, vq1 = vq0 + 128 - 1024;
  unsigned xso = (4 * tb + w) * 1024 - 131072, qso = 0u - 65536;
  const unsigned mx = lds0 + w * 1024, mq = lds0 + 4096 + w * 2048;
#define PCQ(SL, i) do { if constexpr ((i) == 0) dma1a_c<(SL) * QS, 131072>(mx, vlin, rX, xso); else if constexpr ((i) == 1) dma1a<(SL) * QS, 65536>(mq, vq0, rWq, qso); else dma1b<1024>(vq1, rWq, qso); } while (0)
#define STGQ(SL) do { PCQ(SL, 0); PCQ(SL, 1); PCQ(SL, 2); } while (0)
  if (w == 0) await(ws, l < 8 ? FL_XN + 4 * tb + (l & 3) + 128 * (l >> 2) : -1, tag);
  asm volatile("s_waitcnt lgkmcnt(0)\n\ts_barrier" ::: "memory");
  constexpr int NPL = 64 / (4 * NTB);
  const int pl = ((idx % NTB) * 4 + w) * NPL + l;
  const unsigned vpq = l < NPL ? (pl >> 1) * 2048 + hg * 256 + (pl & 1) * 128 + 1024 : 0x80000000u;
  const unsigned mpq = lds0 + 6 * QS;
  STGQ(0); STGQ(1); STGQ(2); STGQ(3); STGQ(4); STGQ(5);
  dma4_ahead<1 * 65536>(mpq, vpq, rWq, qso); dma4_ahead<2 * 65536>(mpq, vpq, rWq, qso); dma4_ahead<3 * 65536>(mpq, vpq, rWq, qso);
  dma4_ahead<4 * 65536>(mpq, vpq, rWq, qso); dma4_ahead<5 * 65536>(mpq, vpq, rWq, qso); dma4_ahead<6 * 65536>(mpq, vpq, rWq, qso);
  f32x4 qacc[2][4];
#pragma unroll
  for (int i = 0; i < 2; ++i)
#pragma unroll
    for (int j = 0; j < 4; ++j) qacc[i][j] = (f32x4){0.f, 0.f, 0.f, 0.f};
  const LAS char* xrd = lds + l * 16;
  const LAS char* wrd0 = lds + 4096 + (g * 2 + (w >> 1)) * 1024 + wrd_off(l, 2 * (w & 1));
  const LAS char* wrd1 = lds + 4096 + (g * 2 + (w >> 1)) * 1024 + wrd_off(l, 2 * (w & 1) + 1);
  bf16x8 qxa[4], qwa[2], qxb[4], qwb[2];
#define QRX(X, m, SL) X[m] = ldsfrag(xrd + (SL) * QS + (m) * 1024)
#define QRW(W, n, SL) W[n] = ((n) == 0 ? ldstr(wrd0 + (SL) * QS, wrd0 + (SL) * QS + 512) : ldstr(wrd1 + (SL) * QS, wrd1 + (SL) * QS + 512))
#define QM(X, W, m, n) qacc[n][m] = MFMA16(W[n], X[m], qacc[n][m])
#define STEPQ(X, W, XN, WN, RD, RS, DMA, DS) do { \
    QM(X, W, 0, 0); if (RD) { QRW(WN, 0, RS); QRX(XN, 0, RS); } SB(); QM(X, W, 1, 0); if (RD) { QRW(WN, 1, RS); QRX(XN, 1, RS); } SB(); QM(X, W, 2, 0); if (RD) QRX(XN, 2, RS); SB(); QM(X, W, 3, 0); if (RD) QRX(XN, 3, RS); SB(); \
    QM(X, W, 0, 1); if (DMA) PCQ(DS, 0); SB(); QM(X, W, 1, 1); if (DMA) PCQ(DS, 1); SB(); QM(X, W, 2, 1); if (DMA) PCQ(DS, 2); SB(); QM(X, W, 3, 1); if (DMA) dma4_ahead<6 * 65536>(mpq, vpq, rWq, qso); SB(); } while (0)
  WAIT_BAR(21);
#pragma unroll
  for (int m = 0; m < 4; ++m) QRX(qxa, m, 0);
  QRW(qwa, 0, 0); QRW(qwa, 1, 0);
#define QEV(SC, SN, W_, RD, DMA) do { WAIT_BAR(W_); SB(); STEPQ(qxa, qwa, qxb, qwb, RD, SN, DMA, SC); } while (0)
#define QOD(SC, SN, W_, RD, DMA) do { WAIT_BAR(W_); SB(); STEPQ(qxb, qwb, qxa, qwa, RD, SN, DMA, SC); } while (0)
  QEV(0, 1, 18, true, true); QOD(1, 2, 19, true, true); QEV(2, 3, 20, true, true); QOD(3, 4, 21, true, true); QEV(4, 5, 22, true, true); QOD(5, 0, 17, true, true);
  for (int it = 0; it < 3; ++it) { QEV(0, 1, 17, true, true); QOD(1, 2, 17, true, true); QEV(2, 3, 17, true, true); QOD(3, 4, 17, true, true); QEV(4, 5, 17, true, true); QOD(5, 0, 17, true, true); }
  QEV(0, 1, 17, true, true); QOD(1, 2, 17, true, true);
  QEV(2, 3, 17, true, false); QOD(3, 4, 13, true, false); QEV(4, 5, 9, true, false); QOD(5, 0, 5, true, false);
  if (w == 0) await_n<4>(ws, FL_KV + l, tag);
  QEV(0, 1, 0, true, false);
  unsigned kvso = w * 4096 - 16384;
  const unsigned kvm = lds0 + w * 4096;
#define STGKV(SF) do { dma1a_c<(SF) * SLOT, 16384>(kvm, vlin, rKV, kvso); dma1b_c<1024>(vlin, rKV, kvso); dma1b_c<2048>(vlin, rKV, kvso); dma1b_c<3072>(vlin, rKV, kvso); } while (0)
  STGKV(1); STGKV(2);
  {
    const unsigned line = (a * 4 + w) * 64 + l;
    dma4(lds0 + 6 * QS, line * 128, mkrsrc(w2, F_ * D_ * 2), 0);
    dma4(lds0 + 6 * QS, line * 128, mkrsrc(wo, D_ * D_ * 2), 0);
  }
  STEPQ(qxb, qwb, qxa, qwa, false, 0, false, 0);
  __builtin_amdgcn_s_setprio(PRIO_A);
  bf16x8 qf[4];
#pragma unroll
  for (int m = 0; m < 4; ++m)
    qf[m] = BF(((u32x4){cvtpk(qacc[0][m][0] * QSCALE, qacc[0][m][1] * QSCALE), cvtpk(qacc[0][m][2] * QSCALE, qacc[0][m][3] * QSCALE),
                        cvtpk(qacc[1][m][0] * QSCALE, qacc[1][m][1] * QSCALE), cvtpk(qacc[1][m][2] * QSCALE, qacc[1][m][3] * QSCALE)}));
  WAIT_BAR(6);
  const LAS char* kvb = lds + l * 16;
  f32x4 negm[4], S[4][2], O[4][2];
  bf16x8 k0, k1;
  {
    const LAS char* p = kvb + 1 * SLOT;
    k0 = ldsfrag(p); k1 = ldsfrag(p + 1024);
    const f32x4 z = {0.f, 0.f, 0.f, 0.f};
#pragma unroll
    for (int m = 0; m < 4; ++m) {
      const f32x4 s0 = MFMA16(k0, qf[m], z), s1 = MFMA16(k1, qf[m], z);
      float mx = fmaxf(fmaxf(fmaxf(s0[0], s0[1]), fmaxf(s0[2], s0[3])), fmaxf(fmaxf(s1[0], s1[1]), fmaxf(s1[2], s1[3])));
      mx = xmax4(mx);
      negm[m] = (f32x4){-mx, -mx, -mx, -mx}; PIN(negm[m]);
      O[m][0] = z; O[m][1] = z;
    }
    S[0][0] = MFMA16(k0, qf[0], negm[0]); S[0][1] = MFMA16(k1, qf[0], negm[0]);
    S[1][0] = MFMA16(k0, qf[1], negm[1]); S[1][1] = MFMA16(k1, qf[1], negm[1]);
  }
  u32x4 P[4];
  P[2] = (u32x4){0, 0, 0, 0}; P[3] = P[2];
  bf16x8 va0 = BF(P[2]), va1 = va0, vb0, vb1;
  S[3][1] = (f32x4){0.f, 0.f, 0.f, 0.f};
  f32x4 L[4];
#pragma unroll
  for (int m = 0; m < 4; ++m) L[m] = (f32x4){0.f, 0.f, 0.f, 0.f};
  bf16x8 ones; { const short o1 = (l & 15) == 0 ? (short)0x3f80 : (short)0; s16x8 ov = {o1, o1, o1, o1, o1, o1, o1, o1}; ones = __builtin_bit_cast(bf16x8, ov); PIN(ones); }
#define LS(m) do { L[m] = MFMA16(ones, BF(P[m]), L[m]); PIN(L[m]); SB(); } while (0)
#define NOLS(m)
#define EX2(m, kb, i) do { float t0_ = __builtin_amdgcn_exp2f(S[m][kb][i]), t1_ = __builtin_amdgcn_exp2f(S[m][kb][i + 1]); PIN(t0_); PIN(t1_); S[m][kb][i] = t0_; S[m][kb][i + 1] = t1_; SB(); } while (0)
#define CVA(m, kb) do { unsigned c_ = cvtpk(S[m][kb][0], S[m][kb][1]); PIN(c_); P[m][2 * kb] = c_; SB(); } while (0)
#define CVB(m, kb) do { unsigned c_ = cvtpk(S[m][kb][2], S[m][kb][3]); PIN(c_); P[m][2 * kb + 1] = c_; SB(); } while (0)
#define GRP(mq, kq, KF, pm, pk, me, ke, mp, db, VF, LSM, X) do { \
    S[mq][kq] = MFMA16(KF, qf[mq], negm[mq]); PIN(S[mq][kq]); SB(); X; CVB(pm, pk); EX2(me, ke, 0); \
    O[mp][db] = MFMA16(VF, BF(P[mp]), O[mp][db]); PIN(O[mp][db]); SB(); EX2(me, ke, 2); LSM(mp); CVA(me, ke); } while (0)
#define TILE(KC0, KC1, KN0, KN1, VP0, VP1, VC0, VC1, kn, vp, D) do { \
    GRP(2, 0, KC0, 3, 1, 0, 0, 2, 0, VP0, NOLS, KN0 = ldsfrag(kn); SB()); \
    GRP(2, 1, KC1, 0, 0, 0, 1, 2, 1, VP1, LS,   KN1 = ldsfrag((kn) + 1024); SB()); \
    GRP(3, 0, KC0, 0, 1, 1, 0, 3, 0, VP0, NOLS, VC0 = ldsfrag(vp); SB()); \
    GRP(3, 1, KC1, 1, 0, 1, 1, 3, 1, VP1, LS,   VC1 = ldsfrag((vp) + 1024); SB()); \
    GRP(0, 0, KN0, 1, 1, 2, 0, 0, 0, VC0, NOLS, D(0)); \
    GRP(0, 1, KN1, 2, 0, 2, 1, 0, 1, VC1, LS,   D(1)); \
    GRP(1, 0, KN0, 2, 1, 3, 0, 1, 0, VC0, NOLS, D(2)); \
    GRP(1, 1, KN1, 3, 0, 3, 1, 1, 1, VC1, LS,   D(3)); \
  } while (0)
#define NOD(i)
  bf16x8 kx0, kx1;
#define STAGE(SC, SN, SF, DM, LAST) do { \
    TILE(k0, k1, kx0, kx1, va0, va1, vb0, vb1, kvb + (SC) * SLOT + 4096, kvb + (SC) * SLOT + 2048, NOD); \
    TILE(kx0, kx1, k0, k1, vb0, vb1, va0, va1, kvb + (SC) * SLOT + 8192, kvb + (SC) * SLOT + 4096 + 2048, NOD); \
    TILE(k0, k1, kx0, kx1, va0, va1, vb0, vb1, kvb + (SC) * SLOT + 12288, kvb + (SC) * SLOT + 8192 + 2048, NOD); \
    WAIT_BAR(0);        \
    TILE(kx0, kx1, k0, k1, vb0, vb1, va0, va1, kvb + ((LAST) ? (SC) : (SN)) * SLOT, kvb + (SC) * SLOT + 12288 + 2048, DM); } while (0)
#define DM_(SF, i) do { if constexpr ((i) == 0) { dma1a_c<(SF) * SLOT, 16384>(kvm, vlin, rKV, kvso); } else { dma1b_c<(i) * 1024>(vlin, rKV, kvso); } SB(); } while (0)
#define DM0(i) DM_(0, i)
#define DM1(i) DM_(1, i)
#define DM2(i) DM_(2, i)
  for (int j = 0; j < 12; j += 3) { STAGE(1, 2, 0, DM0, false); STAGE(2, 0, 1, DM1, false); STAGE(0, 1, 2, DM2, false); }
  STAGE(1, 2, 0, DM0, false); STAGE(2, 0, 1, DM1, false); STAGE(0, 1, 2, NOD, false); STAGE(1, 2, 0, NOD, true);
  CVB(3, 1);
  O[2][0] = MFMA16(va0, BF(P[2]), O[2][0]); O[2][1] = MFMA16(va1, BF(P[2]), O[2][1]); LS(2);
  O[3][0] = MFMA16(va0, BF(P[3]), O[3][0]); O[3][1] = MFMA16(va1, BF(P[3]), O[3][1]); LS(3);
  const __amdgpu_buffer_rsrc_t rO = mkrsrc(ws + WS_O, 4194304);
#pragma unroll
  for (int m = 0; m < 4; ++m) {
    float ls = __shfl(L[m][0], l & 15);
    float inv = 1.0f / ls;
    f32x4 o0 = O[m][0], o1 = O[m][1];
    const float mo = fmaxf(fmaxf(fmaxf(fabsf(o0[0]), fabsf(o0[1])), fmaxf(fabsf(o0[2]), fabsf(o0[3]))), fmaxf(fmaxf(fabsf(o1[0]), fabsf(o1[1])), fmaxf(fabsf(o1[2]), fabsf(o1[3]))));
    const bool ok = (ls <= 1.0e18f) && (mo <= 1.0e37f);
    if (__builtin_expect(__any(!ok), 0)) { attn_slow(ws + WS_KV, qf[m], o0, o1); inv = 1.0f; }
    const u32x4 ch = chunk_of(cvtpk(o0[0] * inv, o0[1] * inv), cvtpk(o0[2] * inv, o0[3] * inv), cvtpk(o1[0] * inv, o1[1] * inv), cvtpk(o1[2] * inv, o1[3] * inv));
    const int c = ((g & 1) << 1) | (g >> 1);
    st_wt16(rO, ((4 * hg + w) * 128 + 4 * tb + m) * 1024 + c * 256 + (l & 15) * 16, ch);
  }
  asm volatile("s_waitcnt vmcnt(0)\n\ts_barrier" ::: "memory");
  if (w == 0 && lane_id() == 0) signal(ws, FL_O + tb * 8 + hg, tag);
}

DI int w2_kh(int gi) { return gi & 1; }
DI void w2_weights_early(int gi, const u16* __restrict__ w2, LAS char* lds, const int w) {
  const int l = lane_id(), nt = gi >> 5;
  const __amdgpu_buffer_rsrc_t rW = mkrsrc((const char*)w2 - 1024, F_ * D_ * 2 + 1024);
  const unsigned vw0 = wdma_off(l, w, 2048) + nt * 256 + w * 8 * 2048 + 1024, vw1 = vw0 + 128 - 1024;
  unsigned wso = 56 * w2_kh(gi) * 65536 - 65536;
  const unsigned mw = (unsigned)(uintptr_t)lds + 8192 + w * 2048;
  dma1a<0 * 16384, 65536>(mw, vw0, rW, wso); dma1b<1024>(vw1, rW, wso); dma1a<1 * 16384, 65536>(mw, vw0, rW, wso); dma1b<1024>(vw1, rW, wso);
  dma1a<2 * 16384, 65536>(mw, vw0, rW, wso); dma1b<1024>(vw1, rW, wso); dma1a<3 * 16384, 65536>(mw, vw0, rW, wso); dma1b<1024>(vw1, rW, wso);
}

DI f32x4 gelu4(f32x4 acc, float rs, f32x4 b, float c1v) {
  constexpr float c2 = -2.0f * 0.7978845608028654f * 1.4426950408889634f * 0.044715f;
  float x0 = acc[0], x1 = acc[1], x2 = acc[2], x3 = acc[3], t0, t1, t2, t3;
  asm("v_fma_f32 %0, %0, %8, %9\n\tv_fma_f32 %1, %1, %8, %10\n\tv_fma_f32 %2, %2, %8, %11\n\tv_fma_f32 %3, %3, %8, %12\n\t"
      "v_mul_f32 %4, %0, %0\n\tv_mul_f32 %5, %1, %1\n\tv_mul_f32 %6, %2, %2\n\tv_mul_f32 %7, %3, %3\n\t"
      "v_fma_f32 %4, %4, %14, %13\n\tv_fma_f32 %5, %5, %14, %13\n\tv_fma_f32 %6, %6, %14, %13\n\tv_fma_f32 %7, %7, %14, %13\n\t"
      "v_mul_f32 %4, %0, %4\n\tv_mul_f32 %5, %1, %5\n\tv_mul_f32 %6, %2, %6\n\tv_mul_f32 %7, %3, %7\n\t"
      "v_exp_f32 %4, %4\n\tv_exp_f32 %5, %5\n\tv_exp_f32 %6, %6\n\tv_exp_f32 %7, %7\n\t"
      "v_add_f32 %4, 1.0, %4\n\tv_add_f32 %5, 1.0, %5\n\tv_add_f32 %6, 1.0, %6\n\tv_add_f32 %7, 1.0, %7\n\t"
      "v_rcp_f32 %4, %4\n\tv_rcp_f32 %5, %5\n\tv_rcp_f32 %6, %6\n\tv_rcp_f32 %7, %7\n\t"
      "v_mul_f32 %0, %0, %4\n\tv_mul_f32 %1, %1, %5\n\tv_mul_f32 %2, %2, %6\n\tv_mul_f32 %3, %3, %7"
      : "+v"(x0), "+v"(x1), "+v"(x2), "+v"(x3), "=&v"(t0), "=&v"(t1), "=&v"(t2), "=&v"(t3)
      : "v"(rs), "v"(b[0]), "v"(b[1]), "v"(b[2]), "v"(b[3]), "v"(c1v), "s"(c2));
  return (f32x4){x0, x1, x2, x3};
}

DI u64 role_w1(int gi, const u16* __restrict__ wq, const u16* __restrict__ w1, const u16* __restrict__ b1, const u16* __restrict__ w2, char* __restrict__ ws, LAS char* lds, const int w, u64 eraw) {
  const int l = lane_id();
  const int xcd = gi & 7, idx = gi >> 3, p = (xcd >> 2) * 8 + (idx & 7), j = (xcd & 3) * 4 + (idx >> 3);
  const int g = l >> 4, wm = w >> 1, wn = w & 1;
  const unsigned lds0 = (unsigned)(uintptr_t)lds;
  constexpr int SLOT = 24576;
  const __amdgpu_buffer_rsrc_t rX = mkrsrc(ws + WS_XN, 4194304), rW = mkrsrc((const char*)w1 - 4096, D_ * F_ * 2 + 4096);
  const unsigned vlin = l * 16;
  unsigned vw[4];
#pragma unroll
  for (int i = 0; i < 4; ++i) vw[i] = wdma_off(l, w, F_ * 2) + 8 * w * (F_ * 2) + 448 * j - 64 * (j & 1) + 128 * i + 4096 - 1024 * i;
  unsigned xso = (8 * p + 2 * w) * 1024 - 131072, wso = 0u - 32 * F_ * 2;
  const unsigned mx = lds0 + w * 2048, mw = lds0 + 8192 + w * 4096;
#define PC1(SL, i) do { if constexpr ((i) == 0) dma1a_c<(SL) * SLOT, 131072>(mx, vlin, rX, xso); else if constexpr ((i) == 1) dma1b_c<1024>(vlin, rX, xso); \
    else if constexpr ((i) == 2) dma1a<(SL) * SLOT, 32 * F_ * 2>(mw, vw[0], rW, wso); else dma1b<((i) - 2) * 1024>(vw[(i) - 2], rW, wso); } while (0)
#define STG1(SL) do { PC1(SL, 0); PC1(SL, 1); PC1(SL, 2); PC1(SL, 3); PC1(SL, 4); PC1(SL, 5); } while (0)
  PC1(0, 2); PC1(0, 3); PC1(0, 4); PC1(0, 5); PC1(1, 2); PC1(1, 3); PC1(1, 4); PC1(1, 5); PC1(2, 2); PC1(2, 3); PC1(2, 4); PC1(2, 5);
  constexpr int L_R2 = 3 * SLOT, L_B1 = L_R2 + 512;
  const unsigned vpf = l < 4 ? (4 * (idx & 7) + w) * (F_ * 2) + 448 * j - 64 * (j & 1) + l * 128 + 4096 : 0x80000000u;
  const unsigned mpf = lds0 + L_B1 + 1024;
  dma4_ahead<1 * 32 * F_ * 2>(mpf, vpf, rW, wso); dma4_ahead<2 * 32 * F_ * 2>(mpf, vpf, rW, wso); dma4_ahead<3 * 32 * F_ * 2>(mpf, vpf, rW, wso);
  dma4_ahead<4 * 32 * F_ * 2>(mpf, vpf, rW, wso); dma4_ahead<5 * 32 * F_ * 2>(mpf, vpf, rW, wso);
  if (w != 0 && PFSLEEP > 0) __builtin_amdgcn_s_sleep(PFSLEEP);
  if (w == 1) dma4(mpf, (gi * 64 + l) * 128, mkrsrc(wq, D_ * D_ * 2), 0);
  if (w >= 2 && PFROWS > 0) { constexpr int NL = PFROWS * 56 / 512;
#pragma unroll
    for (int i = 0; i < (NL + 63) / 64; ++i) dma4(mpf, i * 64 + l < NL ? 256 * F_ * 2 + ((gi * 2 + (w - 2)) * NL + i * 64 + l) * 128 : 0x80000000u, mkrsrc(w1, D_ * F_ * 2), 0);
  }
  if (w >= 2) dma4(lds0 + L_B1 + (w - 2) * 256, 448 * j + ((w - 2) * 64 + l) * 4, mkrsrc(b1, F_ * 2), 0);
  asm volatile("s_waitcnt vmcnt(17)" : "+v"(eraw));
  const u64 tag = take_tag(ws, eraw, w);
  if (w == 0) await(ws, l < 16 ? FL_XN + 8 * p + (l & 7) + 128 * (l >> 3) : -1, tag);
  asm volatile("s_barrier" ::: "memory");
  if (w < 2) dma4_c(lds0 + L_R2 + w * 256, (128 * p + 64 * w + l) * 4, mkrsrc(ws + WS_R2, 8192), 0);
  PC1(0, 0); PC1(0, 1); PC1(1, 0); PC1(1, 1); PC1(2, 0); PC1(2, 1);
  dma4(mpf, 0x80000000u, rW, 0);
  f32x4 acc[4][7];
#pragma unroll
  for (int m = 0; m < 4; ++m)
#pragma unroll
    for (int n = 0; n < 7; ++n) acc[m][n] = (f32x4){0.f, 0.f, 0.f, 0.f};
  const LAS char* xrd = lds + 4 * wm * 1024 + l * 16;
  const LAS char* wrb[5];
  { const int off = 6 * wn + 2 * (j & 1), nl = 12 + wn + 2 * (j & 1);
#pragma unroll
    for (int i = 0; i < 4; ++i) wrb[i] = lds + 8192 + (g * 4 + ((i + off) >> 2)) * 1024 + wrd_off(l, (i + off) & 3);
    wrb[4] = lds + 8192 + (g * 4 + (nl >> 2)) * 1024 + wrd_off(l, nl & 3); }
  bf16x8 xa[4], wa[7], xb[4], wb[7];
#define RDX(X, m, so) X[m] = ldsfrag(xrd + (so) + (m) * 1024)
#define RDW(W, n, so) W[n] = ((n) < 6 ? ldstr(wrb[(n) & 3] + (so) + ((n) >> 2) * 1024, wrb[(n) & 3] + (so) + ((n) >> 2) * 1024 + 512) : ldstr(wrb[4] + (so), wrb[4] + (so) + 512))
#define M1(X, W, m, n) acc[m][n] = MFMA16(W[n], X[m], acc[m][n])
#define STEP1(X, W, XN, WN, RD, RS, DMA, DS) do { \
    M1(X, W, 0, 0); if (RD) RDW(WN, 0, (RS) * SLOT); SB(); M1(X, W, 1, 0); if (RD) RDX(XN, 0, (RS) * SLOT); SB(); M1(X, W, 2, 0); if (RD) RDW(WN, 1, (RS) * SLOT); SB(); M1(X, W, 3, 0); if (DMA) PC1(DS, 0); SB(); \
    M1(X, W, 0, 1); if (RD) RDX(XN, 1, (RS) * SLOT); SB(); M1(X, W, 1, 1); if (RD) RDW(WN, 2, (RS) * SLOT); SB(); M1(X, W, 2, 1); if (RD) RDX(XN, 2, (RS) * SLOT); SB(); M1(X, W, 3, 1); if (DMA) PC1(DS, 1); SB(); \
    M1(X, W, 0, 2); if (RD) RDW(WN, 3, (RS) * SLOT); SB(); M1(X, W, 1, 2); if (RD) RDX(XN, 3, (RS) * SLOT); SB(); M1(X, W, 2, 2); if (RD) RDW(WN, 4, (RS) * SLOT); SB(); M1(X, W, 3, 2); if (DMA) PC1(DS, 2); SB(); \
    M1(X, W, 0, 3); if (RD) RDW(WN, 5, (RS) * SLOT); SB(); M1(X, W, 1, 3); SB(); M1(X, W, 2, 3); if (RD) RDW(WN, 6, (RS) * SLOT); SB(); M1(X, W, 3, 3); if (DMA) PC1(DS, 3); SB(); \
    M1(X, W, 0, 4); SB(); M1(X, W, 1, 4); SB(); M1(X, W, 2, 4); SB(); M1(X, W, 3, 4); if (DMA) PC1(DS, 4); SB(); \
    M1(X, W, 0, 5); SB(); M1(X, W, 1, 5); SB(); M1(X, W, 2, 5); SB(); M1(X, W, 3, 5); if (DMA) { PC1(DS, 5); dma4_ahead<5 * 32 * F_ * 2>(mpf, vpf, rW, wso); } SB(); \
    M1(X, W, 0, 6); SB(); M1(X, W, 1, 6); SB(); M1(X, W, 2, 6); SB(); M1(X, W, 3, 6); SB(); } while (0)
  WAIT_BAR(3);
#pragma unroll
  for (int m = 0; m < 4; ++m) RDX(xa, m, 0);
#pragma unroll
  for (int n = 0; n < 7; ++n) RDW(wa, n, 0);
#define EV(SC, SN, W_, RD, DMA) do { WAIT_BAR(W_); SB(); STEP1(xa, wa, xb, wb, RD, SN, DMA, SC); } while (0)
#define OD(SC, SN, W_, RD, DMA) do { WAIT_BAR(W_); SB(); STEP1(xb, wb, xa, wa, RD, SN, DMA, SC); } while (0)
#define PR1(i) do { if constexpr ((i) == 0 || (((G1PAT >> (i)) & 1) != ((G1PAT >> ((i) - 1 < 0 ? 0 : (i) - 1)) & 1))) __builtin_amdgcn_s_setprio(((G1PAT >> (i)) & 1) ? 2 : 0); } while (0)
#define SIX() do { PR1(0); EV(0, 1, 8, true, true); PR1(1); OD(1, 2, 8, true, true); PR1(2); EV(2, 0, 8, true, true); PR1(3); OD(0, 1, 8, true, true); PR1(4); EV(1, 2, 8, true, true); PR1(5); OD(2, 0, 8, true, true); } while (0)
  for (int it = 0; it < 4; ++it) { SIX(); }
  EV(0, 1, 8, true, true); OD(1, 2, 8, true, true); EV(2, 0, 8, true, true); OD(0, 1, 8, true, true); EV(1, 2, 8, true, true);
  OD(2, 0, 8, true, false); EV(0, 1, 0, true, false); OD(1, 2, 0, false, false);
  __builtin_amdgcn_s_setprio(PRIO_GE);
  w2_weights_early(gi, w2, lds, w);
  const int le = lane_id(), ge = le >> 4;
  float r2[4];
#pragma unroll
  for (int m = 0; m < 4; ++m) r2[m] = *(const LAS float*)(lds + L_R2 + (64 * wm + 16 * m + (le & 15)) * 4);
  const __amdgpu_buffer_rsrc_t rH = mkrsrc(ws + WS_H, 112 * 128 * 1024);
  const int cch = ((ge & 1) << 1) | (ge >> 1);
  unsigned last[4][2];
  float c1v = -2.0f * 0.7978845608028654f * 1.4426950408889634f; PIN(c1v);
#pragma unroll
  for (int n = 0; n < 7; ++n) {
    const int nb = n < 6 ? 6 * wn + n : 12 + wn;
    const u32x2 braw = *(const LAS u32x2*)(lds + L_B1 + (16 * nb + 4 * ge) * 2);
    const f32x4 bb = {lo2f(braw[0]), hi2f(braw[0]), lo2f(braw[1]), hi2f(braw[1])};
#pragma unroll
    for (int m = 0; m < 4; ++m) acc[m][n] = gelu4(acc[m][n], r2[m], bb, c1v);
    if (n < 6) { if (n & 1) {
#pragma unroll
        for (int m = 0; m < 4; ++m) {
          const u32x4 ch = chunk_of(cvtpk(acc[m][n - 1][0], acc[m][n - 1][1]), cvtpk(acc[m][n - 1][2], acc[m][n - 1][3]), cvtpk(acc[m][n][0], acc[m][n][1]), cvtpk(acc[m][n][2], acc[m][n][3]));
          st_wt16(rH, ((7 * j + 3 * wn + (n >> 1)) * 128 + 8 * p + 4 * wm + m) * 1024 + cch * 256 + (le & 15) * 16, ch);
        } }
    } else {
#pragma unroll
      for (int m = 0; m < 4; ++m) { last[m][0] = cvtpk(acc[m][n][0], acc[m][n][1]); last[m][1] = cvtpk(acc[m][n][2], acc[m][n][3]); }
    }
  }
#pragma unroll
  for (int m = 0; m < 4; m += 2) {
    const u32x4 ch = chunk_of(last[m][0], last[m][1], last[m + 1][0], last[m + 1][1]);
    st_wt16(rH, ((7 * j + 6) * 128 + 8 * p + 4 * wm + m + (ge & 1)) * 1024 + (2 * wn + (ge >> 1)) * 256 + (le & 15) * 16, ch);
  }
  __builtin_amdgcn_s_setprio(PRIO_G2);
  asm volatile("s_waitcnt vmcnt(0)\n\ts_barrier" ::: "memory");
  if (w == 0 && lane_id() == 0) signal(ws, FL_H + p * 16 + j, tag);
#undef RDX
#undef RDW
#undef EV
#undef OD
  return tag;
}

DI void role_w2(int gi, const u16* __restrict__ w2, const u16* __restrict__ wo, const u16* __restrict__ b2, const u16* __restrict__ x, u16* __restrict__ out, char* __restrict__ ws, LAS char* lds, const int w, const u64 tag) {
  const int l = lane_id();
  const int xcd = gi & 7, idx = gi >> 3, kh = xcd & 1, p = (xcd >> 1) * 4 + (idx & 3), nt = idx >> 2;
  const int partner = gi ^ 1;
  const int g = l >> 4, wm = w >> 1, wn = w & 1;
  const unsigned lds0 = (unsigned)(uintptr_t)lds;
  constexpr int SLOT = 16384;
  const __amdgpu_buffer_rsrc_t rA = mkrsrc(ws + WS_H, 144 * 128 * 1024);
  __amdgpu_buffer_rsrc_t rW = mkrsrc((const char*)w2 - 1024, F_ * D_ * 2 + 1024);
  const unsigned vlin = l * 16;
  const unsigned vw0 = wdma_off(l, w, 2048) + nt * 256 + w * 8 * 2048 + 1024, vw1 = vw0 + 128 - 1024;
  if (gi == 0 && w == 0) {
    await_n<8>(ws, FL_RD + l, tag);
    if (l == 0) signal(ws, FL_E, tag);
  }
  if (w == 0) await(ws, l < 8 ? FL_H + p * 16 + 8 * kh + l : -1, tag);
  asm volatile("s_barrier" ::: "memory");
  unsigned aso = (56 * kh * 128 + 8 * p + 2 * w) * 1024 - 131072, wso = 56 * kh * 65536 + 3 * 65536;
  const unsigned ma = lds0 + w * 2048, mw = lds0 + 8192 + w * 2048;
#define PC2(SL, i) do { if constexpr ((i) == 0) dma1a_c<(SL) * SLOT, 131072>(ma, vlin, rA, aso); else if constexpr ((i) == 1) dma1b_c<1024>(vlin, rA, aso); \
    else if constexpr ((i) == 2) dma1a<(SL) * SLOT, 65536>(mw, vw0, rW, wso); else dma1b<1024>(vw1, rW, wso); } while (0)
#define STG2(SL) do { PC2(SL, 0); PC2(SL, 1); PC2(SL, 2); PC2(SL, 3); } while (0)
  PC2(0, 0); PC2(0, 1); PC2(1, 0); PC2(1, 1); PC2(2, 0); PC2(2, 1); PC2(3, 0); PC2(3, 1); STG2(4);
  f32x4 acc[4][4];
#pragma unroll
  for (int m = 0; m < 4; ++m)
#pragma unroll
    for (int n = 0; n < 4; ++n) acc[m][n] = (f32x4){0.f, 0.f, 0.f, 0.f};
  const LAS char* xrd = lds + 4 * wm * 1024 + l * 16;
  const LAS char* wrb[4];
#pragma unroll
  for (int i = 0; i < 4; ++i) wrb[i] = lds + 8192 + (g * 2 + wn) * 1024 + wrd_off(l, i);
  bf16x8 xa[4], wa[4], xb[4], wb[4];
#define RDX(X, m, so) X[m] = ldsfrag(xrd + (so) + (m) * 1024)
#define RDW(W, n, so) W[n] = ldstr(wrb[n] + (so), wrb[n] + (so) + 512)
#define M2(X, W, m, n) acc[m][n] = MFMA16(W[n], X[m], acc[m][n])
#define STEP2(X, W, XN, WN, RD, RS, DMA, DS) do { \
    M2(X, W, 0, 0); if (RD) RDW(WN, 0, (RS) * SLOT); SB(); M2(X, W, 1, 0); if (RD) RDX(XN, 0, (RS) * SLOT); SB(); M2(X, W, 2, 0); if (RD) RDX(XN, 1, (RS) * SLOT); SB(); M2(X, W, 3, 0); if (DMA) PC2(DS, 0); SB(); \
    M2(X, W, 0, 1); if (RD) RDW(WN, 1, (RS) * SLOT); SB(); M2(X, W, 1, 1); if (RD) RDX(XN, 2, (RS) * SLOT); SB(); M2(X, W, 2, 1); if (RD) RDX(XN, 3, (RS) * SLOT); SB(); M2(X, W, 3, 1); if (DMA) PC2(DS, 1); SB(); \
    M2(X, W, 0, 2); if (RD) RDW(WN, 2, (RS) * SLOT); SB(); M2(X, W, 1, 2); if (RD) RDW(WN, 3, (RS) * SLOT); SB(); M2(X, W, 2, 2); SB(); M2(X, W, 3, 2); if (DMA) PC2(DS, 2); SB(); \
    M2(X, W, 0, 3); SB(); M2(X, W, 1, 3); SB(); M2(X, W, 2, 3); SB(); M2(X, W, 3, 3); if (DMA) PC2(DS, 3); SB(); } while (0)
  WAIT_BAR(6);
#pragma unroll
  for (int i = 0; i < 4; ++i) { RDX(xa, i, 0); RDW(wa, i, 0); }
#define EV(SC, SN, W_, RD, DMA) do { WAIT_BAR(W_); SB(); STEP2(xa, wa, xb, wb, RD, SN, DMA, SC); } while (0)
#define OD(SC, SN, W_, RD, DMA) do { WAIT_BAR(W_); SB(); STEP2(xb, wb, xa, wa, RD, SN, DMA, SC); } while (0)
  u64 oflag = 0;
  for (int it = 0; it < 6; ++it) {

    EV(0, 1, 12, true, true);
    if (it == 5) {
      if (w == 0) {
        asm volatile("" : "+v"(oflag));
        if (!__all(l >= 8 || oflag == tag)) await(ws, l < 8 ? FL_O + (2 * p + (l >> 2)) * 8 + 4 * kh + (l & 3) : -1, tag);
      }
      rW = mkrsrc((const char*)wo - 1024, D_ * D_ * 2 + 1024);
      aso = ((112 + 16 * kh) * 128 + 8 * p + 2 * w) * 1024 - 131072; wso = 16 * kh * 65536 - 65536;
    }
    OD(1, 2, 12, true, true); EV(2, 3, 12, true, true); OD(3, 4, 12, true, true); EV(4, 0, 12, true, true);
    OD(0, 1, 12, true, true); EV(1, 2, 12, true, true);
    if (it == 4 && w == 0) {
      const GAS u64* fo = flagp(ws, FL_O + (2 * p + ((l >> 2) & 1)) * 8 + 4 * kh + (l & 3));
      asm volatile("global_load_dwordx2 %0, %1, off sc1" : "=&v"(oflag) : "v"(fo));
    }
    OD(2, 3, 12, true, true); EV(3, 4, 12, true, true); OD(4, 0, 12, true, true);
  }
  EV(0, 1, 12, true, true); OD(1, 2, 12, true, true); EV(2, 3, 12, true, true); OD(3, 4, 12, true, true); EV(4, 0, 12, true, true); OD(0, 1, 12, true, true); EV(1, 2, 12, true, true);
  u32x2 xres[2][4], bres[4];
  { const int le = lane_id();
    const u16* xp = x + (size_t)(128 * p + 64 * wm + 32 * kh + (le & 15)) * D_ + 128 * nt + 64 * wn + 4 * (le >> 4); const u16* xq = xp + 16 * D_; const u16* bp = b2 + 128 * nt + 64 * wn + 4 * (le >> 4);
    asm volatile("global_load_dwordx2 %0, %8, off\n\tglobal_load_dwordx2 %1, %8, off offset:32\n\tglobal_load_dwordx2 %2, %8, off offset:64\n\tglobal_load_dwordx2 %3, %8, off offset:96\n\t"
                 "global_load_dwordx2 %4, %9, off\n\tglobal_load_dwordx2 %5, %9, off offset:32\n\tglobal_load_dwordx2 %6, %9, off offset:64\n\tglobal_load_dwordx2 %7, %9, off offset:96"
                 : "=&v"(xres[0][0]), "=&v"(xres[0][1]), "=&v"(xres[0][2]), "=&v"(xres[0][3]), "=&v"(xres[1][0]), "=&v"(xres[1][1]), "=&v"(xres[1][2]), "=&v"(xres[1][3]) : "v"(xp), "v"(xq));
    asm volatile("global_load_dwordx2 %0, %4, off\n\tglobal_load_dwordx2 %1, %4, off offset:32\n\tglobal_load_dwordx2 %2, %4, off offset:64\n\tglobal_load_dwordx2 %3, %4, off offset:96"
                 : "=&v"(bres[0]), "=&v"(bres[1]), "=&v"(bres[2]), "=&v"(bres[3]) : "v"(bp)); }
  OD(2, 3, 24, true, false); EV(3, 4, 20, true, false); OD(4, 0, 16, true, false); EV(0, 1, 12, true, false); OD(1, 2, 12, false, false);
  if (kh) {
#pragma unroll
    for (int n = 0; n < 4; ++n) { f32x4 t = acc[0][n]; acc[0][n] = acc[2][n]; acc[2][n] = t; t = acc[1][n]; acc[1][n] = acc[3][n]; acc[3][n] = t; }
#pragma unroll
    for (int n = 0; n < 4; ++n) { PIN(acc[0][n]); PIN(acc[1][n]); PIN(acc[2][n]); PIN(acc[3][n]); }
  }
  const int le = lane_id(), ge = le >> 4;
  const int token0 = 128 * p + 64 * wm + 32 * kh + (le & 15);
  {
    const __amdgpu_buffer_rsrc_t rP = mkrsrc(ws + WS_PART + (size_t)gi * 32768, 16384);
#pragma unroll
    for (int m = 0; m < 2; ++m)
#pragma unroll
      for (int n2 = 0; n2 < 2; ++n2)
        st_wt16(rP, ((w * 2 + m) * 2 + n2) * 1024 + le * 16, (u32x4){cvtpk(acc[2 + m][2 * n2][0], acc[2 + m][2 * n2][1]), cvtpk(acc[2 + m][2 * n2][2], acc[2 + m][2 * n2][3]),
                                                                       cvtpk(acc[2 + m][2 * n2 + 1][0], acc[2 + m][2 * n2 + 1][1]), cvtpk(acc[2 + m][2 * n2 + 1][2], acc[2 + m][2 * n2 + 1][3])});
    asm volatile("s_waitcnt vmcnt(0)\n\ts_barrier" : "+v"(xres[0][0]), "+v"(xres[0][1]), "+v"(xres[0][2]), "+v"(xres[0][3]), "+v"(xres[1][0]), "+v"(xres[1][1]), "+v"(xres[1][2]), "+v"(xres[1][3]),
                 "+v"(bres[0]), "+v"(bres[1]), "+v"(bres[2]), "+v"(bres[3]) :: "memory");
    if (w == 0) { if (le == 0) signal(ws, FL_X + gi, tag); await(ws, FL_X + partner, tag); }
    asm volatile("s_barrier" ::: "memory");
  }
  const __amdgpu_buffer_rsrc_t rPP = mkrsrc(ws + WS_PART + (size_t)partner * 32768, 16384);
  u32x4 pv[2][2];
#pragma unroll
  for (int m = 0; m < 2; ++m)
#pragma unroll
    for (int n2 = 0; n2 < 2; ++n2) pv[m][n2] = __builtin_amdgcn_raw_buffer_load_b128(rPP, ((w * 2 + m) * 2 + n2) * 1024 + le * 16, 0, 16);
#pragma unroll
  for (int m = 0; m < 2; ++m) {
    unsigned d[4][2];
#pragma unroll
    for (int n = 0; n < 4; ++n) {
      const unsigned p0 = pv[m][n >> 1][2 * (n & 1)], p1 = pv[m][n >> 1][2 * (n & 1) + 1];
      const f32x4 a4 = acc[m][n];
      d[n][0] = cvtpk(a4[0] + lo2f(p0) + lo2f(bres[n][0]) + lo2f(xres[m][n][0]), a4[1] + hi2f(p0) + hi2f(bres[n][0]) + hi2f(xres[m][n][0]));
      d[n][1] = cvtpk(a4[2] + lo2f(p1) + lo2f(bres[n][1]) + lo2f(xres[m][n][1]), a4[3] + hi2f(p1) + hi2f(bres[n][1]) + hi2f(xres[m][n][1]));
    }
    const int cch = ((ge & 1) << 1) | (ge >> 1);
#pragma unroll
    for (int n2 = 0; n2 < 2; ++n2) {
      const u32x4 ch = chunk_of(d[2 * n2][0], d[2 * n2][1], d[2 * n2 + 1][0], d[2 * n2 + 1][1]);
      st_wt16(mkrsrc(out, S_ * D_ * 2), ((token0 + 16 * m) * D_ + 128 * nt + 64 * wn + 32 * n2 + 8 * cch) * 2, ch);
    }
  }
#undef RDX
#undef RDW
}

constexpr int LDS_BYTES = 81920;
__global__ __launch_bounds__(256, 2) void k_main(const u16* __restrict__ x, const u16* __restrict__ wq, const u16* __restrict__ wk, const u16* __restrict__ wv, const u16* __restrict__ wo,
                                                  const u16* __restrict__ w1, const u16* __restrict__ b1, const u16* __restrict__ w2, const u16* __restrict__ b2, u16* __restrict__ out, char* __restrict__ ws) {
  extern __shared__ __attribute__((aligned(16))) char lds_[];
  LAS char* lds = (LAS char*)lds_;
  const int w = __builtin_amdgcn_readfirstlane(threadIdx.x >> 6);
  u64 eraw;
  { const GAS u64* ep = flagp(ws, FL_E); asm volatile("global_load_dwordx2 %0, %1, off sc1" : "=&v"(eraw) : "v"(ep)); }
  if ((blockIdx.x >> 8) == 0) {
    __builtin_amdgcn_s_setprio(PRIO_Q);
    const u64 tag = phase0(blockIdx.x, x, wk, wv, ws, lds, w, eraw);
    role_attn(blockIdx.x, wq, w2, wo, ws, lds, w, tag);
  } else {
    __builtin_amdgcn_s_setprio(PRIO_G1);
    const u64 tag = role_w1(blockIdx.x & 255, wq, w1, b1, w2, ws, lds, w, eraw);
    role_w2(blockIdx.x & 255, w2, wo, b2, x, out, ws, lds, w, tag);
  }
}

extern "C" void kernel_launch(void* const* d_in, const int* in_sizes, int n_in, void* d_out, int out_size, void* d_ws, size_t ws_size, hipStream_t stream) {
  const u16* x = (const u16*)d_in[0]; const u16* wq = (const u16*)d_in[1]; const u16* wk = (const u16*)d_in[2];
  const u16* wv = (const u16*)d_in[3]; const u16* wo = (const u16*)d_in[4];
  const u16* w1 = (const u16*)d_in[6]; const u16* b1 = (const u16*)d_in[7]; const u16* w2 = (const u16*)d_in[8]; const u16* b2 = (const u16*)d_in[9];
  (void)hipFuncSetAttribute((const void*)k_main, hipFuncAttributeMaxDynamicSharedMemorySize, LDS_BYTES);
  k_main<<<512, 256, LDS_BYTES, stream>>>(x, wq, wk, wv, wo, w1, b1, w2, b2, (u16*)d_out, (char*)d_ws);
}
```
